```python
import torch

def _softmax(x, axis=-1):
    return torch.softmax(x, dim=axis)
def _gelu(x):
    return torch.nn.functional.gelu(x, approximate="tanh")
def _silu(x):
    return torch.nn.functional.silu(x)
def _sigmoid(x):
    return torch.sigmoid(x)
def _layernorm(x, axis=-1, eps=1e-5):
    m = x.mean(dim=axis, keepdim=True)
    v = x.var(dim=axis, keepdim=True, unbiased=False)
    return (x - m) / torch.sqrt(v + eps)
def _scatter_add(tgt, idx, src, axis=0):
    return tgt.clone().index_add_(axis, idx, src)
def _segment_sum(src, seg, n):
    out = torch.zeros((n,) + src.shape[1:], dtype=src.dtype)
    return out.index_add_(0, seg, src)
def _pad(x, pw):
    flat = tuple(v for pair in reversed(pw) for v in pair)
    return torch.nn.functional.pad(x, flat)

kernel_name = 'interleave_layers__linear_attn_block__softmax_full_mha__prompt'

def setup_inputs():

    _w = (lambda a: (lambda t: t.to(torch.bfloat16) if t.is_floating_point() else t)(torch.as_tensor(a)))
    def _A_setup():
        B, S, D = 1, 2048, 448
        return {
            "x": _w((lambda *s: torch.randn(*s, dtype=torch.bfloat16) * 0.05)(B, S, D)),
            "wqkv": _w((lambda *s: torch.randn(*s, dtype=torch.bfloat16) * 0.1)(D, 3 * D)),
            "wo": _w((lambda *s: torch.randn(*s, dtype=torch.bfloat16) * 0.05)(D, D)),
        }
    _A_in = _A_setup()

    def _B_setup():
        B, S, D = 1, 2048, 448
        return {
            "x": _w((lambda *s: torch.randn(*s, dtype=torch.bfloat16) * 0.05)(B, S, D)),
            "wqkv": _w((lambda *s: torch.randn(*s, dtype=torch.bfloat16) * 0.1)(D, 3 * D)),
            "wo": _w((lambda *s: torch.randn(*s, dtype=torch.bfloat16) * 0.05)(D, D)),
        }
    _B_in = _B_setup()

    return {
        "A_x": _A_in["x"],
        "A_wqkv": _A_in["wqkv"],
        "A_wo": _A_in["wo"],
        "B_x": _B_in["x"],
        "B_wqkv": _B_in["wqkv"],
        "B_wo": _B_in["wo"],
    }

def reference(A_x, A_wqkv, A_wo, B_x, B_wqkv, B_wo):
    def _A_ref(x, wqkv, wo):
        B, S, D = x.shape
        H = 16; Dh = D // H
        qkv = (lambda x, *d: x.permute(*d))((x @ wqkv).reshape(B, S, 3, H, Dh), 2, 0, 3, 1, 4)
        q, k, v = qkv[0], qkv[1], qkv[2]
        phi = lambda t: torch.where(t > 0, t + 1, torch.exp(t)) + 1e-6
        pq, pk = phi(q), phi(k)
        kv = torch.einsum("bhsd,bhse->bhde", pk, v)
        z = torch.einsum("bhsd->bhd", pk)
        num = torch.einsum("bhqd,bhde->bhqe", pq, kv)
        den = torch.einsum("bhqd,bhd->bhq", pq, z)[..., None] + 1e-6
        o = num / den
        return (lambda x, *d: x.permute(*d))(o, 0, 2, 1, 3).reshape(B, S, D) @ wo

    def _B_ref(x, wqkv, wo):
        B, S, D = x.shape
        H = 16; Dh = D // H
        qkv = (lambda x, *d: x.permute(*d))((x @ wqkv).reshape(B, S, 3, H, Dh), 2, 0, 3, 1, 4)
        q, k, v = qkv[0], qkv[1], qkv[2]
        scale = Dh ** -0.5
        scores = torch.einsum("bhqd,bhkd->bhqk", q, k) * scale
        p = _softmax(scores, axis=-1)
        o = torch.einsum("bhqk,bhkd->bhqd", p, v)
        return (lambda x, *d: x.permute(*d))(o, 0, 2, 1, 3).reshape(B, S, D) @ wo

    x = A_x
    x = x + _A_ref(_layernorm(x, axis=-1), A_wqkv, A_wo)
    x = x + _B_ref(_layernorm(x, axis=-1), B_wqkv, B_wo)
    x = x + _A_ref(_layernorm(x, axis=-1), A_wqkv, A_wo)
    x = x + _B_ref(_layernorm(x, axis=-1), B_wqkv, B_wo)
    return x
```

```cpp
#include <hip/hip_runtime.h>
#include <hip/hip_bf16.h>
typedef unsigned short u16;
constexpr int S=2048,D=448,H=16,DH=28;
__device__ inline float b2f(u16 b){return __uint_as_float(((unsigned)b)<<16);}
__device__ inline u16 f2b(float f){unsigned u=__float_as_uint(f);u+=0x7FFFu+((u>>16)&1u);return (u16)(u>>16);}
__device__ inline float rb(float f){return b2f(f2b(f));}
__global__ void k_ln(const u16* x,u16* y){
  int row=blockIdx.x; int l=threadIdx.x; float v[7]; float s=0;
  for(int i=0;i<7;i++){v[i]=b2f(x[row*D+l+64*i]); s+=v[i];}
  for(int m=32;m;m>>=1) s+=__shfl_xor(s,m);
  float mean=s/D; float q=0; for(int i=0;i<7;i++){float d=v[i]-mean;q+=d*d;}
  for(int m=32;m;m>>=1) q+=__shfl_xor(q,m);
  float r=rsqrtf(q/D+1e-5f);
  for(int i=0;i<7;i++) y[row*D+l+64*i]=f2b((v[i]-mean)*r);
}
__global__ void k_gemm(const u16* a,const u16* w,const u16* res,u16* c,int N){
  int col=blockIdx.x*64+threadIdx.x; int row=blockIdx.y; if(col>=N) return; float acc=0;
  for(int k=0;k<D;k++) acc+=b2f(a[row*D+k])*b2f(w[k*N+col]);
  if(res) acc=rb(acc)+b2f(res[row*N+col]);
  c[row*N+col]=f2b(acc);
}
__device__ inline float phi(float t){return (t>0?t+1.f:__expf(t))+1e-6f;}
__global__ void k_kv(const u16* qkv,float* kv){
  int h=blockIdx.x; int t=threadIdx.x; if(t>=DH*29) return; int d=t/29,e=t%29; float acc=0;
  for(int s=0;s<S;s++){ float pk=rb(phi(b2f(qkv[s*3*D+D+h*DH+d]))); float v=e<DH?b2f(qkv[s*3*D+2*D+h*DH+e]):1.f; acc+=pk*v;}
  kv[(h*DH+d)*29+e]=rb(acc);
}
__global__ void k_lino(const u16* qkv,const float* kv,u16* o){
  int row=blockIdx.x; int c=threadIdx.x; if(c>=D) return; int h=c/DH,e=c%DH; float num=0,den=0;
  for(int d=0;d<DH;d++){ float pq=rb(phi(b2f(qkv[row*3*D+h*DH+d]))); num+=pq*kv[(h*DH+d)*29+e]; den+=pq*kv[(h*DH+d)*29+28];}
  o[row*D+c]=f2b(num/(den+1e-6f));
}
__global__ void k_attn(const u16* qkv,u16* o){
  int row=blockIdx.x*64+threadIdx.x; int h=blockIdx.y; float q[DH],acc[DH]; for(int d=0;d<DH;d++){q[d]=b2f(qkv[row*3*D+h*DH+d]);acc[d]=0;}
  float m=-1e30f,l=0; const float sc=rsqrtf((float)DH);
  for(int s=0;s<S;s++){ float t=0; for(int d=0;d<DH;d++) t+=q[d]*b2f(qkv[s*3*D+D+h*DH+d]); t*=sc; float mn=fmaxf(m,t); float a=__expf(m-mn),p=__expf(t-mn); l=l*a+p; for(int d=0;d<DH;d++) acc[d]=acc[d]*a+p*b2f(qkv[s*3*D+2*D+h*DH+d]); m=mn;}
  for(int d=0;d<DH;d++) o[row*D+h*DH+d]=f2b(acc[d]/l);
}
extern "C" void kernel_launch(void* const* d_in, const int* in_sizes, int n_in, void* d_out, int out_size, void* d_ws, size_t ws_size, hipStream_t stream){
  const u16* x=(const u16*)d_in[0]; const u16* wA=(const u16*)d_in[1]; const u16* woA=(const u16*)d_in[2]; const u16* wB=(const u16*)d_in[4]; const u16* woB=(const u16*)d_in[5];
  char* ws=(char*)d_ws; u16* xn=(u16*)ws; u16* qkv=(u16*)(ws+(4<<20)); u16* o=(u16*)(ws+(16<<20)); float* kv=(float*)(ws+(20<<20)); u16* xb[2]={(u16*)(ws+(24<<20)),(u16*)(ws+(28<<20))};
  const u16* cur=x;
  for(int L=0;L<4;L++){ bool A=!(L&1);
    k_ln<<<S,64,0,stream>>>(cur,xn);
    k_gemm<<<dim3(21,S),64,0,stream>>>(xn,A?wA:wB,nullptr,qkv,3*D);
    if(A){ k_kv<<<H,832,0,stream>>>(qkv,kv); k_lino<<<S,448,0,stream>>>(qkv,kv,o);} else k_attn<<<dim3(S/64,H),64,0,stream>>>(qkv,o);
    u16* nx=L==3?(u16*)d_out:xb[L&1];
    k_gemm<<<dim3(7,S),64,0,stream>>>(o,A?woA:woB,cur,nx,D);
    cur=nx; }
}
```

```cpp
#include <hip/hip_runtime.h>
#include <hip/hip_bf16.h>

typedef unsigned short u16;
typedef unsigned long long u64;
typedef float f32x16 __attribute__((ext_vector_type(16)));
typedef float f32x4 __attribute__((ext_vector_type(4)));
typedef float f32x2 __attribute__((ext_vector_type(2)));
typedef __bf16 bf2 __attribute__((ext_vector_type(2)));
typedef __bf16 bf8 __attribute__((ext_vector_type(8)));
typedef short s16x4 __attribute__((ext_vector_type(4)));
typedef short s16x8 __attribute__((ext_vector_type(8)));
typedef unsigned u32x4 __attribute__((ext_vector_type(4)));
typedef unsigned u32x2 __attribute__((ext_vector_type(2)));
#define LAS __attribute__((address_space(3)))
#define DEVI __device__ __forceinline__
typedef LAS char* lptr;

#define ROWS_PER_XCD 4
#define DBG_TS 0

constexpr int S = 2048, D = 448;
constexpr int SC1 = 16;
constexpr unsigned WS_EPOCH = 0;
constexpr unsigned WS_FLAGS = 4096;
constexpr unsigned WS_XS = WS_FLAGS + 12 * 256 * 256;
constexpr unsigned XS_L = 16 * 4 * 14 * 256;
constexpr unsigned WS_XF = WS_XS + 3 * XS_L;
constexpr unsigned XF_L = 16 * 4 * 28 * 1024;
constexpr unsigned WS_OF = WS_XF + 3 * XF_L;
constexpr unsigned OF_L = 16 * 4 * 32 * 1024;
constexpr unsigned WS_KVP = WS_OF + 4 * OF_L;
constexpr unsigned KVP_L = 16 * 4 * 16 * 1024;
constexpr unsigned WS_KV = WS_KVP + 2 * KVP_L;
constexpr unsigned KV_L = 16 * 4 * 16 * 4096;
constexpr unsigned WS_TS = WS_KV + 2 * KV_L;
constexpr unsigned WS_END = WS_TS + 256 * 4 * 64 * 8;
constexpr int L_WO = 0, L_WQ = 28672, L_SCR = 114688, L_TOTAL = 163840;
constexpr int L_ST = L_SCR + 32768;
constexpr int L_KVF = L_SCR + 16384;
constexpr int L_CS = L_SCR + 24576;
constexpr unsigned SPIN_CAP = 1u << 16;

DEVI f32x16 mfma(bf8 a, bf8 b, f32x16 c) { return __builtin_amdgcn_mfma_f32_32x32x16_bf16(a, b, c, 0, 0, 0); }
DEVI unsigned pk(float lo, float hi) { f32x2 f = {lo, hi}; return __builtin_bit_cast(unsigned, __builtin_convertvector(f, bf2)); }
DEVI float bflo(unsigned u) { return __uint_as_float(u << 16); }
DEVI float bfhi(unsigned u) { return __uint_as_float(u & 0xffff0000u); }
DEVI bf2 asbf2(unsigned v) { return __builtin_bit_cast(bf2, v); }
DEVI bf8 asbf(u32x4 v) { return __builtin_bit_cast(bf8, v); }
DEVI bf8 trfrag(lptr p1, lptr p2) {
  s16x4 a = __builtin_amdgcn_ds_read_tr16_b64_v4i16((LAS s16x4*)p1), b = __builtin_amdgcn_ds_read_tr16_b64_v4i16((LAS s16x4*)p2);
  s16x8 r = __builtin_shufflevector(a, b, 0, 1, 2, 3, 4, 5, 6, 7);
  return __builtin_bit_cast(bf8, r);
}
DEVI float xhalf_sum(float v) { auto r = __builtin_amdgcn_permlane32_swap(__float_as_uint(v), __float_as_uint(v), false, false); return __uint_as_float(r[0]) + __uint_as_float(r[1]); }
DEVI float xhalf_max(float v) { auto r = __builtin_amdgcn_permlane32_swap(__float_as_uint(v), __float_as_uint(v), false, false); return fmaxf(__uint_as_float(r[0]), __uint_as_float(r[1])); }
DEVI float upper_half(float v) { auto r = __builtin_amdgcn_permlane32_swap(__float_as_uint(v), __float_as_uint(v), false, false); return __uint_as_float(r[1]); }
DEVI void swap_pairs(u32x4& d) {
  auto a = __builtin_amdgcn_permlane32_swap(d[0], d[2], false, false); auto b = __builtin_amdgcn_permlane32_swap(d[1], d[3], false, false);
  d = (u32x4){a[0], b[0], a[1], b[1]};
}
DEVI u32x4 mk_srd(const void* p, unsigned bytes) { u64 a = (u64)p; return (u32x4){(unsigned)a, (unsigned)(a >> 32) & 0xffffu, bytes, 0x20000u}; }
DEVI void dma16(u32x4 srd, unsigned voff, unsigned soff, unsigned ldsaddr) {
  unsigned keep;
  asm volatile("s_mov_b32 %0, m0\n\ts_mov_b32 m0, %4\n\ts_nop 2\n\tbuffer_load_dwordx4 %1, %2, %3 offen lds\n\ts_mov_b32 m0, %0"
               : "=&s"(keep) : "v"(voff), "s"(srd), "s"(soff), "s"(ldsaddr) : "memory");
}
DEVI unsigned opq_v(unsigned x) { asm volatile("" : "+v"(x)); return x; }
DEVI unsigned opq_s(unsigned x) { asm volatile("" : "+s"(x)); return x; }
#define LD128(vo, sb, k) __builtin_amdgcn_raw_buffer_load_b128(rs, (vo) + ((k) & 3) * 1024, (sb) + ((k) >> 2) * 4096, SC1)
#define ST128(v, vo, sb, k) __builtin_amdgcn_raw_buffer_store_b128(v, rs, (vo) + (k) * 1024, (sb), SC1)
#define BAR() asm volatile("s_waitcnt lgkmcnt(0)\n\ts_barrier" ::: "memory")
#define DRAIN_BAR() asm volatile("s_waitcnt vmcnt(0) lgkmcnt(0)\n\ts_barrier" ::: "memory")
#define LGKM0() asm volatile("s_waitcnt lgkmcnt(0)" ::: "memory")

__global__ void __launch_bounds__(256, 1)
fused_kernel(const u16* __restrict__ x0, const u16* __restrict__ wA, const u16* __restrict__ woA, const u16* __restrict__ wB, const u16* __restrict__ woB,
             u16* __restrict__ out, char* ws) {
  __shared__ __attribute__((aligned(16))) char lds_raw[L_TOTAL];
  const lptr lds = (lptr)lds_raw;
  const unsigned lds0 = (unsigned)(size_t)lds;
  const int tid = threadIdx.x;
  unsigned lane = tid & 63, r32, hh, l16, trq, trc;
#define REFRESH() do { lane = opq_v(lane); r32 = lane & 31; hh = lane >> 5; l16 = lane * 16; trq = (lane & 15) >> 2; trc = ((lane >> 4) & 1) * 32 + (lane & 3) * 8; } while (0)
  REFRESH();
  const int w = __builtin_amdgcn_readfirstlane(tid >> 6);
  int m, h;
  { constexpr int R = ROWS_PER_XCD, C = 32 / R, NXC = 16 / C; const int xc = blockIdx.x & 7, j = blockIdx.x >> 3; m = (xc / NXC) * R + j / C; h = (xc % NXC) * C + j % C; }
  const __amdgpu_buffer_rsrc_t rs = __builtin_amdgcn_make_buffer_rsrc(ws, 0, WS_END, 0x00020000);
  const u32x4 ws_srd = mk_srd(ws, WS_END);
  const int row0 = m * 128 + w * 32;
#define row (row0 + r32)
#define TS() do {} while (0)
  TS();
  auto dma_wq = [&](const u16* W, int i0, int i1, int step) {
    const u32x4 srd = mk_srd(W, D * 3 * D * 2);
    const unsigned wq_voff = (lane >> 2) * 2688 + (lane & 3) * 16 + h * 56;
    for (int i = i0; i < i1; i += step) { const int p = i / 28, rb = i - p * 28; dma16(srd, wq_voff, rb * 16 * 2688 + p * 896, lds0 + L_WQ + i * 1024); }
  };
  auto dma_wo = [&](const u16* W, int i0, int step) {
    const u32x4 srd = mk_srd(W, D * D * 2);
    const unsigned wo_voff = (lane >> 2) * 896 + (lane & 3) * 16 + h * 64;
    if (h < 14) for (int i = i0; i < 28; i += step) dma16(srd, wo_voff, i * 16 * 896, lds0 + L_WO + i * 1024);
  };
  u64 tag = 0;
  auto publish = [&](int hop, int grp, int mem) {
    DRAIN_BAR();
    REFRESH();
    if (w == 0 && lane == 0) __builtin_amdgcn_raw_buffer_store_b64((u32x2){(unsigned)tag, (unsigned)(tag >> 32)}, rs, WS_FLAGS + ((hop * 16 + grp) * 16 + mem) * 256, 0, SC1);
  };
  auto waitf = [&](int hop, int grp, int n) {
    if (w == 0) {
      const unsigned off = WS_FLAGS + ((hop * 16 + grp) * 16 + (lane & 15)) * 256;
      for (unsigned spins = 0; spins < SPIN_CAP; ++spins) {
        bool ok = true;
        if (lane < (unsigned)n) { u32x2 f = __builtin_amdgcn_raw_buffer_load_b64(rs, off, 0, SC1); ok = f[0] == (unsigned)tag && f[1] == (unsigned)(tag >> 32); }
        if (__all(ok)) break;
        __builtin_amdgcn_s_sleep(2);
        asm volatile("" ::: "memory");
      }
    }
    BAR();
    REFRESH();
  };

  const lptr stw = lds + L_ST + w * 1024;
  float xr[16];
  float csA[3], csB[3];
  dma_wq(wA, 28 + w, 84, 4);
  if (h < 14) {
    const u16* xp = x0 + (size_t)row * D + h * 32 + hh * 4;
#pragma unroll
    for (int g = 0; g < 4; ++g) { u32x2 v = *(const u32x2*)(xp + g * 8); xr[4 * g] = bflo(v[0]); xr[4 * g + 1] = bfhi(v[0]); xr[4 * g + 2] = bflo(v[1]); xr[4 * g + 3] = bfhi(v[1]); }
  } else {
#pragma unroll
    for (int r = 0; r < 16; ++r) xr[r] = 0.f;
  }

  auto colsums = [&](float (&cs)[3]) {
    float a[3] = {0.f, 0.f, 0.f};
    const bf2 one = {(__bf16)1.f, (__bf16)1.f};
    const lptr b1 = lds + L_WQ + (trq + 8 * hh) * 64 + trc + w * 7 * 1024;
#pragma unroll
    for (int p = 0; p < 3; ++p)
#pragma unroll
      for (int u = 0; u < 7; ++u) { u32x4 f = __builtin_bit_cast(u32x4, trfrag(b1 + p * 28672 + u * 1024, b1 + p * 28672 + u * 1024 + 256));
#pragma unroll
        for (int i = 0; i < 4; ++i) a[p] = __builtin_amdgcn_fdot2_f32_bf16(asbf2(f[i]), one, a[p], false); }
#pragma unroll
    for (int p = 0; p < 3; ++p) { a[p] = xhalf_sum(a[p]); if (hh == 0) *(LAS float*)(lds + L_CS + (w * 96 + p * 32 + r32) * 4) = a[p]; }
    BAR();
#pragma unroll
    for (int p = 0; p < 3; ++p) { float t = 0.f;
#pragma unroll
      for (int q = 0; q < 4; ++q) t += *(LAS float*)(lds + L_CS + (q * 96 + p * 32 + r32) * 4);
      cs[p] = t; }
    BAR();
  };
  auto bcast16 = [&](float v, int slot, float (&o)[16]) {
    if (hh == 0) *(LAS float*)(stw + slot * 128 + r32 * 4) = v;
    LGKM0();
#pragma unroll
    for (int g = 0; g < 4; ++g) { f32x4 t = *(LAS f32x4*)(stw + slot * 128 + (8 * g + 4 * hh) * 4); o[4 * g] = t[0]; o[4 * g + 1] = t[1]; o[4 * g + 2] = t[2]; o[4 * g + 3] = t[3]; }
  };
  auto load_x = [&](int xl, bf8 (&xf)[28], float& sum, float& sq) {
    const unsigned sb = opq_s(WS_XS + xl * XS_L + (m * 4 + w) * 14 * 256);
    u32x2 st[7];
#pragma unroll
    for (int i = 0; i < 7; ++i) st[i] = __builtin_amdgcn_raw_buffer_load_b64(rs, hh * 7 * 256 + r32 * 8 + i * 256, sb, SC1);
    const unsigned xb = opq_s(WS_XF + xl * XF_L + (m * 4 + w) * 28 * 1024);
#pragma unroll
    for (int u = 0; u < 28; ++u) xf[u] = asbf(LD128(l16, xb, u));
    float a = 0.f, b = 0.f;
#pragma unroll
    for (int i = 0; i < 7; ++i) { a += __uint_as_float(st[i][0]); b += __uint_as_float(st[i][1]); }
    sum = xhalf_sum(a); sq = xhalf_sum(b);
  };
  auto wo_phase = [&](int layer, bool last) {
    if (h < 14) {
      bf8 xf[32];
      const unsigned ob = opq_s(WS_OF + layer * OF_L + (m * 4 + w) * 32 * 1024);
#pragma unroll
      for (int u = 0; u < 32; ++u) xf[u] = asbf(LD128(l16, ob, u));
      const lptr b1 = lds + L_WO + (trq + 4 * hh) * 64 + trc;
      f32x16 acc = {};
#pragma unroll
      for (int u = 0; u < 32; ++u) { const int ro = ((u >> 1) * 28 + (u & 1) * 16) * 64; acc = mfma(trfrag(b1 + ro, b1 + ro + 512), xf[u], acc); }
      u32x4 d0, d1;
#pragma unroll
      for (int i = 0; i < 4; ++i) { d0[i] = pk(xr[2 * i] + acc[2 * i], xr[2 * i + 1] + acc[2 * i + 1]); d1[i] = pk(xr[8 + 2 * i] + acc[8 + 2 * i], xr[9 + 2 * i] + acc[9 + 2 * i]); }
      if (!last) {
        const unsigned xb = WS_XF + layer * XF_L + ((m * 4 + w) * 28 + h * 2) * 1024;
        ST128(d0, l16, xb, 0); ST128(d1, l16, xb, 1);
        float a = 0.f, b = 0.f;
#pragma unroll
        for (int i = 0; i < 4; ++i) { xr[2 * i] = bflo(d0[i]); xr[2 * i + 1] = bfhi(d0[i]); xr[8 + 2 * i] = bflo(d1[i]); xr[9 + 2 * i] = bfhi(d1[i]); }
#pragma unroll
        for (int r = 0; r < 16; ++r) { a += xr[r]; b = fmaf(xr[r], xr[r], b); }
        a = xhalf_sum(a); b = xhalf_sum(b);
        if (hh == 0) __builtin_amdgcn_raw_buffer_store_b64((u32x2){__float_as_uint(a), __float_as_uint(b)}, rs, r32 * 8, WS_XS + layer * XS_L + ((m * 4 + w) * 14 + h) * 256, SC1);
      } else {
        swap_pairs(d0); swap_pairs(d1);
        u16* op = out + (size_t)row * D + h * 32 + hh * 8;
        *(u32x4*)op = d0; *(u32x4*)(op + 16) = d1;
      }
    }
  };
  auto store_o = [&](int layer, const f32x16& o, float inv) {
    u32x4 d0, d1;
#pragma unroll
    for (int i = 0; i < 4; ++i) { d0[i] = pk(o[2 * i] * inv, o[2 * i + 1] * inv); d1[i] = pk(o[8 + 2 * i] * inv, o[9 + 2 * i] * inv); }
    if (hh) { d1[2] = 0u; d1[3] = 0u; }
    const unsigned ob = WS_OF + layer * OF_L + ((m * 4 + w) * 32 + h * 2) * 1024;
    ST128(d0, l16, ob, 0); ST128(d1, l16, ob, 1);
  };
  constexpr float LOG2E = 1.4426950408889634f;
  auto phi = [&](float t) { float e = __builtin_amdgcn_fmed3f(__builtin_amdgcn_exp2f(t * LOG2E), 0.f, 1.f); return fmaxf(t + 1.f, e) + 1e-6f; };

#pragma unroll 1
  for (int rep = 0; rep < 2; ++rep) {
    const int hop0 = rep * 6;
    REFRESH();
    {
      float sum, sq; int ro1, ro2;
      bf8 xf[28];
      if (rep == 0) {
        {
          const u16* xp = x0 + (size_t)row * D + hh * 8;
#pragma unroll
          for (int u = 0; u < 28; ++u) xf[u] = *(const bf8*)(xp + u * 16);
        }
        dma_wq(wA, w, 28, 4);
        dma_wo(woA, w, 4);
        const u32x2 ep = __builtin_amdgcn_raw_buffer_load_b64(rs, WS_EPOCH, 0, SC1);
        const bf2 one = {(__bf16)1.f, (__bf16)1.f};
        float a = 0.f, b = 0.f;
#pragma unroll
        for (int u = 0; u < 28; ++u) { u32x4 f = __builtin_bit_cast(u32x4, xf[u]);
#pragma unroll
          for (int i = 0; i < 4; ++i) { bf2 t = asbf2(f[i]); a = __builtin_amdgcn_fdot2_f32_bf16(t, one, a, false); b = __builtin_amdgcn_fdot2_f32_bf16(t, t, b, false); } }
        sum = xhalf_sum(a); sq = xhalf_sum(b);
        ro1 = 8 * hh; ro2 = 8 * hh + 4;
        tag = (((u64)__builtin_amdgcn_readfirstlane(ep[1]) << 32) | (unsigned)__builtin_amdgcn_readfirstlane(ep[0])) + 1ull;
        DRAIN_BAR();
        colsums(csA);
      } else {
        waitf(5, m, 14);
        load_x(1, xf, sum, sq);
        ro1 = 4 * hh; ro2 = 4 * hh + 8;
      }
      TS();
      const float mean = sum * (1.f / D), rstd = __builtin_amdgcn_rsqf(fmaxf(sq * (1.f / D) - mean * mean, 0.f) + 1e-5f), nmr = -mean * rstd;
      const lptr b1 = lds + L_WQ + (trq + ro1) * 64 + trc, b2 = lds + L_WQ + (trq + ro2) * 64 + trc;
      f32x16 ak = {}, av = {};
#pragma unroll
      for (int u = 0; u < 28; ++u) {
        ak = mfma(xf[u], trfrag(b1 + 28672 + u * 1024, b2 + 28672 + u * 1024), ak);
        av = mfma(xf[u], trfrag(b1 + 57344 + u * 1024, b2 + 57344 + u * 1024), av);
      }
      float ra[16], rb[16];
      bcast16(rstd, 0, ra); bcast16(nmr, 1, rb);
      u32x4 pk0, pk1, v0, v1;
      {
        float kk[16], vv[16];
#pragma unroll
        for (int r = 0; r < 16; ++r) { kk[r] = phi(fmaf(ak[r], ra[r], csA[1] * rb[r])); vv[r] = fmaf(av[r], ra[r], csA[2] * rb[r]); }
#pragma unroll
        for (int i = 0; i < 4; ++i) { pk0[i] = pk(kk[2 * i], kk[2 * i + 1]); pk1[i] = pk(kk[8 + 2 * i], kk[9 + 2 * i]); v0[i] = pk(vv[2 * i], vv[2 * i + 1]); v1[i] = pk(vv[8 + 2 * i], vv[9 + 2 * i]); }
        if (r32 >= 28) { const unsigned c = r32 == 28 ? 0x3f803f80u : 0u; pk0 = (u32x4){0u, 0u, 0u, 0u}; pk1 = pk0; v0 = (u32x4){c, c, c, c}; v1 = v0; }
      }
      f32x16 kvp = {};
      kvp = mfma(asbf(pk0), asbf(v0), kvp); kvp = mfma(asbf(pk1), asbf(v1), kvp);
#pragma unroll
      for (int c = 0; c < 4; ++c) *(LAS f32x4*)(lds + L_SCR + w * 4096 + c * 1024 + lane * 16) = (f32x4){kvp[4 * c], kvp[4 * c + 1], kvp[4 * c + 2], kvp[4 * c + 3]};
      BAR();
      {
        f32x4 t = *(LAS f32x4*)(lds + L_SCR + w * 1024 + lane * 16);
#pragma unroll
        for (int q = 1; q < 4; ++q) t += *(LAS f32x4*)(lds + L_SCR + q * 4096 + w * 1024 + lane * 16);
        ST128(__builtin_bit_cast(u32x4, t), l16, WS_KVP + rep * KVP_L + ((h * 4 + w) * 16 + m) * 1024, 0);
      }
      TS();
      publish(hop0 + 0, h, m);
      u32x4 pq0, pq1;
      {
        f32x16 aq = {};
#pragma unroll
        for (int u = 0; u < 28; ++u) aq = mfma(trfrag(b1 + u * 1024, b2 + u * 1024), xf[u], aq);
        float cq[16]; bcast16(csA[0], 2, cq);
        float qq[16];
#pragma unroll
        for (int r = 0; r < 16; ++r) qq[r] = phi(fmaf(aq[r], rstd, cq[r] * nmr));
#pragma unroll
        for (int i = 0; i < 4; ++i) { pq0[i] = pk(qq[2 * i], qq[2 * i + 1]); pq1[i] = pk(qq[8 + 2 * i], qq[9 + 2 * i]); }
        if (hh) { pq1[2] = 0u; pq1[3] = 0u; }
      }
      TS();
      waitf(hop0 + 0, h, 16);
      TS();
      {
        const unsigned kb = opq_s(WS_KVP + rep * KVP_L + (h * 4 + w) * 16 * 1024);
        f32x4 p[16];
#pragma unroll
        for (int i = 0; i < 16; ++i) p[i] = __builtin_bit_cast(f32x4, LD128(l16, kb, i));
        f32x4 t = ((p[0] + p[1]) + (p[2] + p[3])) + ((p[4] + p[5]) + (p[6] + p[7])) + (((p[8] + p[9]) + (p[10] + p[11])) + ((p[12] + p[13]) + (p[14] + p[15])));
        *(LAS u32x2*)(lds + L_KVF + (w >> 1) * 1024 + lane * 16 + (w & 1) * 8) = (u32x2){pk(t[0], t[1]), pk(t[2], t[3])};
      }
      BAR();
      {
        const bf8 k0 = *(LAS bf8*)(lds + L_KVF + lane * 16), k1 = *(LAS bf8*)(lds + L_KVF + 1024 + lane * 16);
        f32x16 o = {};
        o = mfma(k0, asbf(pq0), o); o = mfma(k1, asbf(pq1), o);
        const float den = upper_half(o[12]) + 1e-6f;
        store_o(rep * 2, o, __builtin_amdgcn_rcpf(den));
      }
      TS();
      publish(hop0 + 1, m, h);
      dma_wq(wB, w, 84, 4);
      waitf(hop0 + 1, m, 16);
      TS();
      wo_phase(rep * 2, false);
      TS();
      publish(hop0 + 2, m, h);
      dma_wo(woB, w, 4);
      if (rep == 0) colsums(csB);
    }
    {
      float sum, sq;
      waitf(hop0 + 2, m, 14);
      TS();
      bf8 xf[28];
      load_x(rep * 2, xf, sum, sq);
      const float mean = sum * (1.f / D), rstd = __builtin_amdgcn_rsqf(fmaxf(sq * (1.f / D) - mean * mean, 0.f) + 1e-5f), nmr = -mean * rstd;
      const lptr b1 = lds + L_WQ + (trq + 4 * hh) * 64 + trc, b2 = b1 + 512;
      f32x16 ak = {}, av = {};
#pragma unroll
      for (int u = 0; u < 28; ++u) {
        ak = mfma(trfrag(b1 + 28672 + u * 1024, b2 + 28672 + u * 1024), xf[u], ak);
        av = mfma(xf[u], trfrag(b1 + 57344 + u * 1024, b2 + 57344 + u * 1024), av);
      }
      u32x4 k0, k1, v0, v1;
      {
        float ra[16], rb[16], ck[16];
        bcast16(rstd, 0, ra); bcast16(nmr, 1, rb); bcast16(csB[1], 2, ck);
        float kk[16], vv[16];
#pragma unroll
        for (int r = 0; r < 16; ++r) { kk[r] = fmaf(ak[r], rstd, ck[r] * nmr); vv[r] = fmaf(av[r], ra[r], csB[2] * rb[r]); }
#pragma unroll
        for (int i = 0; i < 4; ++i) { k0[i] = pk(kk[2 * i], kk[2 * i + 1]); k1[i] = pk(kk[8 + 2 * i], kk[9 + 2 * i]); v0[i] = pk(vv[2 * i], vv[2 * i + 1]); v1[i] = pk(vv[8 + 2 * i], vv[9 + 2 * i]); }
        if (hh) { k1[2] = 0x3f803f80u; k1[3] = 0x00003f80u; }
        if (r32 >= 28) { const unsigned c = r32 == 28 ? 0x3f803f80u : 0u; v0 = (u32x4){c, c, c, c}; v1 = v0; }
        swap_pairs(v0); swap_pairs(v1);
      }
      const unsigned kvs = WS_KV + rep * KV_L + (h * 4 + w) * 65536;
      ST128(k0, l16, kvs + m * 4096, 0); ST128(k1, l16, kvs + m * 4096, 1); ST128(v0, l16, kvs + m * 4096, 2); ST128(v1, l16, kvs + m * 4096, 3);
      TS();
      publish(hop0 + 3, h, m);
      u32x4 q0, q1;
      {
        f32x16 aq = {};
#pragma unroll
        for (int u = 0; u < 28; ++u) aq = mfma(trfrag(b1 + u * 1024, b2 + u * 1024), xf[u], aq);
        float cq[16]; bcast16(csB[0], 3, cq);
        constexpr float QS = 0.18898223650461362f * LOG2E;
        const float a = rstd * QS, b = nmr * QS;
#pragma unroll
        for (int i = 0; i < 4; ++i) { q0[i] = pk(fmaf(aq[2 * i], a, cq[2 * i] * b), fmaf(aq[2 * i + 1], a, cq[2 * i + 1] * b)); q1[i] = pk(fmaf(aq[8 + 2 * i], a, cq[8 + 2 * i] * b), fmaf(aq[9 + 2 * i], a, cq[9 + 2 * i] * b)); }
        if (hh) { q1[2] = 0u; q1[3] = 0u; }
      }
      auto set_shift = [&](float mx) {
        const float n0 = -mx; const unsigned p0 = pk(n0, 0.f) & 0xffffu; const float n1 = n0 - bflo(p0); const unsigned p1 = pk(n1, 0.f) & 0xffffu; const float n2 = n1 - bflo(p1);
        u32x4 t = q1; if (hh) { t[2] = p0 | (p1 << 16); t[3] = pk(n2, 0.f) & 0xffffu; }
        *(LAS u32x4*)(lds + L_SCR + w * 2048 + lane * 16) = q0; *(LAS u32x4*)(lds + L_SCR + w * 2048 + 1024 + lane * 16) = t;
      };
      {
        f32x16 s = {};
        s = mfma(asbf(k0), asbf(q0), s); s = mfma(asbf(k1), asbf(q1), s);
        float mx = s[0];
#pragma unroll
        for (int r = 1; r < 16; ++r) mx = fmaxf(mx, s[r]);
        set_shift(xhalf_max(mx));
      }
      TS();
      waitf(hop0 + 3, h, 16);
      TS();
#pragma unroll 1
      for (int pass = 0; pass < 2; ++pass) {
        f32x16 o;
        unsigned so = ((m + 1) & 15) * 4096;
        const unsigned qb = lds0 + L_SCR + lane * 16;
        asm volatile(
      "s_nop 4\n"
      "buffer_load_dwordx4 a[0:3], %[vo], %[srd], %[so] offen offset:0 sc1\n"
      "buffer_load_dwordx4 a[4:7], %[vo], %[srd], %[so] offen offset:1024 sc1\n"
      "buffer_load_dwordx4 a[8:11], %[vo], %[srd], %[so] offen offset:2048 sc1\n"
      "buffer_load_dwordx4 a[12:15], %[vo], %[srd], %[so] offen offset:3072 sc1\n"
      "s_add_u32 %[so], %[so], 0x1000\n"
      "s_and_b32 %[so], %[so], 0xffff\n"
      "buffer_load_dwordx4 a[16:19], %[vo], %[srd], %[so] offen offset:0 sc1\n"
      "buffer_load_dwordx4 a[20:23], %[vo], %[srd], %[so] offen offset:1024 sc1\n"
      "buffer_load_dwordx4 a[24:27], %[vo], %[srd], %[so] offen offset:2048 sc1\n"
      "buffer_load_dwordx4 a[28:31], %[vo], %[srd], %[so] offen offset:3072 sc1\n"
      "s_add_u32 %[so], %[so], 0x1000\n"
      "s_and_b32 %[so], %[so], 0xffff\n"
      "buffer_load_dwordx4 a[32:35], %[vo], %[srd], %[so] offen offset:0 sc1\n"
      "buffer_load_dwordx4 a[36:39], %[vo], %[srd], %[so] offen offset:1024 sc1\n"
      "buffer_load_dwordx4 a[40:43], %[vo], %[srd], %[so] offen offset:2048 sc1\n"
      "buffer_load_dwordx4 a[44:47], %[vo], %[srd], %[so] offen offset:3072 sc1\n"
      "s_add_u32 %[so], %[so], 0x1000\n"
      "s_and_b32 %[so], %[so], 0xffff\n"
      "buffer_load_dwordx4 a[48:51], %[vo], %[srd], %[so] offen offset:0 sc1\n"
      "buffer_load_dwordx4 a[52:55], %[vo], %[srd], %[so] offen offset:1024 sc1\n"
      "buffer_load_dwordx4 a[56:59], %[vo], %[srd], %[so] offen offset:2048 sc1\n"
      "buffer_load_dwordx4 a[60:63], %[vo], %[srd], %[so] offen offset:3072 sc1\n"
      "s_add_u32 %[so], %[so], 0x1000\n"
      "s_and_b32 %[so], %[so], 0xffff\n"
      "buffer_load_dwordx4 a[64:67], %[vo], %[srd], %[so] offen offset:0 sc1\n"
      "buffer_load_dwordx4 a[68:71], %[vo], %[srd], %[so] offen offset:1024 sc1\n"
      "buffer_load_dwordx4 a[72:75], %[vo], %[srd], %[so] offen offset:2048 sc1\n"
      "buffer_load_dwordx4 a[76:79], %[vo], %[srd], %[so] offen offset:3072 sc1\n"
      "ds_read_b128 v[112:115], %[qa0] offset:0\n"
      "ds_read_b128 v[116:119], %[qa0] offset:1024\n"
      "ds_read_b128 v[120:123], %[qa1] offset:0\n"
      "ds_read_b128 v[124:127], %[qa1] offset:1024\n"
      "ds_read_b128 v[128:131], %[qa2] offset:0\n"
      "ds_read_b128 v[132:135], %[qa2] offset:1024\n"
      "ds_read_b128 v[136:139], %[qa3] offset:0\n"
      "ds_read_b128 v[140:143], %[qa3] offset:1024\n"
      "v_mov_b32 v144, 0\n"
      "v_mov_b32 v145, 0\n"
      "v_mov_b32 v146, 0\n"
      "v_mov_b32 v147, 0\n"
      "v_mov_b32 v148, 0\n"
      "v_mov_b32 v149, 0\n"
      "v_mov_b32 v150, 0\n"
      "v_mov_b32 v151, 0\n"
      "v_mov_b32 v152, 0\n"
      "v_mov_b32 v153, 0\n"
      "v_mov_b32 v154, 0\n"
      "v_mov_b32 v155, 0\n"
      "v_mov_b32 v156, 0\n"
      "v_mov_b32 v157, 0\n"
      "v_mov_b32 v158, 0\n"
      "v_mov_b32 v159, 0\n"
      "v_mov_b32 v160, 0\n"
      "v_mov_b32 v161, 0\n"
      "v_mov_b32 v162, 0\n"
      "v_mov_b32 v163, 0\n"
      "v_mov_b32 v164, 0\n"
      "v_mov_b32 v165, 0\n"
      "v_mov_b32 v166, 0\n"
      "v_mov_b32 v167, 0\n"
      "v_mov_b32 v168, 0\n"
      "v_mov_b32 v169, 0\n"
      "v_mov_b32 v170, 0\n"
      "v_mov_b32 v171, 0\n"
      "v_mov_b32 v172, 0\n"
      "v_mov_b32 v173, 0\n"
      "v_mov_b32 v174, 0\n"
      "v_mov_b32 v175, 0\n"
      "v_mov_b32 v176, 0\n"
      "v_mov_b32 v177, 0\n"
      "v_mov_b32 v178, 0\n"
      "v_mov_b32 v179, 0\n"
      "v_mov_b32 v180, 0\n"
      "v_mov_b32 v181, 0\n"
      "v_mov_b32 v182, 0\n"
      "v_mov_b32 v183, 0\n"
      "v_mov_b32 v184, 0\n"
      "v_mov_b32 v185, 0\n"
      "v_mov_b32 v186, 0\n"
      "v_mov_b32 v187, 0\n"
      "v_mov_b32 v188, 0\n"
      "v_mov_b32 v189, 0\n"
      "v_mov_b32 v190, 0\n"
      "v_mov_b32 v191, 0\n"
      "s_waitcnt lgkmcnt(0)\n"
      "s_barrier\n"
      "v_mfma_f32_32x32x16_bf16 v[208:223], a[128:131], v[112:115], 0\n"
      "v_mfma_f32_32x32x16_bf16 v[208:223], a[132:135], v[116:119], v[208:223]\n"
      "v_mov_b32 v192, 0\n"
      "v_mov_b32 v193, 0\n"
      "v_mov_b32 v194, 0\n"
      "v_mov_b32 v195, 0\n"
      "v_mov_b32 v196, 0\n"
      "v_mov_b32 v197, 0\n"
      "v_mov_b32 v198, 0\n"
      "v_mov_b32 v199, 0\n"
      "v_mov_b32 v200, 0\n"
      "v_mov_b32 v201, 0\n"
      "v_mov_b32 v202, 0\n"
      "v_mov_b32 v203, 0\n"
      "v_mov_b32 v204, 0\n"
      "v_mov_b32 v205, 0\n"
      "v_mov_b32 v206, 0\n"
      "v_mov_b32 v207, 0\n"
      "v_mfma_f32_32x32x16_bf16 v[224:239], a[128:131], v[120:123], 0\n"
      "s_add_u32 %[so], %[so], 0x1000\n"
      "s_and_b32 %[so], %[so], 0xffff\n"
      "buffer_load_dwordx4 a[80:83], %[vo], %[srd], %[so] offen offset:0 sc1\n"
      "v_exp_f32 v208, v208\n"
      "v_exp_f32 v209, v209\n"
      "v_exp_f32 v210, v210\n"
      "v_exp_f32 v211, v211\n"
      "v_mfma_f32_32x32x16_bf16 v[224:239], a[132:135], v[124:127], v[224:239]\n"
      "v_exp_f32 v212, v212\n"
      "v_exp_f32 v213, v213\n"
      "v_exp_f32 v214, v214\n"
      "v_exp_f32 v215, v215\n"
      "v_exp_f32 v216, v216\n"
      "v_exp_f32 v217, v217\n"
      "v_exp_f32 v218, v218\n"
      "v_exp_f32 v219, v219\n"
      "v_smfmac_f32_32x32x32_bf16 v[144:159], a[136:139], v[208:215], %[idx]\n"
      "v_exp_f32 v220, v220\n"
      "v_exp_f32 v221, v221\n"
      "v_exp_f32 v222, v222\n"
      "v_exp_f32 v223, v223\n"
      "v_mfma_f32_32x32x16_bf16 v[240:255], a[128:131], v[128:131], 0\n"
      "buffer_load_dwordx4 a[84:87], %[vo], %[srd], %[so] offen offset:1024 sc1\n"
      "v_exp_f32 v224, v224\n"
      "v_exp_f32 v225, v225\n"
      "v_exp_f32 v226, v226\n"
      "v_exp_f32 v227, v227\n"
      "v_mfma_f32_32x32x16_bf16 v[240:255], a[132:135], v[132:135], v[240:255]\n"
      "v_exp_f32 v228, v228\n"
      "v_exp_f32 v229, v229\n"
      "v_exp_f32 v230, v230\n"
      "v_exp_f32 v231, v231\n"
      "v_smfmac_f32_32x32x32_bf16 v[144:159], a[140:143], v[216:223], %[idx]\n"
      "v_exp_f32 v232, v232\n"
      "v_exp_f32 v233, v233\n"
      "v_exp_f32 v234, v234\n"
      "v_exp_f32 v235, v235\n"
      "v_smfmac_f32_32x32x32_bf16 v[160:175], a[136:139], v[224:231], %[idx]\n"
      "v_exp_f32 v236, v236\n"
      "v_exp_f32 v237, v237\n"
      "v_exp_f32 v238, v238\n"
      "v_exp_f32 v239, v239\n"
      "v_mfma_f32_32x32x16_bf16 v[208:223], a[128:131], v[136:139], 0\n"
      "buffer_load_dwordx4 a[88:91], %[vo], %[srd], %[so] offen offset:2048 sc1\n"
      "v_exp_f32 v240, v240\n"
      "v_exp_f32 v241, v241\n"
      "v_exp_f32 v242, v242\n"
      "v_exp_f32 v243, v243\n"
      "v_mfma_f32_32x32x16_bf16 v[208:223], a[132:135], v[140:143], v[208:223]\n"
      "v_exp_f32 v244, v244\n"
      "v_exp_f32 v245, v245\n"
      "v_exp_f32 v246, v246\n"
      "v_exp_f32 v247, v247\n"
      "v_smfmac_f32_32x32x32_bf16 v[160:175], a[140:143], v[232:239], %[idx]\n"
      "v_exp_f32 v248, v248\n"
      "v_exp_f32 v249, v249\n"
      "v_exp_f32 v250, v250\n"
      "v_exp_f32 v251, v251\n"
      "v_smfmac_f32_32x32x32_bf16 v[176:191], a[136:139], v[240:247], %[idx]\n"
      "v_exp_f32 v252, v252\n"
      "v_exp_f32 v253, v253\n"
      "v_exp_f32 v254, v254\n"
      "v_exp_f32 v255, v255\n"
      "s_waitcnt vmcnt(19)\n"
      "v_mfma_f32_32x32x16_bf16 v[224:239], a[0:3], v[112:115], 0\n"
      "buffer_load_dwordx4 a[92:95], %[vo], %[srd], %[so] offen offset:3072 sc1\n"
      "v_exp_f32 v208, v208\n"
      "v_exp_f32 v209, v209\n"
      "v_exp_f32 v210, v210\n"
      "v_exp_f32 v211, v211\n"
      "v_mfma_f32_32x32x16_bf16 v[224:239], a[4:7], v[116:119], v[224:239]\n"
      "v_exp_f32 v212, v212\n"
      "v_exp_f32 v213, v213\n"
      "v_exp_f32 v214, v214\n"
      "v_exp_f32 v215, v215\n"
      "v_smfmac_f32_32x32x32_bf16 v[176:191], a[140:143], v[248:255], %[idx]\n"
      "v_exp_f32 v216, v216\n"
      "v_exp_f32 v217, v217\n"
      "v_exp_f32 v218, v218\n"
      "v_exp_f32 v219, v219\n"
      "v_smfmac_f32_32x32x32_bf16 v[192:207], a[136:139], v[208:215], %[idx]\n"
      "v_exp_f32 v220, v220\n"
      "v_exp_f32 v221, v221\n"
      "v_exp_f32 v222, v222\n"
      "v_exp_f32 v223, v223\n"
      "v_mfma_f32_32x32x16_bf16 v[240:255], a[0:3], v[120:123], 0\n"
      "s_add_u32 %[so], %[so], 0x1000\n"
      "s_and_b32 %[so], %[so], 0xffff\n"
      "buffer_load_dwordx4 a[96:99], %[vo], %[srd], %[so] offen offset:0 sc1\n"
      "v_exp_f32 v208, v208\n"
      "v_exp_f32 v209, v209\n"
      "v_exp_f32 v210, v210\n"
      "v_exp_f32 v211, v211\n"
      "v_mfma_f32_32x32x16_bf16 v[224:239], a[132:135], v[124:127], v[224:239]\n"
      "v_exp_f32 v212, v212\n"
      "v_exp_f32 v213, v213\n"
      "v_exp_f32 v214, v214\n"
      "v_exp_f32 v215, v215\n"
      "v_exp_f32 v216, v216\n"
      "v_exp_f32 v217, v217\n"
      "v_exp_f32 v218, v218\n"
      "v_exp_f32 v219, v219\n"
      "v_smfmac_f32_32x32x32_bf16 v[144:159], a[136:139], v[208:215], %[idx]\n"
      "v_exp_f32 v220, v220\n"
      "v_exp_f32 v221, v221\n"
      "v_exp_f32 v222, v222\n"
      "v_exp_f32 v223, v223\n"
      "v_mfma_f32_32x32x16_bf16 v[208:223], a[0:3], v[128:131], 0\n"
      "buffer_load_dwordx4 a[100:103], %[vo], %[srd], %[so] offen offset:1024 sc1\n"
      "v_exp_f32 v224, v224\n"
      "v_exp_f32 v225, v225\n"
      "v_exp_f32 v226, v226\n"
      "v_exp_f32 v227, v227\n"
      "v_mfma_f32_32x32x16_bf16 v[240:255], a[132:135], v[132:135], v[240:255]\n"
      "v_exp_f32 v228, v228\n"
      "v_exp_f32 v229, v229\n"
      "v_exp_f32 v230, v230\n"
      "v_exp_f32 v231, v231\n"
      "v_smfmac_f32_32x32x32_bf16 v[144:159], a[140:143], v[216:223], %[idx]\n"
      "v_exp_f32 v232, v232\n"
      "v_exp_f32 v233, v233\n"
      "v_exp_f32 v234, v234\n"
      "v_exp_f32 v235, v235\n"
      "v_smfmac_f32_32x32x32_bf16 v[160:175], a[136:139], v[224:231], %[idx]\n"
      "v_exp_f32 v236, v236\n"
      "v_exp_f32 v237, v237\n"
      "v_exp_f32 v238, v238\n"
      "v_exp_f32 v239, v239\n"
      "v_mfma_f32_32x32x16_bf16 v[224:239], a[0:3], v[136:139], 0\n"
      "buffer_load_dwordx4 a[104:107], %[vo], %[srd], %[so] offen offset:2048 sc1\n"
      "v_exp_f32 v240, v240\n"
      "v_exp_f32 v241, v241\n"
      "v_exp_f32 v242, v242\n"
      "v_exp_f32 v243, v243\n"
      "v_mfma_f32_32x32x16_bf16 v[208:223], a[132:135], v[140:143], v[208:223]\n"
      "v_exp_f32 v244, v244\n"
      "v_exp_f32 v245, v245\n"
      "v_exp_f32 v246, v246\n"
      "v_exp_f32 v247, v247\n"
      "v_smfmac_f32_32x32x32_bf16 v[160:175], a[140:143], v[232:239], %[idx]\n"
      "v_exp_f32 v248, v248\n"
      "v_exp_f32 v249, v249\n"
      "v_exp_f32 v250, v250\n"
      "v_exp_f32 v251, v251\n"
      "v_smfmac_f32_32x32x32_bf16 v[176:191], a[136:139], v[240:247], %[idx]\n"
      "v_exp_f32 v252, v252\n"
      "v_exp_f32 v253, v253\n"
      "v_exp_f32 v254, v254\n"
      "v_exp_f32 v255, v255\n"
      "s_waitcnt vmcnt(19)\n"
      "v_mfma_f32_32x32x16_bf16 v[240:255], a[16:19], v[112:115], 0\n"
      "buffer_load_dwordx4 a[108:111], %[vo], %[srd], %[so] offen offset:3072 sc1\n"
      "v_exp_f32 v208, v208\n"
      "v_exp_f32 v209, v209\n"
      "v_exp_f32 v210, v210\n"
      "v_exp_f32 v211, v211\n"
      "v_mfma_f32_32x32x16_bf16 v[224:239], a[4:7], v[116:119], v[224:239]\n"
      "v_exp_f32 v212, v212\n"
      "v_exp_f32 v213, v213\n"
      "v_exp_f32 v214, v214\n"
      "v_exp_f32 v215, v215\n"
      "v_smfmac_f32_32x32x32_bf16 v[176:191], a[140:143], v[248:255], %[idx]\n"
      "v_exp_f32 v216, v216\n"
      "v_exp_f32 v217, v217\n"
      "v_exp_f32 v218, v218\n"
      "v_exp_f32 v219, v219\n"
      "v_smfmac_f32_32x32x32_bf16 v[192:207], a[136:139], v[208:215], %[idx]\n"
      "v_exp_f32 v220, v220\n"
      "v_exp_f32 v221, v221\n"
      "v_exp_f32 v222, v222\n"
      "v_exp_f32 v223, v223\n"
      "v_mfma_f32_32x32x16_bf16 v[208:223], a[16:19], v[120:123], 0\n"
      "s_add_u32 %[so], %[so], 0x1000\n"
      "s_and_b32 %[so], %[so], 0xffff\n"
      "buffer_load_dwordx4 a[112:115], %[vo], %[srd], %[so] offen offset:0 sc1\n"
      "v_exp_f32 v208, v208\n"
      "v_exp_f32 v209, v209\n"
      "v_exp_f32 v210, v210\n"
      "v_exp_f32 v211, v211\n"
      "v_mfma_f32_32x32x16_bf16 v[224:239], a[132:135], v[124:127], v[224:239]\n"
      "v_exp_f32 v212, v212\n"
      "v_exp_f32 v213, v213\n"
      "v_exp_f32 v214, v214\n"
      "v_exp_f32 v215, v215\n"
      "v_exp_f32 v216, v216\n"
      "v_exp_f32 v217, v217\n"
      "v_exp_f32 v218, v218\n"
      "v_exp_f32 v219, v219\n"
      "v_smfmac_f32_32x32x32_bf16 v[144:159], a[136:139], v[208:215], %[idx]\n"
      "v_exp_f32 v220, v220\n"
      "v_exp_f32 v221, v221\n"
      "v_exp_f32 v222, v222\n"
      "v_exp_f32 v223, v223\n"
      "v_mfma_f32_32x32x16_bf16 v[224:239], a[16:19], v[128:131], 0\n"
      "buffer_load_dwordx4 a[116:119], %[vo], %[srd], %[so] offen offset:1024 sc1\n"
      "v_exp_f32 v224, v224\n"
      "v_exp_f32 v225, v225\n"
      "v_exp_f32 v226, v226\n"
      "v_exp_f32 v227, v227\n"
      "v_mfma_f32_32x32x16_bf16 v[240:255], a[132:135], v[132:135], v[240:255]\n"
      "v_exp_f32 v228, v228\n"
      "v_exp_f32 v229, v229\n"
      "v_exp_f32 v230, v230\n"
      "v_exp_f32 v231, v231\n"
      "v_smfmac_f32_32x32x32_bf16 v[144:159], a[140:143], v[216:223], %[idx]\n"
      "v_exp_f32 v232, v232\n"
      "v_exp_f32 v233, v233\n"
      "v_exp_f32 v234, v234\n"
      "v_exp_f32 v235, v235\n"
      "v_smfmac_f32_32x32x32_bf16 v[160:175], a[136:139], v[224:231], %[idx]\n"
      "v_exp_f32 v236, v236\n"
      "v_exp_f32 v237, v237\n"
      "v_exp_f32 v238, v238\n"
      "v_exp_f32 v239, v239\n"
      "v_mfma_f32_32x32x16_bf16 v[240:255], a[16:19], v[136:139], 0\n"
      "buffer_load_dwordx4 a[120:123], %[vo], %[srd], %[so] offen offset:2048 sc1\n"
      "v_exp_f32 v240, v240\n"
      "v_exp_f32 v241, v241\n"
      "v_exp_f32 v242, v242\n"
      "v_exp_f32 v243, v243\n"
      "v_mfma_f32_32x32x16_bf16 v[208:223], a[132:135], v[140:143], v[208:223]\n"
      "v_exp_f32 v244, v244\n"
      "v_exp_f32 v245, v245\n"
      "v_exp_f32 v246, v246\n"
      "v_exp_f32 v247, v247\n"
      "v_smfmac_f32_32x32x32_bf16 v[160:175], a[140:143], v[232:239], %[idx]\n"
      "v_exp_f32 v248, v248\n"
      "v_exp_f32 v249, v249\n"
      "v_exp_f32 v250, v250\n"
      "v_exp_f32 v251, v251\n"
      "v_smfmac_f32_32x32x32_bf16 v[176:191], a[136:139], v[240:247], %[idx]\n"
      "v_exp_f32 v252, v252\n"
      "v_exp_f32 v253, v253\n"
      "v_exp_f32 v254, v254\n"
      "v_exp_f32 v255, v255\n"
      "s_waitcnt vmcnt(19)\n"
      "v_mfma_f32_32x32x16_bf16 v[208:223], a[32:35], v[112:115], 0\n"
      "buffer_load_dwordx4 a[124:127], %[vo], %[srd], %[so] offen offset:3072 sc1\n"
      "v_exp_f32 v208, v208\n"
      "v_exp_f32 v209, v209\n"
      "v_exp_f32 v210, v210\n"
      "v_exp_f32 v211, v211\n"
      "v_mfma_f32_32x32x16_bf16 v[224:239], a[4:7], v[116:119], v[224:239]\n"
      "v_exp_f32 v212, v212\n"
      "v_exp_f32 v213, v213\n"
      "v_exp_f32 v214, v214\n"
      "v_exp_f32 v215, v215\n"
      "v_smfmac_f32_32x32x32_bf16 v[176:191], a[140:143], v[248:255], %[idx]\n"
      "v_exp_f32 v216, v216\n"
      "v_exp_f32 v217, v217\n"
      "v_exp_f32 v218, v218\n"
      "v_exp_f32 v219, v219\n"
      "v_smfmac_f32_32x32x32_bf16 v[192:207], a[136:139], v[208:215], %[idx]\n"
      "v_exp_f32 v220, v220\n"
      "v_exp_f32 v221, v221\n"
      "v_exp_f32 v222, v222\n"
      "v_exp_f32 v223, v223\n"
      "v_mfma_f32_32x32x16_bf16 v[224:239], a[32:35], v[120:123], 0\n"
      "s_add_u32 %[so], %[so], 0x1000\n"
      "s_and_b32 %[so], %[so], 0xffff\n"
      "buffer_load_dwordx4 a[0:3], %[vo], %[srd], %[so] offen offset:0 sc1\n"
      "v_exp_f32 v208, v208\n"
      "v_exp_f32 v209, v209\n"
      "v_exp_f32 v210, v210\n"
      "v_exp_f32 v211, v211\n"
      "v_mfma_f32_32x32x16_bf16 v[224:239], a[132:135], v[124:127], v[224:239]\n"
      "v_exp_f32 v212, v212\n"
      "v_exp_f32 v213, v213\n"
      "v_exp_f32 v214, v214\n"
      "v_exp_f32 v215, v215\n"
      "v_exp_f32 v216, v216\n"
      "v_exp_f32 v217, v217\n"
      "v_exp_f32 v218, v218\n"
      "v_exp_f32 v219, v219\n"
      "v_smfmac_f32_32x32x32_bf16 v[144:159], a[136:139], v[208:215], %[idx]\n"
      "v_exp_f32 v220, v220\n"
      "v_exp_f32 v221, v221\n"
      "v_exp_f32 v222, v222\n"
      "v_exp_f32 v223, v223\n"
      "v_mfma_f32_32x32x16_bf16 v[240:255], a[32:35], v[128:131], 0\n"
      "buffer_load_dwordx4 a[4:7], %[vo], %[srd], %[so] offen offset:1024 sc1\n"
      "v_exp_f32 v224, v224\n"
      "v_exp_f32 v225, v225\n"
      "v_exp_f32 v226, v226\n"
      "v_exp_f32 v227, v227\n"
      "v_mfma_f32_32x32x16_bf16 v[240:255], a[132:135], v[132:135], v[240:255]\n"
      "v_exp_f32 v228, v228\n"
      "v_exp_f32 v229, v229\n"
      "v_exp_f32 v230, v230\n"
      "v_exp_f32 v231, v231\n"
      "v_smfmac_f32_32x32x32_bf16 v[144:159], a[140:143], v[216:223], %[idx]\n"
      "v_exp_f32 v232, v232\n"
      "v_exp_f32 v233, v233\n"
      "v_exp_f32 v234, v234\n"
      "v_exp_f32 v235, v235\n"
      "v_smfmac_f32_32x32x32_bf16 v[160:175], a[136:139], v[224:231], %[idx]\n"
      "v_exp_f32 v236, v236\n"
      "v_exp_f32 v237, v237\n"
      "v_exp_f32 v238, v238\n"
      "v_exp_f32 v239, v239\n"
      "v_mfma_f32_32x32x16_bf16 v[208:223], a[32:35], v[136:139], 0\n"
      "buffer_load_dwordx4 a[8:11], %[vo], %[srd], %[so] offen offset:2048 sc1\n"
      "v_exp_f32 v240, v240\n"
      "v_exp_f32 v241, v241\n"
      "v_exp_f32 v242, v242\n"
      "v_exp_f32 v243, v243\n"
      "v_mfma_f32_32x32x16_bf16 v[208:223], a[132:135], v[140:143], v[208:223]\n"
      "v_exp_f32 v244, v244\n"
      "v_exp_f32 v245, v245\n"
      "v_exp_f32 v246, v246\n"
      "v_exp_f32 v247, v247\n"
      "v_smfmac_f32_32x32x32_bf16 v[160:175], a[140:143], v[232:239], %[idx]\n"
      "v_exp_f32 v248, v248\n"
      "v_exp_f32 v249, v249\n"
      "v_exp_f32 v250, v250\n"
      "v_exp_f32 v251, v251\n"
      "v_smfmac_f32_32x32x32_bf16 v[176:191], a[136:139], v[240:247], %[idx]\n"
      "v_exp_f32 v252, v252\n"
      "v_exp_f32 v253, v253\n"
      "v_exp_f32 v254, v254\n"
      "v_exp_f32 v255, v255\n"
      "s_waitcnt vmcnt(19)\n"
      "v_mfma_f32_32x32x16_bf16 v[224:239], a[48:51], v[112:115], 0\n"
      "buffer_load_dwordx4 a[12:15], %[vo], %[srd], %[so] offen offset:3072 sc1\n"
      "v_exp_f32 v208, v208\n"
      "v_exp_f32 v209, v209\n"
      "v_exp_f32 v210, v210\n"
      "v_exp_f32 v211, v211\n"
      "v_mfma_f32_32x32x16_bf16 v[224:239], a[4:7], v[116:119], v[224:239]\n"
      "v_exp_f32 v212, v212\n"
      "v_exp_f32 v213, v213\n"
      "v_exp_f32 v214, v214\n"
      "v_exp_f32 v215, v215\n"
      "v_smfmac_f32_32x32x32_bf16 v[176:191], a[140:143], v[248:255], %[idx]\n"
      "v_exp_f32 v216, v216\n"
      "v_exp_f32 v217, v217\n"
      "v_exp_f32 v218, v218\n"
      "v_exp_f32 v219, v219\n"
      "v_smfmac_f32_32x32x32_bf16 v[192:207], a[136:139], v[208:215], %[idx]\n"
      "v_exp_f32 v220, v220\n"
      "v_exp_f32 v221, v221\n"
      "v_exp_f32 v222, v222\n"
      "v_exp_f32 v223, v223\n"
      "v_mfma_f32_32x32x16_bf16 v[240:255], a[48:51], v[120:123], 0\n"
      "s_add_u32 %[so], %[so], 0x1000\n"
      "s_and_b32 %[so], %[so], 0xffff\n"
      "buffer_load_dwordx4 a[16:19], %[vo], %[srd], %[so] offen offset:0 sc1\n"
      "v_exp_f32 v208, v208\n"
      "v_exp_f32 v209, v209\n"
      "v_exp_f32 v210, v210\n"
      "v_exp_f32 v211, v211\n"
      "v_mfma_f32_32x32x16_bf16 v[224:239], a[132:135], v[124:127], v[224:239]\n"
      "v_exp_f32 v212, v212\n"
      "v_exp_f32 v213, v213\n"
      "v_exp_f32 v214, v214\n"
      "v_exp_f32 v215, v215\n"
      "v_exp_f32 v216, v216\n"
      "v_exp_f32 v217, v217\n"
      "v_exp_f32 v218, v218\n"
      "v_exp_f32 v219, v219\n"
      "v_smfmac_f32_32x32x32_bf16 v[144:159], a[136:139], v[208:215], %[idx]\n"
      "v_exp_f32 v220, v220\n"
      "v_exp_f32 v221, v221\n"
      "v_exp_f32 v222, v222\n"
      "v_exp_f32 v223, v223\n"
      "v_mfma_f32_32x32x16_bf16 v[208:223], a[48:51], v[128:131], 0\n"
      "buffer_load_dwordx4 a[20:23], %[vo], %[srd], %[so] offen offset:1024 sc1\n"
      "v_exp_f32 v224, v224\n"
      "v_exp_f32 v225, v225\n"
      "v_exp_f32 v226, v226\n"
      "v_exp_f32 v227, v227\n"
      "v_mfma_f32_32x32x16_bf16 v[240:255], a[132:135], v[132:135], v[240:255]\n"
      "v_exp_f32 v228, v228\n"
      "v_exp_f32 v229, v229\n"
      "v_exp_f32 v230, v230\n"
      "v_exp_f32 v231, v231\n"
      "v_smfmac_f32_32x32x32_bf16 v[144:159], a[140:143], v[216:223], %[idx]\n"
      "v_exp_f32 v232, v232\n"
      "v_exp_f32 v233, v233\n"
      "v_exp_f32 v234, v234\n"
      "v_exp_f32 v235, v235\n"
      "v_smfmac_f32_32x32x32_bf16 v[160:175], a[136:139], v[224:231], %[idx]\n"
      "v_exp_f32 v236, v236\n"
      "v_exp_f32 v237, v237\n"
      "v_exp_f32 v238, v238\n"
      "v_exp_f32 v239, v239\n"
      "v_mfma_f32_32x32x16_bf16 v[224:239], a[48:51], v[136:139], 0\n"
      "buffer_load_dwordx4 a[24:27], %[vo], %[srd], %[so] offen offset:2048 sc1\n"
      "v_exp_f32 v240, v240\n"
      "v_exp_f32 v241, v241\n"
      "v_exp_f32 v242, v242\n"
      "v_exp_f32 v243, v243\n"
      "v_mfma_f32_32x32x16_bf16 v[208:223], a[132:135], v[140:143], v[208:223]\n"
      "v_exp_f32 v244, v244\n"
      "v_exp_f32 v245, v245\n"
      "v_exp_f32 v246, v246\n"
      "v_exp_f32 v247, v247\n"
      "v_smfmac_f32_32x32x32_bf16 v[160:175], a[140:143], v[232:239], %[idx]\n"
      "v_exp_f32 v248, v248\n"
      "v_exp_f32 v249, v249\n"
      "v_exp_f32 v250, v250\n"
      "v_exp_f32 v251, v251\n"
      "v_smfmac_f32_32x32x32_bf16 v[176:191], a[136:139], v[240:247], %[idx]\n"
      "v_exp_f32 v252, v252\n"
      "v_exp_f32 v253, v253\n"
      "v_exp_f32 v254, v254\n"
      "v_exp_f32 v255, v255\n"
      "s_waitcnt vmcnt(19)\n"
      "v_mfma_f32_32x32x16_bf16 v[240:255], a[64:67], v[112:115], 0\n"
      "buffer_load_dwordx4 a[28:31], %[vo], %[srd], %[so] offen offset:3072 sc1\n"
      "v_exp_f32 v208, v208\n"
      "v_exp_f32 v209, v209\n"
      "v_exp_f32 v210, v210\n"
      "v_exp_f32 v211, v211\n"
      "v_mfma_f32_32x32x16_bf16 v[224:239], a[4:7], v[116:119], v[224:239]\n"
      "v_exp_f32 v212, v212\n"
      "v_exp_f32 v213, v213\n"
      "v_exp_f32 v214, v214\n"
      "v_exp_f32 v215, v215\n"
      "v_smfmac_f32_32x32x32_bf16 v[176:191], a[140:143], v[248:255], %[idx]\n"
      "v_exp_f32 v216, v216\n"
      "v_exp_f32 v217, v217\n"
      "v_exp_f32 v218, v218\n"
      "v_exp_f32 v219, v219\n"
      "v_smfmac_f32_32x32x32_bf16 v[192:207], a[136:139], v[208:215], %[idx]\n"
      "v_exp_f32 v220, v220\n"
      "v_exp_f32 v221, v221\n"
      "v_exp_f32 v222, v222\n"
      "v_exp_f32 v223, v223\n"
      "v_mfma_f32_32x32x16_bf16 v[208:223], a[64:67], v[120:123], 0\n"
      "s_add_u32 %[so], %[so], 0x1000\n"
      "s_and_b32 %[so], %[so], 0xffff\n"
      "buffer_load_dwordx4 a[32:35], %[vo], %[srd], %[so] offen offset:0 sc1\n"
      "v_exp_f32 v208, v208\n"
      "v_exp_f32 v209, v209\n"
      "v_exp_f32 v210, v210\n"
      "v_exp_f32 v211, v211\n"
      "v_mfma_f32_32x32x16_bf16 v[224:239], a[132:135], v[124:127], v[224:239]\n"
      "v_exp_f32 v212, v212\n"
      "v_exp_f32 v213, v213\n"
      "v_exp_f32 v214, v214\n"
      "v_exp_f32 v215, v215\n"
      "v_exp_f32 v216, v216\n"
      "v_exp_f32 v217, v217\n"
      "v_exp_f32 v218, v218\n"
      "v_exp_f32 v219, v219\n"
      "v_smfmac_f32_32x32x32_bf16 v[144:159], a[136:139], v[208:215], %[idx]\n"
      "v_exp_f32 v220, v220\n"
      "v_exp_f32 v221, v221\n"
      "v_exp_f32 v222, v222\n"
      "v_exp_f32 v223, v223\n"
      "v_mfma_f32_32x32x16_bf16 v[224:239], a[64:67], v[128:131], 0\n"
      "buffer_load_dwordx4 a[36:39], %[vo], %[srd], %[so] offen offset:1024 sc1\n"
      "v_exp_f32 v224, v224\n"
      "v_exp_f32 v225, v225\n"
      "v_exp_f32 v226, v226\n"
      "v_exp_f32 v227, v227\n"
      "v_mfma_f32_32x32x16_bf16 v[240:255], a[132:135], v[132:135], v[240:255]\n"
      "v_exp_f32 v228, v228\n"
      "v_exp_f32 v229, v229\n"
      "v_exp_f32 v230, v230\n"
      "v_exp_f32 v231, v231\n"
      "v_smfmac_f32_32x32x32_bf16 v[144:159], a[140:143], v[216:223], %[idx]\n"
      "v_exp_f32 v232, v232\n"
      "v_exp_f32 v233, v233\n"
      "v_exp_f32 v234, v234\n"
      "v_exp_f32 v235, v235\n"
      "v_smfmac_f32_32x32x32_bf16 v[160:175], a[136:139], v[224:231], %[idx]\n"
      "v_exp_f32 v236, v236\n"
      "v_exp_f32 v237, v237\n"
      "v_exp_f32 v238, v238\n"
      "v_exp_f32 v239, v239\n"
      "v_mfma_f32_32x32x16_bf16 v[240:255], a[64:67], v[136:139], 0\n"
      "buffer_load_dwordx4 a[40:43], %[vo], %[srd], %[so] offen offset:2048 sc1\n"
      "v_exp_f32 v240, v240\n"
      "v_exp_f32 v241, v241\n"
      "v_exp_f32 v242, v242\n"
      "v_exp_f32 v243, v243\n"
      "v_mfma_f32_32x32x16_bf16 v[208:223], a[132:135], v[140:143], v[208:223]\n"
      "v_exp_f32 v244, v244\n"
      "v_exp_f32 v245, v245\n"
      "v_exp_f32 v246, v246\n"
      "v_exp_f32 v247, v247\n"
      "v_smfmac_f32_32x32x32_bf16 v[160:175], a[140:143], v[232:239], %[idx]\n"
      "v_exp_f32 v248, v248\n"
      "v_exp_f32 v249, v249\n"
      "v_exp_f32 v250, v250\n"
      "v_exp_f32 v251, v251\n"
      "v_smfmac_f32_32x32x32_bf16 v[176:191], a[136:139], v[240:247], %[idx]\n"
      "v_exp_f32 v252, v252\n"
      "v_exp_f32 v253, v253\n"
      "v_exp_f32 v254, v254\n"
      "v_exp_f32 v255, v255\n"
      "s_waitcnt vmcnt(19)\n"
      "v_mfma_f32_32x32x16_bf16 v[208:223], a[80:83], v[112:115], 0\n"
      "buffer_load_dwordx4 a[44:47], %[vo], %[srd], %[so] offen offset:3072 sc1\n"
      "v_exp_f32 v208, v208\n"
      "v_exp_f32 v209, v209\n"
      "v_exp_f32 v210, v210\n"
      "v_exp_f32 v211, v211\n"
      "v_mfma_f32_32x32x16_bf16 v[224:239], a[4:7], v[116:119], v[224:239]\n"
      "v_exp_f32 v212, v212\n"
      "v_exp_f32 v213, v213\n"
      "v_exp_f32 v214, v214\n"
      "v_exp_f32 v215, v215\n"
      "v_smfmac_f32_32x32x32_bf16 v[176:191], a[140:143], v[248:255], %[idx]\n"
      "v_exp_f32 v216, v216\n"
      "v_exp_f32 v217, v217\n"
      "v_exp_f32 v218, v218\n"
      "v_exp_f32 v219, v219\n"
      "v_smfmac_f32_32x32x32_bf16 v[192:207], a[136:139], v[208:215], %[idx]\n"
      "v_exp_f32 v220, v220\n"
      "v_exp_f32 v221, v221\n"
      "v_exp_f32 v222, v222\n"
      "v_exp_f32 v223, v223\n"
      "v_mfma_f32_32x32x16_bf16 v[224:239], a[80:83], v[120:123], 0\n"
      "s_add_u32 %[so], %[so], 0x1000\n"
      "s_and_b32 %[so], %[so], 0xffff\n"
      "buffer_load_dwordx4 a[48:51], %[vo], %[srd], %[so] offen offset:0 sc1\n"
      "v_exp_f32 v208, v208\n"
      "v_exp_f32 v209, v209\n"
      "v_exp_f32 v210, v210\n"
      "v_exp_f32 v211, v211\n"
      "v_mfma_f32_32x32x16_bf16 v[224:239], a[132:135], v[124:127], v[224:239]\n"
      "v_exp_f32 v212, v212\n"
      "v_exp_f32 v213, v213\n"
      "v_exp_f32 v214, v214\n"
      "v_exp_f32 v215, v215\n"
      "v_exp_f32 v216, v216\n"
      "v_exp_f32 v217, v217\n"
      "v_exp_f32 v218, v218\n"
      "v_exp_f32 v219, v219\n"
      "v_smfmac_f32_32x32x32_bf16 v[144:159], a[136:139], v[208:215], %[idx]\n"
      "v_exp_f32 v220, v220\n"
      "v_exp_f32 v221, v221\n"
      "v_exp_f32 v222, v222\n"
      "v_exp_f32 v223, v223\n"
      "v_mfma_f32_32x32x16_bf16 v[240:255], a[80:83], v[128:131], 0\n"
      "buffer_load_dwordx4 a[52:55], %[vo], %[srd], %[so] offen offset:1024 sc1\n"
      "v_exp_f32 v224, v224\n"
      "v_exp_f32 v225, v225\n"
      "v_exp_f32 v226, v226\n"
      "v_exp_f32 v227, v227\n"
      "v_mfma_f32_32x32x16_bf16 v[240:255], a[132:135], v[132:135], v[240:255]\n"
      "v_exp_f32 v228, v228\n"
      "v_exp_f32 v229, v229\n"
      "v_exp_f32 v230, v230\n"
      "v_exp_f32 v231, v231\n"
      "v_smfmac_f32_32x32x32_bf16 v[144:159], a[140:143], v[216:223], %[idx]\n"
      "v_exp_f32 v232, v232\n"
      "v_exp_f32 v233, v233\n"
      "v_exp_f32 v234, v234\n"
      "v_exp_f32 v235, v235\n"
      "v_smfmac_f32_32x32x32_bf16 v[160:175], a[136:139], v[224:231], %[idx]\n"
      "v_exp_f32 v236, v236\n"
      "v_exp_f32 v237, v237\n"
      "v_exp_f32 v238, v238\n"
      "v_exp_f32 v239, v239\n"
      "v_mfma_f32_32x32x16_bf16 v[208:223], a[80:83], v[136:139], 0\n"
      "buffer_load_dwordx4 a[56:59], %[vo], %[srd], %[so] offen offset:2048 sc1\n"
      "v_exp_f32 v240, v240\n"
      "v_exp_f32 v241, v241\n"
      "v_exp_f32 v242, v242\n"
      "v_exp_f32 v243, v243\n"
      "v_mfma_f32_32x32x16_bf16 v[208:223], a[132:135], v[140:143], v[208:223]\n"
      "v_exp_f32 v244, v244\n"
      "v_exp_f32 v245, v245\n"
      "v_exp_f32 v246, v246\n"
      "v_exp_f32 v247, v247\n"
      "v_smfmac_f32_32x32x32_bf16 v[160:175], a[140:143], v[232:239], %[idx]\n"
      "v_exp_f32 v248, v248\n"
      "v_exp_f32 v249, v249\n"
      "v_exp_f32 v250, v250\n"
      "v_exp_f32 v251, v251\n"
      "v_smfmac_f32_32x32x32_bf16 v[176:191], a[136:139], v[240:247], %[idx]\n"
      "v_exp_f32 v252, v252\n"
      "v_exp_f32 v253, v253\n"
      "v_exp_f32 v254, v254\n"
      "v_exp_f32 v255, v255\n"
      "s_waitcnt vmcnt(19)\n"
      "v_mfma_f32_32x32x16_bf16 v[224:239], a[96:99], v[112:115], 0\n"
      "buffer_load_dwordx4 a[60:63], %[vo], %[srd], %[so] offen offset:3072 sc1\n"
      "v_exp_f32 v208, v208\n"
      "v_exp_f32 v209, v209\n"
      "v_exp_f32 v210, v210\n"
      "v_exp_f32 v211, v211\n"
      "v_mfma_f32_32x32x16_bf16 v[224:239], a[4:7], v[116:119], v[224:239]\n"
      "v_exp_f32 v212, v212\n"
      "v_exp_f32 v213, v213\n"
      "v_exp_f32 v214, v214\n"
      "v_exp_f32 v215, v215\n"
      "v_smfmac_f32_32x32x32_bf16 v[176:191], a[140:143], v[248:255], %[idx]\n"
      "v_exp_f32 v216, v216\n"
      "v_exp_f32 v217, v217\n"
      "v_exp_f32 v218, v218\n"
      "v_exp_f32 v219, v219\n"
      "v_smfmac_f32_32x32x32_bf16 v[192:207], a[136:139], v[208:215], %[idx]\n"
      "v_exp_f32 v220, v220\n"
      "v_exp_f32 v221, v221\n"
      "v_exp_f32 v222, v222\n"
      "v_exp_f32 v223, v223\n"
      "v_mfma_f32_32x32x16_bf16 v[240:255], a[96:99], v[120:123], 0\n"
      "s_add_u32 %[so], %[so], 0x1000\n"
      "s_and_b32 %[so], %[so], 0xffff\n"
      "buffer_load_dwordx4 a[64:67], %[vo], %[srd], %[so] offen offset:0 sc1\n"
      "v_exp_f32 v208, v208\n"
      "v_exp_f32 v209, v209\n"
      "v_exp_f32 v210, v210\n"
      "v_exp_f32 v211, v211\n"
      "v_mfma_f32_32x32x16_bf16 v[224:239], a[132:135], v[124:127], v[224:239]\n"
      "v_exp_f32 v212, v212\n"
      "v_exp_f32 v213, v213\n"
      "v_exp_f32 v214, v214\n"
      "v_exp_f32 v215, v215\n"
      "v_exp_f32 v216, v216\n"
      "v_exp_f32 v217, v217\n"
      "v_exp_f32 v218, v218\n"
      "v_exp_f32 v219, v219\n"
      "v_smfmac_f32_32x32x32_bf16 v[144:159], a[136:139], v[208:215], %[idx]\n"
      "v_exp_f32 v220, v220\n"
      "v_exp_f32 v221, v221\n"
      "v_exp_f32 v222, v222\n"
      "v_exp_f32 v223, v223\n"
      "v_mfma_f32_32x32x16_bf16 v[208:223], a[96:99], v[128:131], 0\n"
      "buffer_load_dwordx4 a[68:71], %[vo], %[srd], %[so] offen offset:1024 sc1\n"
      "v_exp_f32 v224, v224\n"
      "v_exp_f32 v225, v225\n"
      "v_exp_f32 v226, v226\n"
      "v_exp_f32 v227, v227\n"
      "v_mfma_f32_32x32x16_bf16 v[240:255], a[132:135], v[132:135], v[240:255]\n"
      "v_exp_f32 v228, v228\n"
      "v_exp_f32 v229, v229\n"
      "v_exp_f32 v230, v230\n"
      "v_exp_f32 v231, v231\n"
      "v_smfmac_f32_32x32x32_bf16 v[144:159], a[140:143], v[216:223], %[idx]\n"
      "v_exp_f32 v232, v232\n"
      "v_exp_f32 v233, v233\n"
      "v_exp_f32 v234, v234\n"
      "v_exp_f32 v235, v235\n"
      "v_smfmac_f32_32x32x32_bf16 v[160:175], a[136:139], v[224:231], %[idx]\n"
      "v_exp_f32 v236, v236\n"
      "v_exp_f32 v237, v237\n"
      "v_exp_f32 v238, v238\n"
      "v_exp_f32 v239, v239\n"
      "v_mfma_f32_32x32x16_bf16 v[224:239], a[96:99], v[136:139], 0\n"
      "buffer_load_dwordx4 a[72:75], %[vo], %[srd], %[so] offen offset:2048 sc1\n"
      "v_exp_f32 v240, v240\n"
      "v_exp_f32 v241, v241\n"
      "v_exp_f32 v242, v242\n"
      "v_exp_f32 v243, v243\n"
      "v_mfma_f32_32x32x16_bf16 v[208:223], a[132:135], v[140:143], v[208:223]\n"
      "v_exp_f32 v244, v244\n"
      "v_exp_f32 v245, v245\n"
      "v_exp_f32 v246, v246\n"
      "v_exp_f32 v247, v247\n"
      "v_smfmac_f32_32x32x32_bf16 v[160:175], a[140:143], v[232:239], %[idx]\n"
      "v_exp_f32 v248, v248\n"
      "v_exp_f32 v249, v249\n"
      "v_exp_f32 v250, v250\n"
      "v_exp_f32 v251, v251\n"
      "v_smfmac_f32_32x32x32_bf16 v[176:191], a[136:139], v[240:247], %[idx]\n"
      "v_exp_f32 v252, v252\n"
      "v_exp_f32 v253, v253\n"
      "v_exp_f32 v254, v254\n"
      "v_exp_f32 v255, v255\n"
      "s_waitcnt vmcnt(19)\n"
      "v_mfma_f32_32x32x16_bf16 v[240:255], a[112:115], v[112:115], 0\n"
      "buffer_load_dwordx4 a[76:79], %[vo], %[srd], %[so] offen offset:3072 sc1\n"
      "v_exp_f32 v208, v208\n"
      "v_exp_f32 v209, v209\n"
      "v_exp_f32 v210, v210\n"
      "v_exp_f32 v211, v211\n"
      "v_mfma_f32_32x32x16_bf16 v[224:239], a[4:7], v[116:119], v[224:239]\n"
      "v_exp_f32 v212, v212\n"
      "v_exp_f32 v213, v213\n"
      "v_exp_f32 v214, v214\n"
      "v_exp_f32 v215, v215\n"
      "v_smfmac_f32_32x32x32_bf16 v[176:191], a[140:143], v[248:255], %[idx]\n"
      "v_exp_f32 v216, v216\n"
      "v_exp_f32 v217, v217\n"
      "v_exp_f32 v218, v218\n"
      "v_exp_f32 v219, v219\n"
      "v_smfmac_f32_32x32x32_bf16 v[192:207], a[136:139], v[208:215], %[idx]\n"
      "v_exp_f32 v220, v220\n"
      "v_exp_f32 v221, v221\n"
      "v_exp_f32 v222, v222\n"
      "v_exp_f32 v223, v223\n"
      "v_mfma_f32_32x32x16_bf16 v[208:223], a[112:115], v[120:123], 0\n"
      "s_add_u32 %[so], %[so], 0x1000\n"
      "s_and_b32 %[so], %[so], 0xffff\n"
      "buffer_load_dwordx4 a[80:83], %[vo], %[srd], %[so] offen offset:0 sc1\n"
      "v_exp_f32 v208, v208\n"
      "v_exp_f32 v209, v209\n"
      "v_exp_f32 v210, v210\n"
      "v_exp_f32 v211, v211\n"
      "v_mfma_f32_32x32x16_bf16 v[224:239], a[132:135], v[124:127], v[224:239]\n"
      "v_exp_f32 v212, v212\n"
      "v_exp_f32 v213, v213\n"
      "v_exp_f32 v214, v214\n"
      "v_exp_f32 v215, v215\n"
      "v_exp_f32 v216, v216\n"
      "v_exp_f32 v217, v217\n"
      "v_exp_f32 v218, v218\n"
      "v_exp_f32 v219, v219\n"
      "v_smfmac_f32_32x32x32_bf16 v[144:159], a[136:139], v[208:215], %[idx]\n"
      "v_exp_f32 v220, v220\n"
      "v_exp_f32 v221, v221\n"
      "v_exp_f32 v222, v222\n"
      "v_exp_f32 v223, v223\n"
      "v_mfma_f32_32x32x16_bf16 v[224:239], a[112:115], v[128:131], 0\n"
      "buffer_load_dwordx4 a[84:87], %[vo], %[srd], %[so] offen offset:1024 sc1\n"
      "v_exp_f32 v224, v224\n"
      "v_exp_f32 v225, v225\n"
      "v_exp_f32 v226, v226\n"
      "v_exp_f32 v227, v227\n"
      "v_mfma_f32_32x32x16_bf16 v[240:255], a[132:135], v[132:135], v[240:255]\n"
      "v_exp_f32 v228, v228\n"
      "v_exp_f32 v229, v229\n"
      "v_exp_f32 v230, v230\n"
      "v_exp_f32 v231, v231\n"
      "v_smfmac_f32_32x32x32_bf16 v[144:159], a[140:143], v[216:223], %[idx]\n"
      "v_exp_f32 v232, v232\n"
      "v_exp_f32 v233, v233\n"
      "v_exp_f32 v234, v234\n"
      "v_exp_f32 v235, v235\n"
      "v_smfmac_f32_32x32x32_bf16 v[160:175], a[136:139], v[224:231], %[idx]\n"
      "v_exp_f32 v236, v236\n"
      "v_exp_f32 v237, v237\n"
      "v_exp_f32 v238, v238\n"
      "v_exp_f32 v239, v239\n"
      "v_mfma_f32_32x32x16_bf16 v[240:255], a[112:115], v[136:139], 0\n"
      "buffer_load_dwordx4 a[88:91], %[vo], %[srd], %[so] offen offset:2048 sc1\n"
      "v_exp_f32 v240, v240\n"
      "v_exp_f32 v241, v241\n"
      "v_exp_f32 v242, v242\n"
      "v_exp_f32 v243, v243\n"
      "v_mfma_f32_32x32x16_bf16 v[208:223], a[132:135], v[140:143], v[208:223]\n"
      "v_exp_f32 v244, v244\n"
      "v_exp_f32 v245, v245\n"
      "v_exp_f32 v246, v246\n"
      "v_exp_f32 v247, v247\n"
      "v_smfmac_f32_32x32x32_bf16 v[160:175], a[140:143], v[232:239], %[idx]\n"
      "v_exp_f32 v248, v248\n"
      "v_exp_f32 v249, v249\n"
      "v_exp_f32 v250, v250\n"
      "v_exp_f32 v251, v251\n"
      "v_smfmac_f32_32x32x32_bf16 v[176:191], a[136:139], v[240:247], %[idx]\n"
      "v_exp_f32 v252, v252\n"
      "v_exp_f32 v253, v253\n"
      "v_exp_f32 v254, v254\n"
      "v_exp_f32 v255, v255\n"
      "s_waitcnt vmcnt(19)\n"
      "v_mfma_f32_32x32x16_bf16 v[208:223], a[0:3], v[112:115], 0\n"
      "buffer_load_dwordx4 a[92:95], %[vo], %[srd], %[so] offen offset:3072 sc1\n"
      "v_exp_f32 v208, v208\n"
      "v_exp_f32 v209, v209\n"
      "v_exp_f32 v210, v210\n"
      "v_exp_f32 v211, v211\n"
      "v_mfma_f32_32x32x16_bf16 v[224:239], a[4:7], v[116:119], v[224:239]\n"
      "v_exp_f32 v212, v212\n"
      "v_exp_f32 v213, v213\n"
      "v_exp_f32 v214, v214\n"
      "v_exp_f32 v215, v215\n"
      "v_smfmac_f32_32x32x32_bf16 v[176:191], a[140:143], v[248:255], %[idx]\n"
      "v_exp_f32 v216, v216\n"
      "v_exp_f32 v217, v217\n"
      "v_exp_f32 v218, v218\n"
      "v_exp_f32 v219, v219\n"
      "v_smfmac_f32_32x32x32_bf16 v[192:207], a[136:139], v[208:215], %[idx]\n"
      "v_exp_f32 v220, v220\n"
      "v_exp_f32 v221, v221\n"
      "v_exp_f32 v222, v222\n"
      "v_exp_f32 v223, v223\n"
      "v_mfma_f32_32x32x16_bf16 v[224:239], a[0:3], v[120:123], 0\n"
      "s_add_u32 %[so], %[so], 0x1000\n"
      "s_and_b32 %[so], %[so], 0xffff\n"
      "buffer_load_dwordx4 a[96:99], %[vo], %[srd], %[so] offen offset:0 sc1\n"
      "v_exp_f32 v208, v208\n"
      "v_exp_f32 v209, v209\n"
      "v_exp_f32 v210, v210\n"
      "v_exp_f32 v211, v211\n"
      "v_mfma_f32_32x32x16_bf16 v[224:239], a[132:135], v[124:127], v[224:239]\n"
      "v_exp_f32 v212, v212\n"
      "v_exp_f32 v213, v213\n"
      "v_exp_f32 v214, v214\n"
      "v_exp_f32 v215, v215\n"
      "v_exp_f32 v216, v216\n"
      "v_exp_f32 v217, v217\n"
      "v_exp_f32 v218, v218\n"
      "v_exp_f32 v219, v219\n"
      "v_smfmac_f32_32x32x32_bf16 v[144:159], a[136:139], v[208:215], %[idx]\n"
      "v_exp_f32 v220, v220\n"
      "v_exp_f32 v221, v221\n"
      "v_exp_f32 v222, v222\n"
      "v_exp_f32 v223, v223\n"
      "v_mfma_f32_32x32x16_bf16 v[240:255], a[0:3], v[128:131], 0\n"
      "buffer_load_dwordx4 a[100:103], %[vo], %[srd], %[so] offen offset:1024 sc1\n"
      "v_exp_f32 v224, v224\n"
      "v_exp_f32 v225, v225\n"
      "v_exp_f32 v226, v226\n"
      "v_exp_f32 v227, v227\n"
      "v_mfma_f32_32x32x16_bf16 v[240:255], a[132:135], v[132:135], v[240:255]\n"
      "v_exp_f32 v228, v228\n"
      "v_exp_f32 v229, v229\n"
      "v_exp_f32 v230, v230\n"
      "v_exp_f32 v231, v231\n"
      "v_smfmac_f32_32x32x32_bf16 v[144:159], a[140:143], v[216:223], %[idx]\n"
      "v_exp_f32 v232, v232\n"
      "v_exp_f32 v233, v233\n"
      "v_exp_f32 v234, v234\n"
      "v_exp_f32 v235, v235\n"
      "v_smfmac_f32_32x32x32_bf16 v[160:175], a[136:139], v[224:231], %[idx]\n"
      "v_exp_f32 v236, v236\n"
      "v_exp_f32 v237, v237\n"
      "v_exp_f32 v238, v238\n"
      "v_exp_f32 v239, v239\n"
      "v_mfma_f32_32x32x16_bf16 v[208:223], a[0:3], v[136:139], 0\n"
      "buffer_load_dwordx4 a[104:107], %[vo], %[srd], %[so] offen offset:2048 sc1\n"
      "v_exp_f32 v240, v240\n"
      "v_exp_f32 v241, v241\n"
      "v_exp_f32 v242, v242\n"
      "v_exp_f32 v243, v243\n"
      "v_mfma_f32_32x32x16_bf16 v[208:223], a[132:135], v[140:143], v[208:223]\n"
      "v_exp_f32 v244, v244\n"
      "v_exp_f32 v245, v245\n"
      "v_exp_f32 v246, v246\n"
      "v_exp_f32 v247, v247\n"
      "v_smfmac_f32_32x32x32_bf16 v[160:175], a[140:143], v[232:239], %[idx]\n"
      "v_exp_f32 v248, v248\n"
      "v_exp_f32 v249, v249\n"
      "v_exp_f32 v250, v250\n"
      "v_exp_f32 v251, v251\n"
      "v_smfmac_f32_32x32x32_bf16 v[176:191], a[136:139], v[240:247], %[idx]\n"
      "v_exp_f32 v252, v252\n"
      "v_exp_f32 v253, v253\n"
      "v_exp_f32 v254, v254\n"
      "v_exp_f32 v255, v255\n"
      "s_waitcnt vmcnt(19)\n"
      "v_mfma_f32_32x32x16_bf16 v[224:239], a[16:19], v[112:115], 0\n"
      "buffer_load_dwordx4 a[108:111], %[vo], %[srd], %[so] offen offset:3072 sc1\n"
      "v_exp_f32 v208, v208\n"
      "v_exp_f32 v209, v209\n"
      "v_exp_f32 v210, v210\n"
      "v_exp_f32 v211, v211\n"
      "v_mfma_f32_32x32x16_bf16 v[224:239], a[4:7], v[116:119], v[224:239]\n"
      "v_exp_f32 v212, v212\n"
      "v_exp_f32 v213, v213\n"
      "v_exp_f32 v214, v214\n"
      "v_exp_f32 v215, v215\n"
      "v_smfmac_f32_32x32x32_bf16 v[176:191], a[140:143], v[248:255], %[idx]\n"
      "v_exp_f32 v216, v216\n"
      "v_exp_f32 v217, v217\n"
      "v_exp_f32 v218, v218\n"
      "v_exp_f32 v219, v219\n"
      "v_smfmac_f32_32x32x32_bf16 v[192:207], a[136:139], v[208:215], %[idx]\n"
      "v_exp_f32 v220, v220\n"
      "v_exp_f32 v221, v221\n"
      "v_exp_f32 v222, v222\n"
      "v_exp_f32 v223, v223\n"
      "v_mfma_f32_32x32x16_bf16 v[240:255], a[16:19], v[120:123], 0\n"
      "v_exp_f32 v224, v224\n"
      "v_exp_f32 v225, v225\n"
      "v_exp_f32 v226, v226\n"
      "v_exp_f32 v227, v227\n"
      "v_mfma_f32_32x32x16_bf16 v[240:255], a[20:23], v[124:127], v[240:255]\n"
      "v_exp_f32 v228, v228\n"
      "v_exp_f32 v229, v229\n"
      "v_exp_f32 v230, v230\n"
      "v_exp_f32 v231, v231\n"
      "v_smfmac_f32_32x32x32_bf16 v[192:207], a[12:15], v[216:223], %[idx]\n"
      "v_exp_f32 v232, v232\n"
      "v_exp_f32 v233, v233\n"
      "v_exp_f32 v234, v234\n"
      "v_exp_f32 v235, v235\n"
      "v_smfmac_f32_32x32x32_bf16 v[144:159], a[24:27], v[224:231], %[idx]\n"
      "v_exp_f32 v236, v236\n"
      "v_exp_f32 v237, v237\n"
      "v_exp_f32 v238, v238\n"
      "v_exp_f32 v239, v239\n"
      "v_mfma_f32_32x32x16_bf16 v[208:223], a[16:19], v[128:131], 0\n"
      "v_exp_f32 v240, v240\n"
      "v_exp_f32 v241, v241\n"
      "v_exp_f32 v242, v242\n"
      "v_exp_f32 v243, v243\n"
      "v_mfma_f32_32x32x16_bf16 v[208:223], a[20:23], v[132:135], v[208:223]\n"
      "v_exp_f32 v244, v244\n"
      "v_exp_f32 v245, v245\n"
      "v_exp_f32 v246, v246\n"
      "v_exp_f32 v247, v247\n"
      "v_smfmac_f32_32x32x32_bf16 v[144:159], a[28:31], v[232:239], %[idx]\n"
      "v_exp_f32 v248, v248\n"
      "v_exp_f32 v249, v249\n"
      "v_exp_f32 v250, v250\n"
      "v_exp_f32 v251, v251\n"
      "v_smfmac_f32_32x32x32_bf16 v[160:175], a[24:27], v[240:247], %[idx]\n"
      "v_exp_f32 v252, v252\n"
      "v_exp_f32 v253, v253\n"
      "v_exp_f32 v254, v254\n"
      "v_exp_f32 v255, v255\n"
      "v_mfma_f32_32x32x16_bf16 v[224:239], a[16:19], v[136:139], 0\n"
      "v_exp_f32 v208, v208\n"
      "v_exp_f32 v209, v209\n"
      "v_exp_f32 v210, v210\n"
      "v_exp_f32 v211, v211\n"
      "v_mfma_f32_32x32x16_bf16 v[224:239], a[20:23], v[140:143], v[224:239]\n"
      "v_exp_f32 v212, v212\n"
      "v_exp_f32 v213, v213\n"
      "v_exp_f32 v214, v214\n"
      "v_exp_f32 v215, v215\n"
      "v_smfmac_f32_32x32x32_bf16 v[160:175], a[28:31], v[248:255], %[idx]\n"
      "v_exp_f32 v216, v216\n"
      "v_exp_f32 v217, v217\n"
      "v_exp_f32 v218, v218\n"
      "v_exp_f32 v219, v219\n"
      "v_smfmac_f32_32x32x32_bf16 v[176:191], a[24:27], v[208:215], %[idx]\n"
      "v_exp_f32 v220, v220\n"
      "v_exp_f32 v221, v221\n"
      "v_exp_f32 v222, v222\n"
      "v_exp_f32 v223, v223\n"
      "s_waitcnt vmcnt(16)\n"
      "v_mfma_f32_32x32x16_bf16 v[240:255], a[32:35], v[112:115], 0\n"
      "v_exp_f32 v224, v224\n"
      "v_exp_f32 v225, v225\n"
      "v_exp_f32 v226, v226\n"
      "v_exp_f32 v227, v227\n"
      "v_mfma_f32_32x32x16_bf16 v[240:255], a[36:39], v[116:119], v[240:255]\n"
      "v_exp_f32 v228, v228\n"
      "v_exp_f32 v229, v229\n"
      "v_exp_f32 v230, v230\n"
      "v_exp_f32 v231, v231\n"
      "v_smfmac_f32_32x32x32_bf16 v[176:191], a[28:31], v[216:223], %[idx]\n"
      "v_exp_f32 v232, v232\n"
      "v_exp_f32 v233, v233\n"
      "v_exp_f32 v234, v234\n"
      "v_exp_f32 v235, v235\n"
      "v_smfmac_f32_32x32x32_bf16 v[192:207], a[24:27], v[224:231], %[idx]\n"
      "v_exp_f32 v236, v236\n"
      "v_exp_f32 v237, v237\n"
      "v_exp_f32 v238, v238\n"
      "v_exp_f32 v239, v239\n"
      "v_mfma_f32_32x32x16_bf16 v[208:223], a[32:35], v[120:123], 0\n"
      "v_exp_f32 v240, v240\n"
      "v_exp_f32 v241, v241\n"
      "v_exp_f32 v242, v242\n"
      "v_exp_f32 v243, v243\n"
      "v_mfma_f32_32x32x16_bf16 v[208:223], a[36:39], v[124:127], v[208:223]\n"
      "v_exp_f32 v244, v244\n"
      "v_exp_f32 v245, v245\n"
      "v_exp_f32 v246, v246\n"
      "v_exp_f32 v247, v247\n"
      "v_smfmac_f32_32x32x32_bf16 v[192:207], a[28:31], v[232:239], %[idx]\n"
      "v_exp_f32 v248, v248\n"
      "v_exp_f32 v249, v249\n"
      "v_exp_f32 v250, v250\n"
      "v_exp_f32 v251, v251\n"
      "v_smfmac_f32_32x32x32_bf16 v[144:159], a[40:43], v[240:247], %[idx]\n"
      "v_exp_f32 v252, v252\n"
      "v_exp_f32 v253, v253\n"
      "v_exp_f32 v254, v254\n"
      "v_exp_f32 v255, v255\n"
      "v_mfma_f32_32x32x16_bf16 v[224:239], a[32:35], v[128:131], 0\n"
      "v_exp_f32 v208, v208\n"
      "v_exp_f32 v209, v209\n"
      "v_exp_f32 v210, v210\n"
      "v_exp_f32 v211, v211\n"
      "v_mfma_f32_32x32x16_bf16 v[224:239], a[36:39], v[132:135], v[224:239]\n"
      "v_exp_f32 v212, v212\n"
      "v_exp_f32 v213, v213\n"
      "v_exp_f32 v214, v214\n"
      "v_exp_f32 v215, v215\n"
      "v_smfmac_f32_32x32x32_bf16 v[144:159], a[44:47], v[248:255], %[idx]\n"
      "v_exp_f32 v216, v216\n"
      "v_exp_f32 v217, v217\n"
      "v_exp_f32 v218, v218\n"
      "v_exp_f32 v219, v219\n"
      "v_smfmac_f32_32x32x32_bf16 v[160:175], a[40:43], v[208:215], %[idx]\n"
      "v_exp_f32 v220, v220\n"
      "v_exp_f32 v221, v221\n"
      "v_exp_f32 v222, v222\n"
      "v_exp_f32 v223, v223\n"
      "v_mfma_f32_32x32x16_bf16 v[240:255], a[32:35], v[136:139], 0\n"
      "v_exp_f32 v224, v224\n"
      "v_exp_f32 v225, v225\n"
      "v_exp_f32 v226, v226\n"
      "v_exp_f32 v227, v227\n"
      "v_mfma_f32_32x32x16_bf16 v[240:255], a[36:39], v[140:143], v[240:255]\n"
      "v_exp_f32 v228, v228\n"
      "v_exp_f32 v229, v229\n"
      "v_exp_f32 v230, v230\n"
      "v_exp_f32 v231, v231\n"
      "v_smfmac_f32_32x32x32_bf16 v[160:175], a[44:47], v[216:223], %[idx]\n"
      "v_exp_f32 v232, v232\n"
      "v_exp_f32 v233, v233\n"
      "v_exp_f32 v234, v234\n"
      "v_exp_f32 v235, v235\n"
      "v_smfmac_f32_32x32x32_bf16 v[176:191], a[40:43], v[224:231], %[idx]\n"
      "v_exp_f32 v236, v236\n"
      "v_exp_f32 v237, v237\n"
      "v_exp_f32 v238, v238\n"
      "v_exp_f32 v239, v239\n"
      "s_waitcnt vmcnt(12)\n"
      "v_mfma_f32_32x32x16_bf16 v[208:223], a[48:51], v[112:115], 0\n"
      "v_exp_f32 v240, v240\n"
      "v_exp_f32 v241, v241\n"
      "v_exp_f32 v242, v242\n"
      "v_exp_f32 v243, v243\n"
      "v_mfma_f32_32x32x16_bf16 v[208:223], a[52:55], v[116:119], v[208:223]\n"
      "v_exp_f32 v244, v244\n"
      "v_exp_f32 v245, v245\n"
      "v_exp_f32 v246, v246\n"
      "v_exp_f32 v247, v247\n"
      "v_smfmac_f32_32x32x32_bf16 v[176:191], a[44:47], v[232:239], %[idx]\n"
      "v_exp_f32 v248, v248\n"
      "v_exp_f32 v249, v249\n"
      "v_exp_f32 v250, v250\n"
      "v_exp_f32 v251, v251\n"
      "v_smfmac_f32_32x32x32_bf16 v[192:207], a[40:43], v[240:247], %[idx]\n"
      "v_exp_f32 v252, v252\n"
      "v_exp_f32 v253, v253\n"
      "v_exp_f32 v254, v254\n"
      "v_exp_f32 v255, v255\n"
      "v_mfma_f32_32x32x16_bf16 v[224:239], a[48:51], v[120:123], 0\n"
      "v_exp_f32 v208, v208\n"
      "v_exp_f32 v209, v209\n"
      "v_exp_f32 v210, v210\n"
      "v_exp_f32 v211, v211\n"
      "v_mfma_f32_32x32x16_bf16 v[224:239], a[52:55], v[124:127], v[224:239]\n"
      "v_exp_f32 v212, v212\n"
      "v_exp_f32 v213, v213\n"
      "v_exp_f32 v214, v214\n"
      "v_exp_f32 v215, v215\n"
      "v_smfmac_f32_32x32x32_bf16 v[192:207], a[44:47], v[248:255], %[idx]\n"
      "v_exp_f32 v216, v216\n"
      "v_exp_f32 v217, v217\n"
      "v_exp_f32 v218, v218\n"
      "v_exp_f32 v219, v219\n"
      "v_smfmac_f32_32x32x32_bf16 v[144:159], a[56:59], v[208:215], %[idx]\n"
      "v_exp_f32 v220, v220\n"
      "v_exp_f32 v221, v221\n"
      "v_exp_f32 v222, v222\n"
      "v_exp_f32 v223, v223\n"
      "v_mfma_f32_32x32x16_bf16 v[240:255], a[48:51], v[128:131], 0\n"
      "v_exp_f32 v224, v224\n"
      "v_exp_f32 v225, v225\n"
      "v_exp_f32 v226, v226\n"
      "v_exp_f32 v227, v227\n"
      "v_mfma_f32_32x32x16_bf16 v[240:255], a[52:55], v[132:135], v[240:255]\n"
      "v_exp_f32 v228, v228\n"
      "v_exp_f32 v229, v229\n"
      "v_exp_f32 v230, v230\n"
      "v_exp_f32 v231, v231\n"
      "v_smfmac_f32_32x32x32_bf16 v[144:159], a[60:63], v[216:223], %[idx]\n"
      "v_exp_f32 v232, v232\n"
      "v_exp_f32 v233, v233\n"
      "v_exp_f32 v234, v234\n"
      "v_exp_f32 v235, v235\n"
      "v_smfmac_f32_32x32x32_bf16 v[160:175], a[56:59], v[224:231], %[idx]\n"
      "v_exp_f32 v236, v236\n"
      "v_exp_f32 v237, v237\n"
      "v_exp_f32 v238, v238\n"
      "v_exp_f32 v239, v239\n"
      "v_mfma_f32_32x32x16_bf16 v[208:223], a[48:51], v[136:139], 0\n"
      "v_exp_f32 v240, v240\n"
      "v_exp_f32 v241, v241\n"
      "v_exp_f32 v242, v242\n"
      "v_exp_f32 v243, v243\n"
      "v_mfma_f32_32x32x16_bf16 v[208:223], a[52:55], v[140:143], v[208:223]\n"
      "v_exp_f32 v244, v244\n"
      "v_exp_f32 v245, v245\n"
      "v_exp_f32 v246, v246\n"
      "v_exp_f32 v247, v247\n"
      "v_smfmac_f32_32x32x32_bf16 v[160:175], a[60:63], v[232:239], %[idx]\n"
      "v_exp_f32 v248, v248\n"
      "v_exp_f32 v249, v249\n"
      "v_exp_f32 v250, v250\n"
      "v_exp_f32 v251, v251\n"
      "v_smfmac_f32_32x32x32_bf16 v[176:191], a[56:59], v[240:247], %[idx]\n"
      "v_exp_f32 v252, v252\n"
      "v_exp_f32 v253, v253\n"
      "v_exp_f32 v254, v254\n"
      "v_exp_f32 v255, v255\n"
      "s_waitcnt vmcnt(8)\n"
      "v_mfma_f32_32x32x16_bf16 v[224:239], a[64:67], v[112:115], 0\n"
      "v_exp_f32 v208, v208\n"
      "v_exp_f32 v209, v209\n"
      "v_exp_f32 v210, v210\n"
      "v_exp_f32 v211, v211\n"
      "v_mfma_f32_32x32x16_bf16 v[224:239], a[68:71], v[116:119], v[224:239]\n"
      "v_exp_f32 v212, v212\n"
      "v_exp_f32 v213, v213\n"
      "v_exp_f32 v214, v214\n"
      "v_exp_f32 v215, v215\n"
      "v_smfmac_f32_32x32x32_bf16 v[176:191], a[60:63], v[248:255], %[idx]\n"
      "v_exp_f32 v216, v216\n"
      "v_exp_f32 v217, v217\n"
      "v_exp_f32 v218, v218\n"
      "v_exp_f32 v219, v219\n"
      "v_smfmac_f32_32x32x32_bf16 v[192:207], a[56:59], v[208:215], %[idx]\n"
      "v_exp_f32 v220, v220\n"
      "v_exp_f32 v221, v221\n"
      "v_exp_f32 v222, v222\n"
      "v_exp_f32 v223, v223\n"
      "v_mfma_f32_32x32x16_bf16 v[240:255], a[64:67], v[120:123], 0\n"
      "v_exp_f32 v224, v224\n"
      "v_exp_f32 v225, v225\n"
      "v_exp_f32 v226, v226\n"
      "v_exp_f32 v227, v227\n"
      "v_mfma_f32_32x32x16_bf16 v[240:255], a[68:71], v[124:127], v[240:255]\n"
      "v_exp_f32 v228, v228\n"
      "v_exp_f32 v229, v229\n"
      "v_exp_f32 v230, v230\n"
      "v_exp_f32 v231, v231\n"
      "v_smfmac_f32_32x32x32_bf16 v[192:207], a[60:63], v[216:223], %[idx]\n"
      "v_exp_f32 v232, v232\n"
      "v_exp_f32 v233, v233\n"
      "v_exp_f32 v234, v234\n"
      "v_exp_f32 v235, v235\n"
      "v_smfmac_f32_32x32x32_bf16 v[144:159], a[72:75], v[224:231], %[idx]\n"
      "v_exp_f32 v236, v236\n"
      "v_exp_f32 v237, v237\n"
      "v_exp_f32 v238, v238\n"
      "v_exp_f32 v239, v239\n"
      "v_mfma_f32_32x32x16_bf16 v[208:223], a[64:67], v[128:131], 0\n"
      "v_exp_f32 v240, v240\n"
      "v_exp_f32 v241, v241\n"
      "v_exp_f32 v242, v242\n"
      "v_exp_f32 v243, v243\n"
      "v_mfma_f32_32x32x16_bf16 v[208:223], a[68:71], v[132:135], v[208:223]\n"
      "v_exp_f32 v244, v244\n"
      "v_exp_f32 v245, v245\n"
      "v_exp_f32 v246, v246\n"
      "v_exp_f32 v247, v247\n"
      "v_smfmac_f32_32x32x32_bf16 v[144:159], a[76:79], v[232:239], %[idx]\n"
      "v_exp_f32 v248, v248\n"
      "v_exp_f32 v249, v249\n"
      "v_exp_f32 v250, v250\n"
      "v_exp_f32 v251, v251\n"
      "v_smfmac_f32_32x32x32_bf16 v[160:175], a[72:75], v[240:247], %[idx]\n"
      "v_exp_f32 v252, v252\n"
      "v_exp_f32 v253, v253\n"
      "v_exp_f32 v254, v254\n"
      "v_exp_f32 v255, v255\n"
      "v_mfma_f32_32x32x16_bf16 v[224:239], a[64:67], v[136:139], 0\n"
      "v_exp_f32 v208, v208\n"
      "v_exp_f32 v209, v209\n"
      "v_exp_f32 v210, v210\n"
      "v_exp_f32 v211, v211\n"
      "v_mfma_f32_32x32x16_bf16 v[224:239], a[68:71], v[140:143], v[224:239]\n"
      "v_exp_f32 v212, v212\n"
      "v_exp_f32 v213, v213\n"
      "v_exp_f32 v214, v214\n"
      "v_exp_f32 v215, v215\n"
      "v_smfmac_f32_32x32x32_bf16 v[160:175], a[76:79], v[248:255], %[idx]\n"
      "v_exp_f32 v216, v216\n"
      "v_exp_f32 v217, v217\n"
      "v_exp_f32 v218, v218\n"
      "v_exp_f32 v219, v219\n"
      "v_smfmac_f32_32x32x32_bf16 v[176:191], a[72:75], v[208:215], %[idx]\n"
      "v_exp_f32 v220, v220\n"
      "v_exp_f32 v221, v221\n"
      "v_exp_f32 v222, v222\n"
      "v_exp_f32 v223, v223\n"
      "s_waitcnt vmcnt(4)\n"
      "v_mfma_f32_32x32x16_bf16 v[240:255], a[80:83], v[112:115], 0\n"
      "v_exp_f32 v224, v224\n"
      "v_exp_f32 v225, v225\n"
      "v_exp_f32 v226, v226\n"
      "v_exp_f32 v227, v227\n"
      "v_mfma_f32_32x32x16_bf16 v[240:255], a[84:87], v[116:119], v[240:255]\n"
      "v_exp_f32 v228, v228\n"
      "v_exp_f32 v229, v229\n"
      "v_exp_f32 v230, v230\n"
      "v_exp_f32 v231, v231\n"
      "v_smfmac_f32_32x32x32_bf16 v[176:191], a[76:79], v[216:223], %[idx]\n"
      "v_exp_f32 v232, v232\n"
      "v_exp_f32 v233, v233\n"
      "v_exp_f32 v234, v234\n"
      "v_exp_f32 v235, v235\n"
      "v_smfmac_f32_32x32x32_bf16 v[192:207], a[72:75], v[224:231], %[idx]\n"
      "v_exp_f32 v236, v236\n"
      "v_exp_f32 v237, v237\n"
      "v_exp_f32 v238, v238\n"
      "v_exp_f32 v239, v239\n"
      "v_mfma_f32_32x32x16_bf16 v[208:223], a[80:83], v[120:123], 0\n"
      "v_exp_f32 v240, v240\n"
      "v_exp_f32 v241, v241\n"
      "v_exp_f32 v242, v242\n"
      "v_exp_f32 v243, v243\n"
      "v_mfma_f32_32x32x16_bf16 v[208:223], a[84:87], v[124:127], v[208:223]\n"
      "v_exp_f32 v244, v244\n"
      "v_exp_f32 v245, v245\n"
      "v_exp_f32 v246, v246\n"
      "v_exp_f32 v247, v247\n"
      "v_smfmac_f32_32x32x32_bf16 v[192:207], a[76:79], v[232:239], %[idx]\n"
      "v_exp_f32 v248, v248\n"
      "v_exp_f32 v249, v249\n"
      "v_exp_f32 v250, v250\n"
      "v_exp_f32 v251, v251\n"
      "v_smfmac_f32_32x32x32_bf16 v[144:159], a[88:91], v[240:247], %[idx]\n"
      "v_exp_f32 v252, v252\n"
      "v_exp_f32 v253, v253\n"
      "v_exp_f32 v254, v254\n"
      "v_exp_f32 v255, v255\n"
      "v_mfma_f32_32x32x16_bf16 v[224:239], a[80:83], v[128:131], 0\n"
      "v_exp_f32 v208, v208\n"
      "v_exp_f32 v209, v209\n"
      "v_exp_f32 v210, v210\n"
      "v_exp_f32 v211, v211\n"
      "v_mfma_f32_32x32x16_bf16 v[224:239], a[84:87], v[132:135], v[224:239]\n"
      "v_exp_f32 v212, v212\n"
      "v_exp_f32 v213, v213\n"
      "v_exp_f32 v214, v214\n"
      "v_exp_f32 v215, v215\n"
      "v_smfmac_f32_32x32x32_bf16 v[144:159], a[92:95], v[248:255], %[idx]\n"
      "v_exp_f32 v216, v216\n"
      "v_exp_f32 v217, v217\n"
      "v_exp_f32 v218, v218\n"
      "v_exp_f32 v219, v219\n"
      "v_smfmac_f32_32x32x32_bf16 v[160:175], a[88:91], v[208:215], %[idx]\n"
      "v_exp_f32 v220, v220\n"
      "v_exp_f32 v221, v221\n"
      "v_exp_f32 v222, v222\n"
      "v_exp_f32 v223, v223\n"
      "v_mfma_f32_32x32x16_bf16 v[240:255], a[80:83], v[136:139], 0\n"
      "v_exp_f32 v224, v224\n"
      "v_exp_f32 v225, v225\n"
      "v_exp_f32 v226, v226\n"
      "v_exp_f32 v227, v227\n"
      "v_mfma_f32_32x32x16_bf16 v[240:255], a[84:87], v[140:143], v[240:255]\n"
      "v_exp_f32 v228, v228\n"
      "v_exp_f32 v229, v229\n"
      "v_exp_f32 v230, v230\n"
      "v_exp_f32 v231, v231\n"
      "v_smfmac_f32_32x32x32_bf16 v[160:175], a[92:95], v[216:223], %[idx]\n"
      "v_exp_f32 v232, v232\n"
      "v_exp_f32 v233, v233\n"
      "v_exp_f32 v234, v234\n"
      "v_exp_f32 v235, v235\n"
      "v_smfmac_f32_32x32x32_bf16 v[176:191], a[88:91], v[224:231], %[idx]\n"
      "v_exp_f32 v236, v236\n"
      "v_exp_f32 v237, v237\n"
      "v_exp_f32 v238, v238\n"
      "v_exp_f32 v239, v239\n"
      "s_waitcnt vmcnt(0)\n"
      "v_mfma_f32_32x32x16_bf16 v[208:223], a[96:99], v[112:115], 0\n"
      "v_exp_f32 v240, v240\n"
      "v_exp_f32 v241, v241\n"
      "v_exp_f32 v242, v242\n"
      "v_exp_f32 v243, v243\n"
      "v_mfma_f32_32x32x16_bf16 v[208:223], a[100:103], v[116:119], v[208:223]\n"
      "v_exp_f32 v244, v244\n"
      "v_exp_f32 v245, v245\n"
      "v_exp_f32 v246, v246\n"
      "v_exp_f32 v247, v247\n"
      "v_smfmac_f32_32x32x32_bf16 v[176:191], a[92:95], v[232:239], %[idx]\n"
      "v_exp_f32 v248, v248\n"
      "v_exp_f32 v249, v249\n"
      "v_exp_f32 v250, v250\n"
      "v_exp_f32 v251, v251\n"
      "v_smfmac_f32_32x32x32_bf16 v[192:207], a[88:91], v[240:247], %[idx]\n"
      "v_exp_f32 v252, v252\n"
      "v_exp_f32 v253, v253\n"
      "v_exp_f32 v254, v254\n"
      "v_exp_f32 v255, v255\n"
      "v_mfma_f32_32x32x16_bf16 v[224:239], a[96:99], v[120:123], 0\n"
      "v_exp_f32 v208, v208\n"
      "v_exp_f32 v209, v209\n"
      "v_exp_f32 v210, v210\n"
      "v_exp_f32 v211, v211\n"
      "v_mfma_f32_32x32x16_bf16 v[224:239], a[100:103], v[124:127], v[224:239]\n"
      "v_exp_f32 v212, v212\n"
      "v_exp_f32 v213, v213\n"
      "v_exp_f32 v214, v214\n"
      "v_exp_f32 v215, v215\n"
      "v_smfmac_f32_32x32x32_bf16 v[192:207], a[92:95], v[248:255], %[idx]\n"
      "v_exp_f32 v216, v216\n"
      "v_exp_f32 v217, v217\n"
      "v_exp_f32 v218, v218\n"
      "v_exp_f32 v219, v219\n"
      "v_smfmac_f32_32x32x32_bf16 v[144:159], a[104:107], v[208:215], %[idx]\n"
      "v_exp_f32 v220, v220\n"
      "v_exp_f32 v221, v221\n"
      "v_exp_f32 v222, v222\n"
      "v_exp_f32 v223, v223\n"
      "v_mfma_f32_32x32x16_bf16 v[240:255], a[96:99], v[128:131], 0\n"
      "v_exp_f32 v224, v224\n"
      "v_exp_f32 v225, v225\n"
      "v_exp_f32 v226, v226\n"
      "v_exp_f32 v227, v227\n"
      "v_mfma_f32_32x32x16_bf16 v[240:255], a[100:103], v[132:135], v[240:255]\n"
      "v_exp_f32 v228, v228\n"
      "v_exp_f32 v229, v229\n"
      "v_exp_f32 v230, v230\n"
      "v_exp_f32 v231, v231\n"
      "v_smfmac_f32_32x32x32_bf16 v[144:159], a[108:111], v[216:223], %[idx]\n"
      "v_exp_f32 v232, v232\n"
      "v_exp_f32 v233, v233\n"
      "v_exp_f32 v234, v234\n"
      "v_exp_f32 v235, v235\n"
      "v_smfmac_f32_32x32x32_bf16 v[160:175], a[104:107], v[224:231], %[idx]\n"
      "v_exp_f32 v236, v236\n"
      "v_exp_f32 v237, v237\n"
      "v_exp_f32 v238, v238\n"
      "v_exp_f32 v239, v239\n"
      "v_mfma_f32_32x32x16_bf16 v[208:223], a[96:99], v[136:139], 0\n"
      "v_exp_f32 v240, v240\n"
      "v_exp_f32 v241, v241\n"
      "v_exp_f32 v242, v242\n"
      "v_exp_f32 v243, v243\n"
      "v_mfma_f32_32x32x16_bf16 v[208:223], a[100:103], v[140:143], v[208:223]\n"
      "ds_write_b128 %[la0], v[144:147] offset:0\n"
      "ds_write_b128 %[la0], v[148:151] offset:1024\n"
      "v_exp_f32 v244, v244\n"
      "v_exp_f32 v245, v245\n"
      "v_exp_f32 v246, v246\n"
      "v_exp_f32 v247, v247\n"
      "v_smfmac_f32_32x32x32_bf16 v[160:175], a[108:111], v[232:239], %[idx]\n"
      "ds_write_b128 %[la0], v[152:155] offset:2048\n"
      "ds_write_b128 %[la0], v[156:159] offset:3072\n"
      "v_exp_f32 v248, v248\n"
      "v_exp_f32 v249, v249\n"
      "v_exp_f32 v250, v250\n"
      "v_exp_f32 v251, v251\n"
      "v_smfmac_f32_32x32x32_bf16 v[176:191], a[104:107], v[240:247], %[idx]\n"
      "v_exp_f32 v252, v252\n"
      "v_exp_f32 v253, v253\n"
      "v_exp_f32 v254, v254\n"
      "v_exp_f32 v255, v255\n"
      "v_exp_f32 v208, v208\n"
      "v_exp_f32 v209, v209\n"
      "v_exp_f32 v210, v210\n"
      "v_exp_f32 v211, v211\n"
      "ds_write_b128 %[la1], v[160:163] offset:0\n"
      "ds_write_b128 %[la1], v[164:167] offset:1024\n"
      "v_exp_f32 v212, v212\n"
      "v_exp_f32 v213, v213\n"
      "v_exp_f32 v214, v214\n"
      "v_exp_f32 v215, v215\n"
      "v_smfmac_f32_32x32x32_bf16 v[176:191], a[108:111], v[248:255], %[idx]\n"
      "ds_write_b128 %[la1], v[168:171] offset:2048\n"
      "ds_write_b128 %[la1], v[172:175] offset:3072\n"
      "v_exp_f32 v216, v216\n"
      "v_exp_f32 v217, v217\n"
      "v_exp_f32 v218, v218\n"
      "v_exp_f32 v219, v219\n"
      "v_smfmac_f32_32x32x32_bf16 v[192:207], a[104:107], v[208:215], %[idx]\n"
      "v_exp_f32 v220, v220\n"
      "v_exp_f32 v221, v221\n"
      "v_exp_f32 v222, v222\n"
      "v_exp_f32 v223, v223\n"
      "s_nop 1\n"
      "v_smfmac_f32_32x32x32_bf16 v[192:207], a[108:111], v[216:223], %[idx]\n"
      "s_nop 1\n"
      "ds_write_b128 %[la2], v[176:179] offset:0\n"
      "ds_write_b128 %[la2], v[180:183] offset:1024\n"
      "ds_write_b128 %[la2], v[184:187] offset:2048\n"
      "ds_write_b128 %[la2], v[188:191] offset:3072\n"
      "s_nop 7\n"
      "s_nop 3\n"
      "s_waitcnt lgkmcnt(0)\n"
      "s_barrier\n"
            : "=&{v[192:207]}"(o), [so] "+s"(so)
            : [vo] "v"(kvs + l16), [srd] "s"(ws_srd), [idx] "v"(0xddddu),
              [qa0] "v"(qb + ((w + 1) & 3) * 2048), [qa1] "v"(qb + ((w + 2) & 3) * 2048), [qa2] "v"(qb + ((w + 3) & 3) * 2048), [qa3] "v"(qb + w * 2048),
              [la0] "v"(qb + (((w + 1) & 3) * 3 + 0) * 4096), [la1] "v"(qb + (((w + 2) & 3) * 3 + 1) * 4096), [la2] "v"(qb + (((w + 3) & 3) * 3 + 2) * 4096),
              "{a[128:131]}"(k0), "{a[132:135]}"(k1), "{a[136:139]}"(v0), "{a[140:143]}"(v1)
            : "memory", "scc", "a0","a1","a2","a3","a4","a5","a6","a7","a8","a9","a10","a11","a12","a13","a14","a15","a16","a17","a18","a19","a20","a21","a22","a23","a24","a25","a26","a27","a28","a29","a30","a31","a32","a33","a34","a35","a36","a37","a38","a39","a40","a41","a42","a43","a44","a45","a46","a47","a48","a49","a50","a51","a52","a53","a54","a55","a56","a57","a58","a59","a60","a61","a62","a63","a64","a65","a66","a67","a68","a69","a70","a71","a72","a73","a74","a75","a76","a77","a78","a79","a80","a81","a82","a83","a84","a85","a86","a87","a88","a89","a90","a91","a92","a93","a94","a95","a96","a97","a98","a99","a100","a101","a102","a103","a104","a105","a106","a107","a108","a109","a110","a111","a112","a113","a114","a115","a116","a117","a118","a119","a120","a121","a122","a123","a124","a125","a126","a127","v112","v113","v114","v115","v116","v117","v118","v119","v120","v121","v122","v123","v124","v125","v126","v127","v128","v129","v130","v131","v132","v133","v134","v135","v136","v137","v138","v139","v140","v141","v142","v143","v144","v145","v146","v147","v148","v149","v150","v151","v152","v153","v154","v155","v156","v157","v158","v159","v160","v161","v162","v163","v164","v165","v166","v167","v168","v169","v170","v171","v172","v173","v174","v175","v176","v177","v178","v179","v180","v181","v182","v183","v184","v185","v186","v187","v188","v189","v190","v191","v208","v209","v210","v211","v212","v213","v214","v215","v216","v217","v218","v219","v220","v221","v222","v223","v224","v225","v226","v227","v228","v229","v230","v231","v232","v233","v234","v235","v236","v237","v238","v239","v240","v241","v242","v243","v244","v245","v246","v247","v248","v249","v250","v251","v252","v253","v254","v255");
        REFRESH();
#pragma unroll
        for (int j = 0; j < 3; ++j)
#pragma unroll
          for (int c = 0; c < 4; ++c) { f32x4 t = *(LAS f32x4*)(lds + L_SCR + (w * 3 + j) * 4096 + c * 1024 + lane * 16); o[4 * c] += t[0]; o[4 * c + 1] += t[1]; o[4 * c + 2] += t[2]; o[4 * c + 3] += t[3]; }
        const float l = upper_half(o[12]);
        store_o(rep * 2 + 1, o, __builtin_amdgcn_rcpf(l));
        float amax = 0.f;
#pragma unroll
        for (int r = 0; r < 16; ++r) amax = fmaxf(amax, fabsf(o[r]) + 0.f * o[r]);
        bool okv = l < 1.8e19f && l > 1e-12f;
#pragma unroll
        for (int r = 0; r < 16; ++r) okv = okv && fabsf(o[r]) < 3.0e38f;
        const int bad = (pass == 0 && !__all(okv)) ? 1 : 0;
        LGKM0();
        if (lane == 0) *(LAS int*)(lds + L_SCR + w * 3 * 4096) = bad;
        DRAIN_BAR();
        const int anybad = *(LAS int*)(lds + L_SCR) | *(LAS int*)(lds + L_SCR + 3 * 4096) | *(LAS int*)(lds + L_SCR + 6 * 4096) | *(LAS int*)(lds + L_SCR + 9 * 4096);
        if (!__builtin_amdgcn_readfirstlane(anybad)) break;
        BAR();
        float mx = -INFINITY;
        const unsigned kb = WS_KV + rep * KV_L + h * 4 * 65536 + lane * 16;
#pragma unroll 1
        for (int t = 0; t < 64; ++t) {
          const u32x4 a0 = __builtin_amdgcn_raw_buffer_load_b128(rs, kb + t * 4096, 0, SC1), a1 = __builtin_amdgcn_raw_buffer_load_b128(rs, kb + t * 4096 + 1024, 0, SC1);
          f32x16 s = {};
          s = mfma(asbf(a0), asbf(q0), s); s = mfma(asbf(a1), asbf(q1), s);
#pragma unroll
          for (int r = 0; r < 16; ++r) mx = fmaxf(mx, s[r]);
        }
        set_shift(xhalf_max(mx));
        BAR();
      }
      TS();
      if (w == 0 && lane == 0) __builtin_amdgcn_raw_buffer_store_b64((u32x2){(unsigned)tag, (unsigned)(tag >> 32)}, rs, WS_FLAGS + (((hop0 + 4) * 16 + m) * 16 + h) * 256, 0, SC1);
      if (rep == 0) dma_wq(wA, w, 84, 4);
      waitf(hop0 + 4, m, 16);
      TS();
      wo_phase(rep * 2 + 1, rep == 1);
      TS();
      if (rep == 0) { publish(5, m, h); dma_wo(woA, w, 4); }
    }
  }
  if (blockIdx.x == 0 && w == 0 && lane == 0) __builtin_amdgcn_raw_buffer_store_b64((u32x2){(unsigned)tag, (unsigned)(tag >> 32)}, rs, WS_EPOCH, 0, SC1);
  TS();
}

extern "C" void kernel_launch(void* const* d_in, const int* in_sizes, int n_in, void* d_out, int out_size, void* d_ws, size_t ws_size, hipStream_t stream) {
  fused_kernel<<<256, 256, 0, stream>>>((const u16*)d_in[0], (const u16*)d_in[1], (const u16*)d_in[2], (const u16*)d_in[4], (const u16*)d_in[5], (u16*)d_out, (char*)d_ws);
}
```

```cpp
#include <hip/hip_runtime.h>
#include <hip/hip_bf16.h>

__global__ void __launch_bounds__(256, 1)
fused_kernel(const void* x0, const void* wA, const void* woA, const void* wB, const void* woB, void* out, void* ws) {
  __shared__ __attribute__((aligned(16))) char lds_raw[163840];
  unsigned tid = threadIdx.x;
  const unsigned bid = blockIdx.x;
  const unsigned lds0 = (unsigned)(size_t)(__attribute__((address_space(3))) char*)lds_raw;
  const unsigned long long ka = (unsigned long long)__builtin_amdgcn_kernarg_segment_ptr();
  asm volatile(
      "s_load_dwordx8 s[40:47], s[94:95], 0\n"
      "s_load_dwordx4 s[48:51], s[94:95], 32\n"
      "s_load_dwordx2 s[52:53], s[94:95], 48\n"
      "v_lshrrev_b32 v16, 6, v0\n"
      "v_and_b32 v17, 63, v0\n"
      "v_readfirstlane_b32 s16, v16\n"
      "s_and_b32 s28, s54, 7\n"
      "s_lshr_b32 s29, s54, 3\n"
      "s_getpc_b64 s[30:31]\n"
      ".Lpc2_%=:\n"
      "s_and_b32 s32, s29, 7\n"
      "s_lshl_b32 s32, s32, 12\n"
      "v_lshlrev_b32 v21, 10, v16\n"
      "v_lshl_add_u32 v21, v17, 7, v21\n"
      "v_add_u32 v21, s32, v21\n"
      "v_min_u32 v21, .Lend1_%=-.Lpc2_%=-4, v21\n"
      "s_mov_b64 exec, 0xff\n"
      "global_load_dword v252, v21, s[30:31]\n"
      "s_mov_b64 exec, -1\n"
      "s_lshr_b32 s14, s28, 1\n"
      "s_lshl_b32 s14, s14, 2\n"
      "s_lshr_b32 s30, s29, 3\n"
      "s_add_u32 s14, s14, s30\n"
      "s_lshl_b32 s18, s14, 2\n"
      "s_add_u32 s18, s18, s16\n"
      "s_mul_i32 s19, s18, 28672\n"
      "v_lshrrev_b32 v21, 3, v17\n"
      "v_mul_u32_u24 v21, 896, v21\n"
      "v_lshrrev_b32 v22, 4, v17\n"
      "v_and_b32 v23, 7, v17\n"
      "v_xor_b32 v22, v22, v23\n"
      "v_lshl_add_u32 v248, v22, 4, v21\n"
      "v_xor_b32 v249, 64, v248\n"
      "v_add_u32 v249, 6144, v249\n"
      "v_add_u32 v250, 12288, v248\n"
      "v_add_u32 v251, 12288, v249\n"
      "s_lshl_b32 s35, s16, 14\n"
      "s_lshl_b32 s59, s16, 12\n"
      "s_add_u32 s59, s59, 122880\n"
      "s_and_b32 s82, s29, 7\n"
      "s_lshr_b32 s30, s29, 3\n"
      "s_mul_i32 s30, s30, 3\n"
      "s_add_u32 s82, s82, s30\n"
      "s_mul_i32 s30, s82, 37\n"
      "s_lshr_b32 s30, s30, 8\n"
      "s_mul_i32 s30, s30, 7\n"
      "s_sub_u32 s82, s82, s30\n"
      "s_waitcnt lgkmcnt(0)\n"
      "s_mov_b32 s8, s40\n"
      "s_and_b32 s9, s41, 0xffff\n"
      "s_mov_b32 s10, 1835008\n"
      "s_mov_b32 s11, 0x20000\n"
      "s_add_u32 s30, s35, 0\n"
      "s_mov_b32 m0, s30\n"
      "s_lshl_b32 s31, s82, 7\n"
      "s_add_u32 s31, s31, s19\n"
      "buffer_load_dwordx4 v248, s[8:11], s31 offen offset:0 sc1 lds\n"
      "buffer_load_dwordx4 v249, s[8:11], s31 offen offset:1024 sc1 lds\n"
      "buffer_load_dwordx4 v250, s[8:11], s31 offen offset:2048 sc1 lds\n"
      "buffer_load_dwordx4 v251, s[8:11], s31 offen offset:3072 sc1 lds\n"
      "s_add_u32 s83, s82, 1\n"
      "s_cmp_eq_u32 s83, 7\n"
      "s_cselect_b32 s83, 0, s83\n"
      "s_add_u32 s30, s35, 4096\n"
      "s_mov_b32 m0, s30\n"
      "s_lshl_b32 s31, s83, 7\n"
      "s_add_u32 s31, s31, s19\n"
      "buffer_load_dwordx4 v248, s[8:11], s31 offen offset:0 sc1 lds\n"
      "buffer_load_dwordx4 v249, s[8:11], s31 offen offset:1024 sc1 lds\n"
      "buffer_load_dwordx4 v250, s[8:11], s31 offen offset:2048 sc1 lds\n"
      "buffer_load_dwordx4 v251, s[8:11], s31 offen offset:3072 sc1 lds\n"
      "s_and_b32 s15, s28, 1\n"
      "s_lshl_b32 s15, s15, 3\n"
      "s_and_b32 s29, s29, 7\n"
      "s_add_u32 s15, s15, s29\n"
      "v_lshrrev_b32 v21, 2, v17\n"
      "v_and_b32 v22, 3, v17\n"
      "v_lshlrev_b32 v22, 4, v22\n"
      "s_mul_i32 s28, s15, 56\n"
      "v_mul_u32_u24 v87, 2688, v21\n"
      "v_add3_u32 v87, v87, v22, s28\n"
      "s_mov_b32 s4, s42\n"
      "s_and_b32 s5, s43, 0xffff\n"
      "s_mov_b32 s6, 1204224\n"
      "s_mov_b32 s7, 0x20000\n"
      "s_mul_i32 s32, s16, 43008\n"
      "s_lshl_b32 s33, s16, 10\n"
      "s_add_u32 s33, s33, 65536\n"
      "s_add_u32 s84, s83, 1\n"
      "s_cmp_eq_u32 s84, 7\n"
      "s_cselect_b32 s84, 0, s84\n"
      "s_add_u32 s30, s59, 0\n"
      "s_mov_b32 m0, s30\n"
      "s_lshl_b32 s31, s84, 7\n"
      "s_add_u32 s31, s31, s19\n"
      "buffer_load_dwordx4 v248, s[8:11], s31 offen offset:0 sc1 lds\n"
      "s_lshl_b32 s28, s15, 6\n"
      "v_mul_u32_u24 v90, 896, v21\n"
      "v_add3_u32 v90, v90, v22, s28\n"
      "v_and_b32 v18, 31, v17\n"
      "v_lshrrev_b32 v19, 5, v17\n"
      "v_lshlrev_b32 v0, 4, v17\n"
      "v_add_u32 v7, s33, v0\n"
      "v_and_b32 v21, 15, v17\n"
      "v_lshlrev_b32 v4, 8, v21\n"
      "v_and_b32 v20, 16, v17\n"
      "buffer_load_dwordx4 v249, s[8:11], s31 offen offset:1024 sc1 lds\n"
      "s_lshl_b32 s28, s16, 8\n"
      "s_add_u32 s28, s28, 159744\n"
      "v_lshl_add_u32 v5, v21, 1, v19\n"
      "v_lshlrev_b32 v5, 3, v5\n"
      "v_lshrrev_b32 v16, 2, v20\n"
      "v_add3_u32 v5, v5, v16, s28\n"
      "buffer_load_dwordx4 v250, s[8:11], s31 offen offset:2048 sc1 lds\n"
      "v_lshrrev_b32 v21, 2, v21\n"
      "v_lshl_add_u32 v21, v19, 2, v21\n"
      "v_lshlrev_b32 v21, 6, v21\n"
      "v_lshrrev_b32 v22, 1, v22\n"
      "v_lshl_add_u32 v22, v20, 1, v22\n"
      "v_add3_u32 v95, v21, v22, s33\n"
      "v_add3_u32 v91, v21, v22, s35\n"
      "v_lshl_add_u32 v21, v19, 8, v21\n"
      "v_add_u32 v21, 65536, v21\n"
      "v_add_u32 v3, v21, v22\n"
      "buffer_load_dwordx4 v251, s[8:11], s31 offen offset:3072 sc1 lds\n"
      "v_lshrrev_b32 v21, 3, v18\n"
      "v_lshlrev_b32 v21, 10, v21\n"
      "v_and_b32 v22, 7, v18\n"
      "v_lshl_add_u32 v21, v22, 7, v21\n"
      "v_lshrrev_b32 v22, 1, v18\n"
      "v_and_b32 v22, 7, v22\n"
      "v_xor_b32 v22, v22, v19\n"
      "s_add_u32 s30, s33, 0\n"
      "s_mov_b32 m0, s30\n"
      "s_mul_i32 s31, s82, 172032\n"
      "s_add_u32 s31, s31, s32\n"
      "s_add_u32 s31, s31, 896\n"
      "buffer_load_dwordx4 v87, s[4:7], s31 offen sc1 lds\n"
      "v_xor_b32 v23, 0, v22\n"
      "v_lshl_add_u32 v23, v23, 4, v21\n"
      "v_add_u32 v240, s59, v23\n"
      "v_add_u32 v244, s35, v23\n"
      "v_xor_b32 v23, 2, v22\n"
      "v_lshl_add_u32 v23, v23, 4, v21\n"
      "v_add_u32 v241, s59, v23\n"
      "v_add_u32 v245, s35, v23\n"
      "s_add_u32 s30, s33, 28672\n"
      "s_mov_b32 m0, s30\n"
      "s_mul_i32 s31, s82, 172032\n"
      "s_add_u32 s31, s31, s32\n"
      "s_add_u32 s31, s31, 1792\n"
      "buffer_load_dwordx4 v87, s[4:7], s31 offen sc1 lds\n"
      "v_xor_b32 v23, 4, v22\n"
      "v_lshl_add_u32 v23, v23, 4, v21\n"
      "v_add_u32 v242, s59, v23\n"
      "v_add_u32 v246, s35, v23\n"
      "v_xor_b32 v23, 6, v22\n"
      "v_lshl_add_u32 v23, v23, 4, v21\n"
      "v_add_u32 v243, s59, v23\n"
      "v_add_u32 v247, s35, v23\n"
      "s_add_u32 s85, s84, 1\n"
      "s_cmp_eq_u32 s85, 7\n"
      "s_cselect_b32 s85, 0, s85\n"
      "s_add_u32 s30, s35, 8192\n"
      "s_mov_b32 m0, s30\n"
      "s_lshl_b32 s31, s85, 7\n"
      "s_add_u32 s31, s31, s19\n"
      "buffer_load_dwordx4 v248, s[8:11], s31 offen offset:0 sc1 lds\n"
      "v_mov_b32 v255, 0x3f803f80\n"
      "v_mov_b64 v[84:85], 0\n"
      "v_accvgpr_write_b32 a192, v255\n"
      "v_accvgpr_write_b32 a193, v255\n"
      "v_accvgpr_write_b32 a194, v255\n"
      "v_accvgpr_write_b32 a195, v255\n"
      "v_add_u32 v1, 65536, v0\n"
      "v_add_u32 v2, 122880, v0\n"
      "v_lshlrev_b32 v6, 3, v18\n"
      "v_mov_b32 v11, 0xdddd\n"
      "v_mov_b64 v[82:83], 0\n"
      "buffer_load_dwordx4 v249, s[8:11], s31 offen offset:1024 sc1 lds\n"
      "s_lshl_b32 s28, s16, 7\n"
      "s_add_u32 s28, s28, 163328\n"
      "v_lshl_add_u32 v9, v18, 2, s28\n"
      "v_lshl_add_u32 v10, v19, 4, s28\n"
      "s_mul_i32 s28, s16, 384\n"
      "s_add_u32 s28, s28, 161792\n"
      "v_lshl_add_u32 v8, v18, 2, s28\n"
      "buffer_load_dwordx4 v250, s[8:11], s31 offen offset:2048 sc1 lds\n"
      "v_and_b32 v21, 4, v18\n"
      "v_lshl_add_u32 v21, v21, 3, v18\n"
      "v_lshlrev_b32 v21, 4, v21\n"
      "v_lshrrev_b32 v22, 3, v18\n"
      "v_lshl_add_u32 v21, v22, 10, v21\n"
      "v_and_b32 v22, 3, v18\n"
      "v_lshl_add_u32 v21, v22, 2, v21\n"
      "v_add_u32 v253, s35, v21\n"
      "v_add_u32 v254, s35, v0\n"
      "s_waitcnt vmcnt(13)\n"
      "ds_read_b128 v[128:131], v244 offset:0\n"
      "ds_read_b128 v[132:135], v245 offset:0\n"
      "ds_read_b128 v[136:139], v246 offset:0\n"
      "ds_read_b128 v[140:143], v247 offset:0\n"
      "s_mov_b32 s0, s52\n"
      "s_and_b32 s1, s53, 0xffff\n"
      "s_mov_b32 s2, 34144256\n"
      "s_mov_b32 s3, 0x20000\n"
      "s_mul_i32 s96, s14, 2101248\n"
      "s_mul_i32 s97, s15, 2101248\n"
      "s_add_u32 s25, s96, 1875968\n"
      "s_add_u32 s26, s97, 1875968\n"
      "s_add_u32 s72, s97, 1638400\n"
      "s_add_u32 s73, s97, 1810432\n"
      "s_lshr_b32 s36, s16, 1\n"
      "s_and_b32 s37, s16, 1\n"
      "s_lshl_b32 s38, s36, 4\n"
      "s_add_u32 s38, s38, s14\n"
      "s_lshl_b32 s39, s37, 4\n"
      "s_add_u32 s39, s39, s14\n"
      "s_mul_i32 s56, s38, 43008\n"
      "s_mul_i32 s28, s37, 896\n"
      "s_add_u32 s56, s56, s28\n"
      "s_add_u32 s56, s56, 896\n"
      "s_mul_i32 s57, s39, 43008\n"
      "s_cmp_lt_u32 s16, 2\n"
      "s_cselect_b32 s57, 2147483648, s57\n"
      "s_lshl_b32 s75, s38, 10\n"
      "s_mul_i32 s28, s37, 28672\n"
      "s_add_u32 s75, s75, s28\n"
      "s_add_u32 s75, s75, s72\n"
      "s_cmp_ge_u32 s38, 28\n"
      "s_cselect_b32 s75, 2147483648, s75\n"
      "s_lshl_b32 s98, s39, 10\n"
      "s_add_u32 s98, s98, 57344\n"
      "s_add_u32 s98, s98, s72\n"
      "s_cmp_ge_u32 s39, 28\n"
      "s_cselect_b32 s98, 2147483648, s98\n"
      "s_cmp_lt_u32 s16, 2\n"
      "s_cselect_b32 s98, 2147483648, s98\n"
      "s_lshl_b32 s28, s37, 15\n"
      "s_add_u32 s28, s28, s73\n"
      "s_lshl_b32 s29, s14, 1\n"
      "s_add_u32 s29, s29, s36\n"
      "s_lshl_b32 s29, s29, 10\n"
      "s_add_u32 s99, s28, s29\n"
      "s_waitcnt vmcnt(9)\n"
      "s_waitcnt lgkmcnt(3)\n"
      "v_mfma_f32_32x32x16_bf16 v[64:79], v[128:131], v[128:131], 0\n"
      "ds_read_b128 v[144:147], v244 offset:4096\n"
      "ds_read_b128 v[148:151], v245 offset:4096\n"
      "ds_read_b128 v[152:155], v246 offset:4096\n"
      "buffer_load_dwordx4 v251, s[8:11], s31 offen offset:3072 sc1 lds\n"
      "v_dot2c_f32_bf16 v84, v128, v255\n"
      "v_dot2c_f32_bf16 v85, v129, v255\n"
      "v_dot2c_f32_bf16 v84, v130, v255\n"
      "v_dot2c_f32_bf16 v85, v131, v255\n"
      "ds_read_b128 v[156:159], v247 offset:4096\n"
      "s_waitcnt lgkmcnt(6)\n"
      "v_mfma_f32_32x32x16_bf16 v[64:79], v[132:135], v[132:135], v[64:79]\n"
      "s_add_u32 s30, s33, 4096\n"
      "s_mov_b32 m0, s30\n"
      "s_mul_i32 s31, s83, 172032\n"
      "s_add_u32 s31, s31, s32\n"
      "s_add_u32 s31, s31, 896\n"
      "buffer_load_dwordx4 v87, s[4:7], s31 offen sc1 lds\n"
      "v_dot2c_f32_bf16 v84, v132, v255\n"
      "v_dot2c_f32_bf16 v85, v133, v255\n"
      "v_dot2c_f32_bf16 v84, v134, v255\n"
      "v_dot2c_f32_bf16 v85, v135, v255\n"
      "s_waitcnt lgkmcnt(5)\n"
      "v_mfma_f32_32x32x16_bf16 v[64:79], v[136:139], v[136:139], v[64:79]\n"
      "s_add_u32 s30, s33, 32768\n"
      "s_mov_b32 m0, s30\n"
      "s_mul_i32 s31, s83, 172032\n"
      "s_add_u32 s31, s31, s32\n"
      "s_add_u32 s31, s31, 1792\n"
      "buffer_load_dwordx4 v87, s[4:7], s31 offen sc1 lds\n"
      "v_dot2c_f32_bf16 v84, v136, v255\n"
      "v_dot2c_f32_bf16 v85, v137, v255\n"
      "v_dot2c_f32_bf16 v84, v138, v255\n"
      "v_dot2c_f32_bf16 v85, v139, v255\n"
      "s_waitcnt lgkmcnt(4)\n"
      "v_mfma_f32_32x32x16_bf16 v[64:79], v[140:143], v[140:143], v[64:79]\n"
      "s_add_u32 s86, s85, 1\n"
      "s_cmp_eq_u32 s86, 7\n"
      "s_cselect_b32 s86, 0, s86\n"
      "s_add_u32 s30, s35, 12288\n"
      "s_mov_b32 m0, s30\n"
      "s_lshl_b32 s31, s86, 7\n"
      "s_add_u32 s31, s31, s19\n"
      "buffer_load_dwordx4 v248, s[8:11], s31 offen offset:0 sc1 lds\n"
      "v_dot2c_f32_bf16 v84, v140, v255\n"
      "v_dot2c_f32_bf16 v85, v141, v255\n"
      "v_dot2c_f32_bf16 v84, v142, v255\n"
      "v_dot2c_f32_bf16 v85, v143, v255\n"
      "s_waitcnt vmcnt(9)\n"
      "s_waitcnt lgkmcnt(3)\n"
      "v_mfma_f32_32x32x16_bf16 v[64:79], v[144:147], v[144:147], v[64:79]\n"
      "ds_read_b128 v[160:163], v240 offset:0\n"
      "ds_read_b128 v[164:167], v241 offset:0\n"
      "ds_read_b128 v[168:171], v242 offset:0\n"
      "buffer_load_dwordx4 v249, s[8:11], s31 offen offset:1024 sc1 lds\n"
      "v_dot2c_f32_bf16 v84, v144, v255\n"
      "v_dot2c_f32_bf16 v85, v145, v255\n"
      "v_dot2c_f32_bf16 v84, v146, v255\n"
      "v_dot2c_f32_bf16 v85, v147, v255\n"
      "ds_read_b128 v[172:175], v243 offset:0\n"
      "s_waitcnt lgkmcnt(6)\n"
      "v_mfma_f32_32x32x16_bf16 v[64:79], v[148:151], v[148:151], v[64:79]\n"
      "buffer_load_dwordx4 v250, s[8:11], s31 offen offset:2048 sc1 lds\n"
      "v_dot2c_f32_bf16 v84, v148, v255\n"
      "v_dot2c_f32_bf16 v85, v149, v255\n"
      "v_dot2c_f32_bf16 v84, v150, v255\n"
      "v_dot2c_f32_bf16 v85, v151, v255\n"
      "s_waitcnt lgkmcnt(5)\n"
      "v_mfma_f32_32x32x16_bf16 v[64:79], v[152:155], v[152:155], v[64:79]\n"
      "buffer_load_dwordx4 v251, s[8:11], s31 offen offset:3072 sc1 lds\n"
      "v_dot2c_f32_bf16 v84, v152, v255\n"
      "v_dot2c_f32_bf16 v85, v153, v255\n"
      "v_dot2c_f32_bf16 v84, v154, v255\n"
      "v_dot2c_f32_bf16 v85, v155, v255\n"
      "s_waitcnt lgkmcnt(4)\n"
      "v_mfma_f32_32x32x16_bf16 v[64:79], v[156:159], v[156:159], v[64:79]\n"
      "s_add_u32 s30, s33, 8192\n"
      "s_mov_b32 m0, s30\n"
      "s_mul_i32 s31, s84, 172032\n"
      "s_add_u32 s31, s31, s32\n"
      "s_add_u32 s31, s31, 896\n"
      "buffer_load_dwordx4 v87, s[4:7], s31 offen sc1 lds\n"
      "v_dot2c_f32_bf16 v84, v156, v255\n"
      "v_dot2c_f32_bf16 v85, v157, v255\n"
      "v_dot2c_f32_bf16 v84, v158, v255\n"
      "v_dot2c_f32_bf16 v85, v159, v255\n"
      "s_waitcnt vmcnt(11)\n"
      "s_barrier\n"
      "s_waitcnt lgkmcnt(3)\n"
      "v_mfma_f32_32x32x16_bf16 v[64:79], v[160:163], v[160:163], v[64:79]\n"
      "ds_read_b64_tr_b16 a[0:1], v95 offset:0\n"
      "ds_read_b64_tr_b16 a[2:3], v95 offset:512\n"
      "ds_read_b64_tr_b16 a[28:29], v95 offset:28672\n"
      "s_add_u32 s30, s33, 36864\n"
      "s_mov_b32 m0, s30\n"
      "s_mul_i32 s31, s84, 172032\n"
      "s_add_u32 s31, s31, s32\n"
      "s_add_u32 s31, s31, 1792\n"
      "buffer_load_dwordx4 v87, s[4:7], s31 offen sc1 lds\n"
      "v_dot2c_f32_bf16 v84, v160, v255\n"
      "v_dot2c_f32_bf16 v85, v161, v255\n"
      "v_dot2c_f32_bf16 v84, v162, v255\n"
      "v_dot2c_f32_bf16 v85, v163, v255\n"
      "ds_read_b64_tr_b16 a[30:31], v95 offset:29184\n"
      "ds_read_b64_tr_b16 v[96:97], v3 offset:0\n"
      "ds_read_b64_tr_b16 v[98:99], v3 offset:256\n"
      "s_waitcnt lgkmcnt(8)\n"
      "v_mfma_f32_32x32x16_bf16 v[64:79], v[164:167], v[164:167], v[64:79]\n"
      "ds_read_b64_tr_b16 v[100:101], v3 offset:28672\n"
      "ds_read_b64_tr_b16 v[102:103], v3 offset:28928\n"
      "ds_read_b64_tr_b16 v[104:105], v3 offset:1024\n"
      "v_dot2c_f32_bf16 v84, v164, v255\n"
      "v_dot2c_f32_bf16 v85, v165, v255\n"
      "v_dot2c_f32_bf16 v84, v166, v255\n"
      "v_dot2c_f32_bf16 v85, v167, v255\n"
      "ds_read_b64_tr_b16 v[106:107], v3 offset:1280\n"
      "ds_read_b64_tr_b16 v[108:109], v3 offset:29696\n"
      "ds_read_b64_tr_b16 v[110:111], v3 offset:29952\n"
      "s_waitcnt lgkmcnt(13)\n"
      "v_mfma_f32_32x32x16_bf16 v[64:79], v[168:171], v[168:171], v[64:79]\n"
      "ds_read_b64_tr_b16 v[112:113], v3 offset:2048\n"
      "ds_read_b64_tr_b16 v[114:115], v3 offset:2304\n"
      "ds_read_b64_tr_b16 v[116:117], v3 offset:30720\n"
      "s_add_u32 s87, s86, 1\n"
      "s_cmp_eq_u32 s87, 7\n"
      "s_cselect_b32 s87, 0, s87\n"
      "s_add_u32 s30, s35, 0\n"
      "s_mov_b32 m0, s30\n"
      "s_lshl_b32 s31, s87, 7\n"
      "s_add_u32 s31, s31, s19\n"
      "buffer_load_dwordx4 v248, s[8:11], s31 offen offset:0 sc1 lds\n"
      "v_dot2c_f32_bf16 v84, v168, v255\n"
      "v_dot2c_f32_bf16 v85, v169, v255\n"
      "v_dot2c_f32_bf16 v84, v170, v255\n"
      "v_dot2c_f32_bf16 v85, v171, v255\n"
      "ds_read_b64_tr_b16 v[118:119], v3 offset:30976\n"
      "ds_read_b64_tr_b16 v[120:121], v3 offset:3072\n"
      "ds_read_b64_tr_b16 v[122:123], v3 offset:3328\n"
      "s_waitcnt lgkmcnt(15)\n"
      "v_mfma_f32_32x32x16_bf16 v[64:79], v[172:175], v[172:175], v[64:79]\n"
      "ds_read_b64_tr_b16 v[124:125], v3 offset:31744\n"
      "ds_read_b64_tr_b16 v[126:127], v3 offset:32000\n"
      "v_dot2c_f32_bf16 v84, v172, v255\n"
      "v_dot2c_f32_bf16 v85, v173, v255\n"
      "v_dot2c_f32_bf16 v84, v174, v255\n"
      "v_dot2c_f32_bf16 v85, v175, v255\n"
      "s_waitcnt vmcnt(9)\n"
      "s_waitcnt lgkmcnt(15)\n"
      "v_mfma_f32_32x32x16_bf16 a[208:223], a[192:195], a[0:3], 0\n"
      "ds_read_b128 v[176:179], v244 offset:8192\n"
      "ds_read_b128 v[180:183], v245 offset:8192\n"
      "s_mul_i32 s58, s14, 28\n"
      "s_lshl_b32 s28, s36, 4\n"
      "s_add_u32 s58, s58, s28\n"
      "s_mul_i32 s58, s58, 896\n"
      "v_mfma_f32_32x32x16_bf16 a[224:239], a[192:195], a[28:31], 0\n"
      "ds_read_b128 v[184:187], v246 offset:8192\n"
      "ds_read_b128 v[188:191], v247 offset:8192\n"
      "s_lshl_b32 s28, s16, 10\n"
      "s_add_u32 s72, s72, s28\n"
      "s_lshl_b32 s29, s16, 13\n"
      "s_add_u32 s73, s73, s29\n"
      "s_waitcnt lgkmcnt(15)\n"
      "v_mfma_f32_32x32x16_bf16 v[16:31], v[128:131], v[96:99], 0\n"
      "buffer_load_dwordx4 v249, s[8:11], s31 offen offset:1024 sc1 lds\n"
      "v_mfma_f32_32x32x16_bf16 v[32:47], v[128:131], v[100:103], 0\n"
      "v_permlane32_swap_b32 v128, v130\n"
      "v_permlane32_swap_b32 v129, v131\n"
      "s_mov_b32 s60, 0x3f800000\n"
      "s_mov_b32 s61, 0x3f800000\n"
      "s_waitcnt lgkmcnt(12)\n"
      "v_mfma_f32_32x32x16_bf16 v[16:31], v[132:135], v[104:107], v[16:31]\n"
      "buffer_load_dwordx4 v250, s[8:11], s31 offen offset:2048 sc1 lds\n"
      "v_mfma_f32_32x32x16_bf16 v[32:47], v[132:135], v[108:111], v[32:47]\n"
      "v_permlane32_swap_b32 v132, v134\n"
      "v_permlane32_swap_b32 v133, v135\n"
      "s_mov_b32 s62, 0x3fb8aa3b\n"
      "s_mov_b32 s63, 0x3fb8aa3b\n"
      "s_waitcnt lgkmcnt(8)\n"
      "v_mfma_f32_32x32x16_bf16 v[16:31], v[136:139], v[112:115], v[16:31]\n"
      "buffer_load_dwordx4 v251, s[8:11], s31 offen offset:3072 sc1 lds\n"
      "v_mfma_f32_32x32x16_bf16 v[32:47], v[136:139], v[116:119], v[32:47]\n"
      "v_permlane32_swap_b32 v136, v138\n"
      "v_permlane32_swap_b32 v137, v139\n"
      "s_mov_b32 s64, 0x358637bd\n"
      "s_mov_b32 s65, 0x358637bd\n"
      "s_waitcnt lgkmcnt(4)\n"
      "v_mfma_f32_32x32x16_bf16 v[16:31], v[140:143], v[120:123], v[16:31]\n"
      "s_add_u32 s30, s33, 12288\n"
      "s_mov_b32 m0, s30\n"
      "s_mul_i32 s31, s85, 172032\n"
      "s_add_u32 s31, s31, s32\n"
      "s_add_u32 s31, s31, 896\n"
      "buffer_load_dwordx4 v87, s[4:7], s31 offen sc1 lds\n"
      "v_mfma_f32_32x32x16_bf16 v[32:47], v[140:143], v[124:127], v[32:47]\n"
      "v_permlane32_swap_b32 v140, v142\n"
      "v_permlane32_swap_b32 v141, v143\n"
      "s_mov_b32 s66, 0x0\n"
      "s_mov_b32 s67, 0x0\n"
      "s_waitcnt vmcnt(11)\n"
      "s_barrier\n"
      "s_waitcnt lgkmcnt(3)\n"
      "v_mfma_f32_32x32x16_bf16 v[64:79], v[176:179], v[176:179], v[64:79]\n"
      "ds_read_b64_tr_b16 a[4:5], v95 offset:4096\n"
      "ds_read_b64_tr_b16 a[6:7], v95 offset:4608\n"
      "ds_read_b64_tr_b16 a[32:33], v95 offset:32768\n"
      "s_add_u32 s30, s33, 40960\n"
      "s_mov_b32 m0, s30\n"
      "s_mul_i32 s31, s85, 172032\n"
      "s_add_u32 s31, s31, s32\n"
      "s_add_u32 s31, s31, 1792\n"
      "buffer_load_dwordx4 v87, s[4:7], s31 offen sc1 lds\n"
      "v_dot2c_f32_bf16 v84, v176, v255\n"
      "v_dot2c_f32_bf16 v85, v177, v255\n"
      "v_dot2c_f32_bf16 v84, v178, v255\n"
      "v_dot2c_f32_bf16 v85, v179, v255\n"
      "ds_read_b64_tr_b16 a[34:35], v95 offset:33280\n"
      "ds_read_b64_tr_b16 a[84:85], v3 offset:4096\n"
      "ds_read_b64_tr_b16 a[86:87], v3 offset:4352\n"
      "s_waitcnt lgkmcnt(8)\n"
      "v_mfma_f32_32x32x16_bf16 v[64:79], v[180:183], v[180:183], v[64:79]\n"
      "ds_read_b64_tr_b16 a[88:89], v3 offset:32768\n"
      "ds_read_b64_tr_b16 a[90:91], v3 offset:33024\n"
      "ds_read_b64_tr_b16 a[92:93], v3 offset:5120\n"
      "v_dot2c_f32_bf16 v84, v180, v255\n"
      "v_dot2c_f32_bf16 v85, v181, v255\n"
      "v_dot2c_f32_bf16 v84, v182, v255\n"
      "v_dot2c_f32_bf16 v85, v183, v255\n"
      "ds_read_b64_tr_b16 a[94:95], v3 offset:5376\n"
      "ds_read_b64_tr_b16 a[96:97], v3 offset:33792\n"
      "ds_read_b64_tr_b16 a[98:99], v3 offset:34048\n"
      "s_waitcnt lgkmcnt(13)\n"
      "v_mfma_f32_32x32x16_bf16 v[64:79], v[184:187], v[184:187], v[64:79]\n"
      "ds_read_b64_tr_b16 a[100:101], v3 offset:6144\n"
      "ds_read_b64_tr_b16 a[102:103], v3 offset:6400\n"
      "ds_read_b64_tr_b16 a[104:105], v3 offset:34816\n"
      "s_add_u32 s88, s87, 1\n"
      "s_cmp_eq_u32 s88, 7\n"
      "s_cselect_b32 s88, 0, s88\n"
      "s_add_u32 s30, s35, 4096\n"
      "s_mov_b32 m0, s30\n"
      "s_lshl_b32 s31, s88, 7\n"
      "s_add_u32 s31, s31, s19\n"
      "buffer_load_dwordx4 v248, s[8:11], s31 offen offset:0 sc1 lds\n"
      "v_dot2c_f32_bf16 v84, v184, v255\n"
      "v_dot2c_f32_bf16 v85, v185, v255\n"
      "v_dot2c_f32_bf16 v84, v186, v255\n"
      "v_dot2c_f32_bf16 v85, v187, v255\n"
      "ds_read_b64_tr_b16 a[106:107], v3 offset:35072\n"
      "ds_read_b64_tr_b16 a[108:109], v3 offset:7168\n"
      "ds_read_b64_tr_b16 a[110:111], v3 offset:7424\n"
      "s_waitcnt lgkmcnt(15)\n"
      "v_mfma_f32_32x32x16_bf16 v[64:79], v[188:191], v[188:191], v[64:79]\n"
      "ds_read_b64_tr_b16 a[112:113], v3 offset:35840\n"
      "ds_read_b64_tr_b16 a[114:115], v3 offset:36096\n"
      "v_dot2c_f32_bf16 v84, v188, v255\n"
      "v_dot2c_f32_bf16 v85, v189, v255\n"
      "v_dot2c_f32_bf16 v84, v190, v255\n"
      "v_dot2c_f32_bf16 v85, v191, v255\n"
      "s_waitcnt vmcnt(9)\n"
      "s_waitcnt lgkmcnt(15)\n"
      "v_mfma_f32_32x32x16_bf16 a[208:223], a[192:195], a[4:7], a[208:223]\n"
      "ds_read_b128 v[192:195], v244 offset:12288\n"
      "ds_read_b128 v[196:199], v245 offset:12288\n"
      "s_mul_i32 s20, s16, 28672\n"
      "s_add_u32 s20, s20, s96\n"
      "s_add_u32 s20, s20, 0\n"
      "v_mfma_f32_32x32x16_bf16 a[224:239], a[192:195], a[32:35], a[224:239]\n"
      "ds_read_b128 v[200:203], v246 offset:12288\n"
      "ds_read_b128 v[204:207], v247 offset:12288\n"
      "s_mul_i32 s21, s16, 4096\n"
      "s_add_u32 s21, s21, s96\n"
      "s_add_u32 s21, s21, 983040\n"
      "s_waitcnt lgkmcnt(15)\n"
      "v_mfma_f32_32x32x16_bf16 v[16:31], v[144:147], a[84:87], v[16:31]\n"
      "buffer_load_dwordx4 v249, s[8:11], s31 offen offset:1024 sc1 lds\n"
      "v_mfma_f32_32x32x16_bf16 v[32:47], v[144:147], a[88:91], v[32:47]\n"
      "v_permlane32_swap_b32 v144, v146\n"
      "v_permlane32_swap_b32 v145, v147\n"
      "s_mul_i32 s22, s16, 32768\n"
      "s_add_u32 s22, s22, s96\n"
      "s_add_u32 s22, s22, 458752\n"
      "s_waitcnt lgkmcnt(12)\n"
      "v_mfma_f32_32x32x16_bf16 v[16:31], v[148:151], a[92:95], v[16:31]\n"
      "buffer_load_dwordx4 v250, s[8:11], s31 offen offset:2048 sc1 lds\n"
      "v_mfma_f32_32x32x16_bf16 v[32:47], v[148:151], a[96:99], v[32:47]\n"
      "v_permlane32_swap_b32 v148, v150\n"
      "v_permlane32_swap_b32 v149, v151\n"
      "s_mul_i32 s23, s16, 8192\n"
      "s_add_u32 s23, s23, s97\n"
      "s_add_u32 s23, s23, 1572864\n"
      "s_waitcnt lgkmcnt(8)\n"
      "v_mfma_f32_32x32x16_bf16 v[16:31], v[152:155], a[100:103], v[16:31]\n"
      "buffer_load_dwordx4 v251, s[8:11], s31 offen offset:3072 sc1 lds\n"
      "v_mfma_f32_32x32x16_bf16 v[32:47], v[152:155], a[104:107], v[32:47]\n"
      "v_permlane32_swap_b32 v152, v154\n"
      "v_permlane32_swap_b32 v153, v155\n"
      "s_mul_i32 s24, s16, 65536\n"
      "s_add_u32 s24, s24, s97\n"
      "s_add_u32 s24, s24, 1048576\n"
      "s_waitcnt lgkmcnt(4)\n"
      "v_mfma_f32_32x32x16_bf16 v[16:31], v[156:159], a[108:111], v[16:31]\n"
      "s_add_u32 s30, s33, 16384\n"
      "s_mov_b32 m0, s30\n"
      "s_mul_i32 s31, s86, 172032\n"
      "s_add_u32 s31, s31, s32\n"
      "s_add_u32 s31, s31, 896\n"
      "buffer_load_dwordx4 v87, s[4:7], s31 offen sc1 lds\n"
      "v_mfma_f32_32x32x16_bf16 v[32:47], v[156:159], a[112:115], v[32:47]\n"
      "v_permlane32_swap_b32 v156, v158\n"
      "v_permlane32_swap_b32 v157, v159\n"
      "s_mov_b32 s27, 4096\n"
      "s_mov_b32 s17, 0\n"
      "s_waitcnt vmcnt(11)\n"
      "s_barrier\n"
      "s_waitcnt lgkmcnt(3)\n"
      "v_mfma_f32_32x32x16_bf16 v[64:79], v[192:195], v[192:195], v[64:79]\n"
      "ds_read_b64_tr_b16 a[8:9], v95 offset:8192\n"
      "ds_read_b64_tr_b16 a[10:11], v95 offset:8704\n"
      "ds_read_b64_tr_b16 a[36:37], v95 offset:36864\n"
      "s_add_u32 s30, s33, 45056\n"
      "s_mov_b32 m0, s30\n"
      "s_mul_i32 s31, s86, 172032\n"
      "s_add_u32 s31, s31, s32\n"
      "s_add_u32 s31, s31, 1792\n"
      "buffer_load_dwordx4 v87, s[4:7], s31 offen sc1 lds\n"
      "v_dot2c_f32_bf16 v84, v192, v255\n"
      "v_dot2c_f32_bf16 v85, v193, v255\n"
      "v_dot2c_f32_bf16 v84, v194, v255\n"
      "v_dot2c_f32_bf16 v85, v195, v255\n"
      "ds_read_b64_tr_b16 a[38:39], v95 offset:37376\n"
      "ds_read_b64_tr_b16 v[96:97], v3 offset:8192\n"
      "ds_read_b64_tr_b16 v[98:99], v3 offset:8448\n"
      "s_waitcnt lgkmcnt(8)\n"
      "v_mfma_f32_32x32x16_bf16 v[64:79], v[196:199], v[196:199], v[64:79]\n"
      "ds_read_b64_tr_b16 v[100:101], v3 offset:36864\n"
      "ds_read_b64_tr_b16 v[102:103], v3 offset:37120\n"
      "ds_read_b64_tr_b16 v[104:105], v3 offset:9216\n"
      "v_dot2c_f32_bf16 v84, v196, v255\n"
      "v_dot2c_f32_bf16 v85, v197, v255\n"
      "v_dot2c_f32_bf16 v84, v198, v255\n"
      "v_dot2c_f32_bf16 v85, v199, v255\n"
      "ds_read_b64_tr_b16 v[106:107], v3 offset:9472\n"
      "ds_read_b64_tr_b16 v[108:109], v3 offset:37888\n"
      "ds_read_b64_tr_b16 v[110:111], v3 offset:38144\n"
      "s_waitcnt lgkmcnt(13)\n"
      "v_mfma_f32_32x32x16_bf16 v[64:79], v[200:203], v[200:203], v[64:79]\n"
      "ds_read_b64_tr_b16 v[112:113], v3 offset:10240\n"
      "ds_read_b64_tr_b16 v[114:115], v3 offset:10496\n"
      "ds_read_b64_tr_b16 v[116:117], v3 offset:38912\n"
      "s_add_u32 s30, s33, 20480\n"
      "s_mov_b32 m0, s30\n"
      "s_mul_i32 s31, s87, 172032\n"
      "s_add_u32 s31, s31, s32\n"
      "s_add_u32 s31, s31, 896\n"
      "buffer_load_dwordx4 v87, s[4:7], s31 offen sc1 lds\n"
      "v_dot2c_f32_bf16 v84, v200, v255\n"
      "v_dot2c_f32_bf16 v85, v201, v255\n"
      "v_dot2c_f32_bf16 v84, v202, v255\n"
      "v_dot2c_f32_bf16 v85, v203, v255\n"
      "ds_read_b64_tr_b16 v[118:119], v3 offset:39168\n"
      "ds_read_b64_tr_b16 v[120:121], v3 offset:11264\n"
      "ds_read_b64_tr_b16 v[122:123], v3 offset:11520\n"
      "s_waitcnt lgkmcnt(15)\n"
      "v_mfma_f32_32x32x16_bf16 v[64:79], v[204:207], v[204:207], v[64:79]\n"
      "ds_read_b64_tr_b16 v[124:125], v3 offset:39936\n"
      "ds_read_b64_tr_b16 v[126:127], v3 offset:40192\n"
      "v_dot2c_f32_bf16 v84, v204, v255\n"
      "v_dot2c_f32_bf16 v85, v205, v255\n"
      "v_dot2c_f32_bf16 v84, v206, v255\n"
      "v_dot2c_f32_bf16 v85, v207, v255\n"
      "s_waitcnt vmcnt(9)\n"
      "s_waitcnt lgkmcnt(15)\n"
      "v_mfma_f32_32x32x16_bf16 a[208:223], a[192:195], a[8:11], a[208:223]\n"
      "ds_read_b128 v[208:211], v244 offset:0\n"
      "ds_read_b128 v[212:215], v245 offset:0\n"
      "v_accvgpr_write_b32 a168, 0\n"
      "v_accvgpr_write_b32 a169, 0\n"
      "v_accvgpr_write_b32 a170, 0\n"
      "v_accvgpr_write_b32 a171, 0\n"
      "v_mfma_f32_32x32x16_bf16 a[224:239], a[192:195], a[36:39], a[224:239]\n"
      "ds_read_b128 v[216:219], v246 offset:0\n"
      "ds_read_b128 v[220:223], v247 offset:0\n"
      "v_accvgpr_write_b32 a172, 0\n"
      "v_accvgpr_write_b32 a173, 0\n"
      "v_accvgpr_write_b32 a174, 0\n"
      "v_accvgpr_write_b32 a175, 0\n"
      "s_waitcnt lgkmcnt(15)\n"
      "v_mfma_f32_32x32x16_bf16 v[16:31], v[160:163], v[96:99], v[16:31]\n"
      "s_add_u32 s30, s33, 49152\n"
      "s_mov_b32 m0, s30\n"
      "s_mul_i32 s31, s87, 172032\n"
      "s_add_u32 s31, s31, s32\n"
      "s_add_u32 s31, s31, 1792\n"
      "buffer_load_dwordx4 v87, s[4:7], s31 offen sc1 lds\n"
      "v_mfma_f32_32x32x16_bf16 v[32:47], v[160:163], v[100:103], v[32:47]\n"
      "v_permlane32_swap_b32 v160, v162\n"
      "v_permlane32_swap_b32 v161, v163\n"
      "v_accvgpr_write_b32 a176, 0\n"
      "v_accvgpr_write_b32 a177, 0\n"
      "v_accvgpr_write_b32 a178, 0\n"
      "v_accvgpr_write_b32 a179, 0\n"
      "s_waitcnt lgkmcnt(12)\n"
      "v_mfma_f32_32x32x16_bf16 v[16:31], v[164:167], v[104:107], v[16:31]\n"
      "s_add_u32 s30, s33, 24576\n"
      "s_mov_b32 m0, s30\n"
      "s_mul_i32 s31, s88, 172032\n"
      "s_add_u32 s31, s31, s32\n"
      "s_add_u32 s31, s31, 896\n"
      "buffer_load_dwordx4 v87, s[4:7], s31 offen sc1 lds\n"
      "v_mfma_f32_32x32x16_bf16 v[32:47], v[164:167], v[108:111], v[32:47]\n"
      "v_permlane32_swap_b32 v164, v166\n"
      "v_permlane32_swap_b32 v165, v167\n"
      "v_accvgpr_write_b32 a180, 0\n"
      "v_accvgpr_write_b32 a181, 0\n"
      "v_accvgpr_write_b32 a182, 0\n"
      "v_accvgpr_write_b32 a183, 0\n"
      "s_waitcnt lgkmcnt(8)\n"
      "v_mfma_f32_32x32x16_bf16 v[16:31], v[168:171], v[112:115], v[16:31]\n"
      "s_add_u32 s30, s33, 53248\n"
      "s_mov_b32 m0, s30\n"
      "s_mul_i32 s31, s88, 172032\n"
      "s_add_u32 s31, s31, s32\n"
      "s_add_u32 s31, s31, 1792\n"
      "buffer_load_dwordx4 v87, s[4:7], s31 offen sc1 lds\n"
      "v_mfma_f32_32x32x16_bf16 v[32:47], v[168:171], v[116:119], v[32:47]\n"
      "v_permlane32_swap_b32 v168, v170\n"
      "v_permlane32_swap_b32 v169, v171\n"
      "v_accvgpr_write_b32 a184, 0\n"
      "v_accvgpr_write_b32 a185, 0\n"
      "v_accvgpr_write_b32 a186, 0\n"
      "v_accvgpr_write_b32 a187, 0\n"
      "s_waitcnt lgkmcnt(4)\n"
      "v_mfma_f32_32x32x16_bf16 v[16:31], v[172:175], v[120:123], v[16:31]\n"
      "v_mfma_f32_32x32x16_bf16 v[32:47], v[172:175], v[124:127], v[32:47]\n"
      "v_permlane32_swap_b32 v172, v174\n"
      "v_permlane32_swap_b32 v173, v175\n"
      "v_accvgpr_write_b32 a188, 0\n"
      "v_accvgpr_write_b32 a189, 0\n"
      "v_accvgpr_write_b32 a190, 0\n"
      "v_accvgpr_write_b32 a191, 0\n"
      "s_waitcnt vmcnt(10)\n"
      "s_barrier\n"
      "s_waitcnt lgkmcnt(3)\n"
      "v_mfma_f32_32x32x16_bf16 v[64:79], v[208:211], v[208:211], v[64:79]\n"
      "ds_read_b64_tr_b16 a[12:13], v95 offset:12288\n"
      "ds_read_b64_tr_b16 a[14:15], v95 offset:12800\n"
      "ds_read_b64_tr_b16 a[40:41], v95 offset:40960\n"
      "v_dot2c_f32_bf16 v84, v208, v255\n"
      "v_dot2c_f32_bf16 v85, v209, v255\n"
      "v_dot2c_f32_bf16 v84, v210, v255\n"
      "v_dot2c_f32_bf16 v85, v211, v255\n"
      "ds_read_b64_tr_b16 a[42:43], v95 offset:41472\n"
      "ds_read_b64_tr_b16 a[84:85], v3 offset:12288\n"
      "ds_read_b64_tr_b16 a[86:87], v3 offset:12544\n"
      "s_waitcnt lgkmcnt(8)\n"
      "v_mfma_f32_32x32x16_bf16 v[64:79], v[212:215], v[212:215], v[64:79]\n"
      "ds_read_b64_tr_b16 a[88:89], v3 offset:40960\n"
      "ds_read_b64_tr_b16 a[90:91], v3 offset:41216\n"
      "ds_read_b64_tr_b16 a[92:93], v3 offset:13312\n"
      "v_dot2c_f32_bf16 v84, v212, v255\n"
      "v_dot2c_f32_bf16 v85, v213, v255\n"
      "v_dot2c_f32_bf16 v84, v214, v255\n"
      "v_dot2c_f32_bf16 v85, v215, v255\n"
      "ds_read_b64_tr_b16 a[94:95], v3 offset:13568\n"
      "ds_read_b64_tr_b16 a[96:97], v3 offset:41984\n"
      "ds_read_b64_tr_b16 a[98:99], v3 offset:42240\n"
      "s_waitcnt lgkmcnt(13)\n"
      "v_mfma_f32_32x32x16_bf16 v[64:79], v[216:219], v[216:219], v[64:79]\n"
      "ds_read_b64_tr_b16 a[100:101], v3 offset:14336\n"
      "ds_read_b64_tr_b16 a[102:103], v3 offset:14592\n"
      "ds_read_b64_tr_b16 a[104:105], v3 offset:43008\n"
      "v_dot2c_f32_bf16 v84, v216, v255\n"
      "v_dot2c_f32_bf16 v85, v217, v255\n"
      "v_dot2c_f32_bf16 v84, v218, v255\n"
      "v_dot2c_f32_bf16 v85, v219, v255\n"
      "ds_read_b64_tr_b16 a[106:107], v3 offset:43264\n"
      "ds_read_b64_tr_b16 a[108:109], v3 offset:15360\n"
      "ds_read_b64_tr_b16 a[110:111], v3 offset:15616\n"
      "s_waitcnt lgkmcnt(15)\n"
      "v_mfma_f32_32x32x16_bf16 v[64:79], v[220:223], v[220:223], v[64:79]\n"
      "ds_read_b64_tr_b16 a[112:113], v3 offset:44032\n"
      "ds_read_b64_tr_b16 a[114:115], v3 offset:44288\n"
      "v_dot2c_f32_bf16 v84, v220, v255\n"
      "v_dot2c_f32_bf16 v85, v221, v255\n"
      "v_dot2c_f32_bf16 v84, v222, v255\n"
      "v_dot2c_f32_bf16 v85, v223, v255\n"
      "s_waitcnt vmcnt(6)\n"
      "s_waitcnt lgkmcnt(15)\n"
      "v_mfma_f32_32x32x16_bf16 a[208:223], a[192:195], a[12:15], a[208:223]\n"
      "ds_read_b128 v[224:227], v244 offset:4096\n"
      "ds_read_b128 v[228:231], v245 offset:4096\n"
      "v_cmp_eq_u32 vcc, 224, v6\n"
      "v_mov_b32 v57, 0x3f803f80\n"
      "s_nop 0 ; hz\n"
      "v_cndmask_b32 v56, 0, v57, vcc\n"
      "v_mov_b32 v57, v56\n"
      "v_mfma_f32_32x32x16_bf16 a[224:239], a[192:195], a[40:43], a[224:239]\n"
      "ds_read_b128 v[232:235], v246 offset:4096\n"
      "ds_read_b128 v[236:239], v247 offset:4096\n"
      "v_mov_b64 v[58:59], v[56:57]\n"
      "v_mov_b64 v[60:61], v[56:57]\n"
      "v_mov_b64 v[62:63], v[56:57]\n"
      "s_nop 0\n"
      "s_waitcnt lgkmcnt(15)\n"
      "v_mfma_f32_32x32x16_bf16 v[16:31], v[176:179], a[84:87], v[16:31]\n"
      "s_mov_b32 s74, 1929216\n"
      "buffer_load_dwordx2 v[12:13], off, s[0:3], s74 sc1\n"
      "s_lshl_b32 s30, s15, 8\n"
      "s_add_u32 s30, s30, s25\n"
      "s_lshl_b32 s31, s14, 8\n"
      "s_add_u32 s31, s31, s26\n"
      "s_cmp_lg_u32 s37, 0\n"
      "s_cselect_b32 s30, s31, s30\n"
      "s_cmp_lt_u32 s16, 2\n"
      "s_cselect_b32 s30, 2147483648, s30\n"
      "v_lshlrev_b32 v248, 8, v0\n"
      "s_mov_b64 exec, 0x1fff\n"
      "buffer_load_dword v248, v248, s[0:3], s30 offen sc1\n"
      "s_mov_b64 exec, -1\n"
      "v_mfma_f32_32x32x16_bf16 v[32:47], v[176:179], a[88:91], v[32:47]\n"
      "v_permlane32_swap_b32 v176, v178\n"
      "v_permlane32_swap_b32 v177, v179\n"
      "s_lshl_b32 s28, s16, 12\n"
      "s_add_u32 s28, s28, 65536\n"
      "v_add_u32 v92, s28, v0\n"
      "s_lshl_b32 s28, s16, 10\n"
      "s_waitcnt lgkmcnt(12)\n"
      "v_mfma_f32_32x32x16_bf16 v[16:31], v[180:183], a[92:95], v[16:31]\n"
      "s_add_u32 s30, s33, 57344\n"
      "s_mov_b32 m0, s30\n"
      "s_mul_i32 s31, s82, 172032\n"
      "s_add_u32 s31, s31, s32\n"
      "s_add_u32 s31, s31, 0\n"
      "buffer_load_dwordx4 v87, s[4:7], s31 offen sc1 lds\n"
      "v_mfma_f32_32x32x16_bf16 v[32:47], v[180:183], a[96:99], v[32:47]\n"
      "v_permlane32_swap_b32 v180, v182\n"
      "v_permlane32_swap_b32 v181, v183\n"
      "s_add_u32 s28, s28, 65536\n"
      "v_add_u32 v93, s28, v0\n"
      "s_lshl_b32 s28, s14, 9\n"
      "s_add_u32 s34, s28, s23\n"
      "s_waitcnt lgkmcnt(8)\n"
      "v_mfma_f32_32x32x16_bf16 v[16:31], v[184:187], a[100:103], v[16:31]\n"
      "s_add_u32 s30, s33, 61440\n"
      "s_mov_b32 m0, s30\n"
      "s_mul_i32 s31, s83, 172032\n"
      "s_add_u32 s31, s31, s32\n"
      "s_add_u32 s31, s31, 0\n"
      "buffer_load_dwordx4 v87, s[4:7], s31 offen sc1 lds\n"
      "v_mfma_f32_32x32x16_bf16 v[32:47], v[184:187], a[104:107], v[32:47]\n"
      "v_permlane32_swap_b32 v184, v186\n"
      "v_permlane32_swap_b32 v185, v187\n"
      "v_lshrrev_b32 v94, 1, v0\n"
      "s_waitcnt lgkmcnt(4)\n"
      "v_mfma_f32_32x32x16_bf16 v[16:31], v[188:191], a[108:111], v[16:31]\n"
      "s_add_u32 s30, s33, 65536\n"
      "s_mov_b32 m0, s30\n"
      "s_mul_i32 s31, s84, 172032\n"
      "s_add_u32 s31, s31, s32\n"
      "s_add_u32 s31, s31, 0\n"
      "buffer_load_dwordx4 v87, s[4:7], s31 offen sc1 lds\n"
      "v_mfma_f32_32x32x16_bf16 v[32:47], v[188:191], a[112:115], v[32:47]\n"
      "v_permlane32_swap_b32 v188, v190\n"
      "v_permlane32_swap_b32 v189, v191\n"
      "s_waitcnt vmcnt(9)\n"
      "s_barrier\n"
      "s_waitcnt lgkmcnt(3)\n"
      "v_mfma_f32_32x32x16_bf16 v[64:79], v[224:227], v[224:227], v[64:79]\n"
      "ds_read_b64_tr_b16 a[16:17], v95 offset:16384\n"
      "ds_read_b64_tr_b16 a[18:19], v95 offset:16896\n"
      "ds_read_b64_tr_b16 a[44:45], v95 offset:45056\n"
      "s_add_u32 s30, s33, 69632\n"
      "s_mov_b32 m0, s30\n"
      "s_mul_i32 s31, s85, 172032\n"
      "s_add_u32 s31, s31, s32\n"
      "s_add_u32 s31, s31, 0\n"
      "buffer_load_dwordx4 v87, s[4:7], s31 offen sc1 lds\n"
      "v_dot2c_f32_bf16 v84, v224, v255\n"
      "v_dot2c_f32_bf16 v85, v225, v255\n"
      "v_dot2c_f32_bf16 v84, v226, v255\n"
      "v_dot2c_f32_bf16 v85, v227, v255\n"
      "ds_read_b64_tr_b16 a[46:47], v95 offset:45568\n"
      "ds_read_b64_tr_b16 v[96:97], v3 offset:16384\n"
      "ds_read_b64_tr_b16 v[98:99], v3 offset:16640\n"
      "s_waitcnt lgkmcnt(8)\n"
      "v_mfma_f32_32x32x16_bf16 v[64:79], v[228:231], v[228:231], v[64:79]\n"
      "ds_read_b64_tr_b16 v[100:101], v3 offset:45056\n"
      "ds_read_b64_tr_b16 v[102:103], v3 offset:45312\n"
      "ds_read_b64_tr_b16 v[104:105], v3 offset:17408\n"
      "v_dot2c_f32_bf16 v84, v228, v255\n"
      "v_dot2c_f32_bf16 v85, v229, v255\n"
      "v_dot2c_f32_bf16 v84, v230, v255\n"
      "v_dot2c_f32_bf16 v85, v231, v255\n"
      "ds_read_b64_tr_b16 v[106:107], v3 offset:17664\n"
      "ds_read_b64_tr_b16 v[108:109], v3 offset:46080\n"
      "ds_read_b64_tr_b16 v[110:111], v3 offset:46336\n"
      "s_waitcnt lgkmcnt(13)\n"
      "v_mfma_f32_32x32x16_bf16 v[64:79], v[232:235], v[232:235], v[64:79]\n"
      "ds_read_b64_tr_b16 v[112:113], v3 offset:18432\n"
      "ds_read_b64_tr_b16 v[114:115], v3 offset:18688\n"
      "ds_read_b64_tr_b16 v[116:117], v3 offset:47104\n"
      "s_add_u32 s30, s33, 73728\n"
      "s_mov_b32 m0, s30\n"
      "s_mul_i32 s31, s86, 172032\n"
      "s_add_u32 s31, s31, s32\n"
      "s_add_u32 s31, s31, 0\n"
      "buffer_load_dwordx4 v87, s[4:7], s31 offen sc1 lds\n"
      "v_dot2c_f32_bf16 v84, v232, v255\n"
      "v_dot2c_f32_bf16 v85, v233, v255\n"
      "v_dot2c_f32_bf16 v84, v234, v255\n"
      "v_dot2c_f32_bf16 v85, v235, v255\n"
      "ds_read_b64_tr_b16 v[118:119], v3 offset:47360\n"
      "ds_read_b64_tr_b16 v[120:121], v3 offset:19456\n"
      "ds_read_b64_tr_b16 v[122:123], v3 offset:19712\n"
      "s_waitcnt lgkmcnt(15)\n"
      "v_mfma_f32_32x32x16_bf16 v[64:79], v[236:239], v[236:239], v[64:79]\n"
      "ds_read_b64_tr_b16 v[124:125], v3 offset:48128\n"
      "ds_read_b64_tr_b16 v[126:127], v3 offset:48384\n"
      "v_dot2c_f32_bf16 v84, v236, v255\n"
      "v_dot2c_f32_bf16 v85, v237, v255\n"
      "v_dot2c_f32_bf16 v84, v238, v255\n"
      "v_dot2c_f32_bf16 v85, v239, v255\n"
      "s_nop 2 ; hz\n"
      "v_add_f32 v84, v84, v85\n"
      "v_mov_b32 v86, v84\n"
      "s_nop 1 ; hz\n"
      "v_permlane32_swap_b32 v84, v86\n"
      "v_add_f32 v84, v84, v86\n"
      "s_waitcnt vmcnt(9)\n"
      "s_barrier\n"
      "s_waitcnt lgkmcnt(15)\n"
      "v_mfma_f32_32x32x16_bf16 a[208:223], a[192:195], a[16:19], a[208:223]\n"
      "ds_read_b64_tr_b16 a[20:21], v95 offset:20480\n"
      "ds_read_b64_tr_b16 a[22:23], v95 offset:20992\n"
      "ds_write_b128 v254, v[64:67] offset:8192\n"
      "v_mfma_f32_32x32x16_bf16 a[224:239], a[192:195], a[44:47], a[224:239]\n"
      "ds_read_b64_tr_b16 a[48:49], v95 offset:49152\n"
      "ds_read_b64_tr_b16 a[50:51], v95 offset:49664\n"
      "ds_write_b128 v254, v[68:71] offset:9216\n"
      "s_waitcnt lgkmcnt(15)\n"
      "v_mfma_f32_32x32x16_bf16 v[16:31], v[192:195], v[96:99], v[16:31]\n"
      "ds_read_b64_tr_b16 a[84:85], v3 offset:20480\n"
      "ds_read_b64_tr_b16 a[86:87], v3 offset:20736\n"
      "ds_read_b64_tr_b16 a[88:89], v3 offset:49152\n"
      "s_add_u32 s30, s33, 77824\n"
      "s_mov_b32 m0, s30\n"
      "s_mul_i32 s31, s87, 172032\n"
      "s_add_u32 s31, s31, s32\n"
      "s_add_u32 s31, s31, 0\n"
      "buffer_load_dwordx4 v87, s[4:7], s31 offen sc1 lds\n"
      "v_mfma_f32_32x32x16_bf16 v[32:47], v[192:195], v[100:103], v[32:47]\n"
      "ds_read_b64_tr_b16 a[90:91], v3 offset:49408\n"
      "ds_read_b64_tr_b16 a[92:93], v3 offset:21504\n"
      "v_permlane32_swap_b32 v192, v194\n"
      "v_permlane32_swap_b32 v193, v195\n"
      "ds_write_b128 v254, v[72:75] offset:10240\n"
      "s_waitcnt lgkmcnt(15)\n"
      "v_mfma_f32_32x32x16_bf16 v[16:31], v[196:199], v[104:107], v[16:31]\n"
      "ds_read_b64_tr_b16 a[94:95], v3 offset:21760\n"
      "ds_read_b64_tr_b16 a[96:97], v3 offset:50176\n"
      "ds_read_b64_tr_b16 a[98:99], v3 offset:50432\n"
      "s_add_u32 s30, s33, 81920\n"
      "s_mov_b32 m0, s30\n"
      "s_mul_i32 s31, s88, 172032\n"
      "s_add_u32 s31, s31, s32\n"
      "s_add_u32 s31, s31, 0\n"
      "buffer_load_dwordx4 v87, s[4:7], s31 offen sc1 lds\n"
      "v_mfma_f32_32x32x16_bf16 v[32:47], v[196:199], v[108:111], v[32:47]\n"
      "ds_read_b64_tr_b16 a[100:101], v3 offset:22528\n"
      "ds_read_b64_tr_b16 a[102:103], v3 offset:22784\n"
      "v_permlane32_swap_b32 v196, v198\n"
      "v_permlane32_swap_b32 v197, v199\n"
      "ds_write_b128 v254, v[76:79] offset:11264\n"
      "v_mfma_f32_32x32x16_bf16 v[16:31], v[200:203], v[112:115], v[16:31]\n"
      "ds_read_b64_tr_b16 a[104:105], v3 offset:51200\n"
      "ds_read_b64_tr_b16 a[106:107], v3 offset:51456\n"
      "ds_read_b64_tr_b16 a[108:109], v3 offset:23552\n"
      "s_mov_b32 s4, s46\n"
      "s_and_b32 s5, s47, 0xffff\n"
      "s_cmp_lg_u32 s37, 0\n"
      "s_cselect_b32 s8, s48, s44\n"
      "s_cselect_b32 s9, s49, s45\n"
      "s_and_b32 s9, s9, 0xffff\n"
      "s_mov_b32 s10, 401408\n"
      "s_add_u32 s30, s35, 12288\n"
      "s_mov_b32 m0, s30\n"
      "s_nop 0\n"
      "buffer_load_dwordx4 v87, s[4:7], s56 offen lds\n"
      "v_mfma_f32_32x32x16_bf16 v[32:47], v[200:203], v[116:119], v[32:47]\n"
      "ds_read_b64_tr_b16 a[110:111], v3 offset:23808\n"
      "ds_read_b64_tr_b16 a[112:113], v3 offset:52224\n"
      "v_permlane32_swap_b32 v200, v202\n"
      "v_permlane32_swap_b32 v201, v203\n"
      "ds_read_b32 v85, v253 offset:8192\n"
      "s_waitcnt lgkmcnt(15)\n"
      "v_mfma_f32_32x32x16_bf16 v[16:31], v[204:207], v[120:123], v[16:31]\n"
      "ds_read_b64_tr_b16 a[114:115], v3 offset:52480\n"
      "s_add_u32 s30, s35, 13312\n"
      "s_mov_b32 m0, s30\n"
      "s_nop 0\n"
      "buffer_load_dwordx4 v87, s[4:7], s57 offen lds\n"
      "v_mfma_f32_32x32x16_bf16 v[32:47], v[204:207], v[124:127], v[32:47]\n"
      "v_permlane32_swap_b32 v204, v206\n"
      "v_permlane32_swap_b32 v205, v207\n"
      "s_waitcnt vmcnt(11)\n"
      "s_barrier\n"
      "v_mfma_f32_32x32x16_bf16 a[208:223], a[192:195], a[20:23], a[208:223]\n"
      "ds_read_b64_tr_b16 a[24:25], v95 offset:24576\n"
      "ds_read_b64_tr_b16 a[26:27], v95 offset:25088\n"
      "s_waitcnt lgkmcnt(3)\n"
      "v_mfma_f32_32x32x16_bf16 a[224:239], a[192:195], a[48:51], a[224:239]\n"
      "ds_read_b64_tr_b16 a[52:53], v95 offset:53248\n"
      "ds_read_b64_tr_b16 a[54:55], v95 offset:53760\n"
      "v_fma_f32 v84, 0, v85, v84\n"
      "v_mul_f32 v84, 0xbb124925, v84\n"
      "v_mul_f32 v85, 0x3b124925, v85\n"
      "v_mfma_f32_32x32x16_bf16 v[16:31], v[208:211], a[84:87], v[16:31]\n"
      "ds_read_b64_tr_b16 v[96:97], v3 offset:24576\n"
      "ds_read_b64_tr_b16 v[98:99], v3 offset:24832\n"
      "ds_read_b64_tr_b16 v[100:101], v3 offset:53248\n"
      "s_add_u32 s30, s35, 14336\n"
      "s_mov_b32 m0, s30\n"
      "s_nop 0\n"
      "buffer_load_dwordx4 v90, s[8:11], s58 offen lds\n"
      "v_mfma_f32_32x32x16_bf16 v[32:47], v[208:211], a[88:91], v[32:47]\n"
      "ds_read_b64_tr_b16 v[102:103], v3 offset:53504\n"
      "ds_read_b64_tr_b16 v[104:105], v3 offset:25600\n"
      "v_permlane32_swap_b32 v208, v210\n"
      "v_permlane32_swap_b32 v209, v211\n"
      "v_fma_f32 v85, -v84, v84, v85\n"
      "v_max_f32 v85, 0, v85\n"
      "v_add_f32 v85, 0x3727c5ac, v85\n"
      "v_mfma_f32_32x32x16_bf16 v[16:31], v[212:215], a[92:95], v[16:31]\n"
      "ds_read_b64_tr_b16 v[106:107], v3 offset:25856\n"
      "ds_read_b64_tr_b16 v[108:109], v3 offset:54272\n"
      "ds_read_b64_tr_b16 v[110:111], v3 offset:54528\n"
      "v_mfma_f32_32x32x16_bf16 v[32:47], v[212:215], a[96:99], v[32:47]\n"
      "ds_read_b64_tr_b16 v[112:113], v3 offset:26624\n"
      "ds_read_b64_tr_b16 v[114:115], v3 offset:26880\n"
      "v_permlane32_swap_b32 v212, v214\n"
      "v_permlane32_swap_b32 v213, v215\n"
      "v_rsq_f32 v88, v85\n"
      "v_cvt_pk_bf16_f32 v81, v84, 0\n"
      "v_lshlrev_b32 v86, 16, v81\n"
      "v_mfma_f32_32x32x16_bf16 v[16:31], v[216:219], a[100:103], v[16:31]\n"
      "ds_read_b64_tr_b16 v[116:117], v3 offset:55296\n"
      "ds_read_b64_tr_b16 v[118:119], v3 offset:55552\n"
      "ds_read_b64_tr_b16 v[120:121], v3 offset:27648\n"
      "v_mfma_f32_32x32x16_bf16 v[32:47], v[216:219], a[104:107], v[32:47]\n"
      "ds_read_b64_tr_b16 v[122:123], v3 offset:27904\n"
      "ds_read_b64_tr_b16 v[124:125], v3 offset:56320\n"
      "v_permlane32_swap_b32 v216, v218\n"
      "v_permlane32_swap_b32 v217, v219\n"
      "v_sub_f32 v86, v84, v86\n"
      "v_cvt_pk_bf16_f32 v80, v84, v86\n"
      "v_mov_b32 v89, v88\n"
      "s_waitcnt lgkmcnt(15)\n"
      "v_mfma_f32_32x32x16_bf16 v[16:31], v[220:223], a[108:111], v[16:31]\n"
      "ds_read_b64_tr_b16 v[126:127], v3 offset:56576\n"
      "v_mfma_f32_32x32x16_bf16 v[32:47], v[220:223], a[112:115], v[32:47]\n"
      "v_permlane32_swap_b32 v220, v222\n"
      "v_permlane32_swap_b32 v221, v223\n"
      "ds_write_b32 v9, v88\n"
      "ds_read_b128 v[64:67], v10 offset:0\n"
      "ds_read_b128 v[68:71], v10 offset:32\n"
      "ds_read_b128 v[72:75], v10 offset:64\n"
      "ds_read_b128 v[76:79], v10 offset:96\n"
      "v_mfma_f32_32x32x16_bf16 a[208:223], a[192:195], a[24:27], a[208:223]\n"
      "v_mfma_f32_32x32x16_bf16 a[224:239], a[192:195], a[52:55], a[224:239]\n"
      "s_waitcnt lgkmcnt(15)\n"
      "v_mfma_f32_32x32x16_bf16 v[16:31], v[224:227], v[96:99], v[16:31]\n"
      "v_mfma_f32_32x32x16_bf16 v[32:47], v[224:227], v[100:103], v[32:47]\n"
      "s_waitcnt lgkmcnt(13)\n"
      "v_mfma_f32_32x32x16_bf16 v[16:31], v[228:231], v[104:107], v[16:31]\n"
      "s_nop 5 ; hz\n"
      "ds_write_b32 v8, a208 offset:0\n"
      "ds_write_b32 v8, a224 offset:128\n"
      "v_mfma_f32_32x32x16_bf16 v[32:47], v[228:231], v[108:111], v[32:47]\n"
      "s_waitcnt lgkmcnt(11)\n"
      "v_mfma_f32_32x32x16_bf16 v[16:31], v[232:235], v[112:115], v[16:31]\n"
      "s_waitcnt lgkmcnt(0)\n"
      "s_barrier\n"
      "v_lshrrev_b32 v55, 1, v6\n"
      "v_add_u32 v55, 161792, v55\n"
      "ds_read_b32 v48, v55 offset:0\n"
      "ds_read_b32 v49, v55 offset:128\n"
      "ds_read_b32 v50, v55 offset:384\n"
      "ds_read_b32 v51, v55 offset:512\n"
      "ds_read_b32 v52, v55 offset:768\n"
      "ds_read_b32 v53, v55 offset:896\n"
      "ds_read_b32 v54, v55 offset:1152\n"
      "ds_read_b32 v55, v55 offset:1280\n"
      "v_mfma_f32_32x32x16_bf16 v[32:47], v[232:235], v[116:119], v[32:47]\n"
      "v_mfma_f32_32x32x16_bf16 v[16:31], v[236:239], v[120:123], v[16:31]\n"
      "v_mfma_f32_32x32x16_bf16 v[32:47], v[236:239], v[124:127], v[32:47]\n"
      "s_waitcnt lgkmcnt(0)\n"
      "v_add_f32 v48, v48, v50\n"
      "v_add_f32 v52, v52, v54\n"
      "v_add_f32 v49, v49, v51\n"
      "v_add_f32 v53, v53, v55\n"
      "v_add_f32 v48, v48, v52\n"
      "v_add_f32 v49, v49, v53\n"
      "v_cvt_pk_bf16_f32 v50, v48, v48\n"
      "v_cvt_pk_bf16_f32 v51, v49, v49\n"
      "v_and_b32 v52, 0xffff0000, v50\n"
      "v_sub_f32 v52, v48, v52\n"
      "v_and_b32 v53, 0xffff0000, v51\n"
      "v_sub_f32 v53, v49, v53\n"
      "v_cvt_pk_bf16_f32 v52, v52, 0\n"
      "v_cvt_pk_bf16_f32 v53, v53, 0\n"
      "s_mov_b32 exec_hi, 0\n"
      "v_accvgpr_write_b32 a168, v50\n"
      "v_accvgpr_write_b32 a169, v52\n"
      "v_accvgpr_write_b32 a172, v51\n"
      "v_accvgpr_write_b32 a173, v53\n"
      "s_mov_b32 exec_hi, -1\n"
      "v_mfma_f32_32x32x16_bf16 v[16:31], v[80:83], a[168:171], v[16:31]\n"
      "v_mfma_f32_32x32x16_bf16 v[32:47], v[80:83], a[172:175], v[32:47]\n"
      "s_lshl_b32 s11, s16, 8\n"
      "s_mov_b32 s5, 1\n"
      "s_mov_b32 s7, 0x207\n"
      "s_cmp_eq_u32 s16, 0\n"
      "s_cselect_b32 s6, 0, 2147483648\n"
      "s_branch .Lepa3_%=\n"
      ".Lxpub4_%=:\n"
      "buffer_store_dwordx4 v[24:27], v0, s[0:3], s32 offen offset:0 sc1\n"
      "buffer_store_dwordx4 v[28:31], v0, s[0:3], s32 offen offset:1024 sc1\n"
      "v_pk_add_f32 v[84:85], v[32:33], v[34:35]\n"
      "v_pk_mul_f32 v[86:87], v[32:33], v[32:33]\n"
      "v_pk_add_f32 v[88:89], v[36:37], v[38:39]\n"
      "v_pk_mul_f32 v[90:91], v[34:35], v[34:35]\n"
      "v_pk_fma_f32 v[86:87], v[36:37], v[36:37], v[86:87]\n"
      "v_pk_fma_f32 v[90:91], v[38:39], v[38:39], v[90:91]\n"
      "v_pk_fma_f32 v[86:87], v[40:41], v[40:41], v[86:87]\n"
      "v_pk_add_f32 v[84:85], v[84:85], v[40:41]\n"
      "v_pk_fma_f32 v[90:91], v[42:43], v[42:43], v[90:91]\n"
      "v_pk_add_f32 v[88:89], v[88:89], v[42:43]\n"
      "v_pk_fma_f32 v[86:87], v[44:45], v[44:45], v[86:87]\n"
      "v_pk_add_f32 v[84:85], v[84:85], v[44:45]\n"
      "v_pk_fma_f32 v[90:91], v[46:47], v[46:47], v[90:91]\n"
      "v_pk_add_f32 v[88:89], v[88:89], v[46:47]\n"
      "v_pk_add_f32 v[84:85], v[84:85], v[88:89]\n"
      "v_pk_add_f32 v[86:87], v[86:87], v[90:91]\n"
      "v_add_f32 v84, v84, v85\n"
      "v_add_f32 v85, v86, v87\n"
      "v_mov_b64 v[86:87], v[84:85]\n"
      "s_nop 1 ; hz\n"
      "v_permlane32_swap_b32 v84, v86\n"
      "v_permlane32_swap_b32 v85, v87\n"
      "v_pk_add_f32 v[84:85], v[84:85], v[86:87]\n"
      "s_mov_b32 exec_hi, 0\n"
      "buffer_store_dwordx2 v[84:85], v6, s[0:3], s33 offen sc1\n"
      "s_mov_b32 exec_hi, -1\n"
      ".Lxhop5_%=:\n"
      "v_lshlrev_b32 v240, 16, v24\n"
      "v_and_b32 v241, 0xffff0000, v24\n"
      "v_lshlrev_b32 v242, 16, v25\n"
      "v_and_b32 v243, 0xffff0000, v25\n"
      "v_lshlrev_b32 v244, 16, v26\n"
      "v_and_b32 v245, 0xffff0000, v26\n"
      "v_lshlrev_b32 v246, 16, v27\n"
      "v_and_b32 v247, 0xffff0000, v27\n"
      "v_lshlrev_b32 v248, 16, v28\n"
      "v_and_b32 v249, 0xffff0000, v28\n"
      "v_lshlrev_b32 v250, 16, v29\n"
      "v_and_b32 v251, 0xffff0000, v29\n"
      "v_lshlrev_b32 v252, 16, v30\n"
      "v_and_b32 v253, 0xffff0000, v30\n"
      "v_lshlrev_b32 v254, 16, v31\n"
      "v_and_b32 v255, 0xffff0000, v31\n"
      "s_cmp_eq_u32 s17, 1\n"
      "s_cbranch_scc0 .Lif9_%=\n"
      "s_barrier\n"
      "s_lshl_b32 s28, s15, 3\n"
      "s_add_u32 s28, s28, s25\n"
      "s_add_u32 s28, s28, s27\n"
      "s_or_b32 s28, s28, s6\n"
      "s_mov_b64 exec, 0xffff\n"
      "buffer_store_dwordx2 v[12:13], v4, s[0:3], s28 offen sc1\n"
      "s_mov_b64 exec, -1\n"
      "s_waitcnt vmcnt(4)\n"
      "s_cmp_ge_u32 s15, 14\n"
      "s_cbranch_scc0 .Lif10_%=\n"
      "s_waitcnt vmcnt(0)\n"
      ".Lif10_%=:\n"
      ".Lif9_%=:\n"
      "s_bitcmp1_b32 s17, 0\n"
      "s_cbranch_scc1 .Lib13_%=\n"
      "s_cmp_eq_u32 s16, 0\n"
      "s_cbranch_scc0 .Lif15_%=\n"
      "s_add_u32 s28, s25, s27\n"
      "s_lshl_b32 s29, s15, 8\n"
      "s_add_u32 s28, s28, s29\n"
      "s_mov_b32 exec_lo, 65535\n"
      "s_mov_b32 exec_hi, 0\n"
      "buffer_load_dwordx2 v[92:93], v6, s[0:3], s28 offen sc1\n"
      "s_mov_b64 exec, -1\n"
      ".Lif15_%=:\n"
      "s_lshl_b32 s28, s85, 12\n"
      "v_add_u32 v87, s28, v7\n"
      "ds_write_b128 v87, a[40:43] offset:28672\n"
      "s_lshl_b32 s28, s86, 12\n"
      "v_add_u32 v86, s28, v7\n"
      "ds_write_b128 v86, a[44:47] offset:28672\n"
      "s_lshl_b32 s28, s87, 12\n"
      "v_add_u32 v87, s28, v7\n"
      "ds_write_b128 v87, a[48:51] offset:28672\n"
      "s_lshl_b32 s28, s88, 12\n"
      "v_add_u32 v86, s28, v7\n"
      "ds_write_b128 v86, a[52:55] offset:28672\n"
      "s_lshl_b32 s28, s82, 12\n"
      "v_add_u32 v86, s28, v7\n"
      "ds_write_b128 v86, a[56:59] offset:57344\n"
      "s_lshl_b32 s28, s83, 12\n"
      "v_add_u32 v87, s28, v7\n"
      "ds_write_b128 v87, a[60:63] offset:57344\n"
      "s_lshl_b32 s28, s84, 12\n"
      "v_add_u32 v86, s28, v7\n"
      "ds_write_b128 v86, a[64:67] offset:57344\n"
      "s_lshl_b32 s28, s85, 12\n"
      "v_add_u32 v87, s28, v7\n"
      "ds_write_b128 v87, a[68:71] offset:57344\n"
      "s_lshl_b32 s28, s86, 12\n"
      "v_add_u32 v86, s28, v7\n"
      "ds_write_b128 v86, a[72:75] offset:57344\n"
      "s_lshl_b32 s28, s87, 12\n"
      "v_add_u32 v87, s28, v7\n"
      "ds_write_b128 v87, a[76:79] offset:57344\n"
      "s_lshl_b32 s28, s88, 12\n"
      "v_add_u32 v86, s28, v7\n"
      "ds_write_b128 v86, a[80:83] offset:57344\n"
      "s_branch .Lid14_%=\n"
      ".Lib13_%=:\n"
      "s_cmp_eq_u32 s17, 1\n"
      "s_cbranch_scc1 .Lic16_%=\n"
      "s_cmp_eq_u32 s16, 0\n"
      "s_cbranch_scc0 .Lif17_%=\n"
      "s_add_u32 s28, s25, s27\n"
      "s_lshl_b32 s29, s15, 8\n"
      "s_add_u32 s28, s28, s29\n"
      "s_mov_b32 exec_lo, 65535\n"
      "s_mov_b32 exec_hi, 0\n"
      "buffer_load_dwordx2 v[92:93], v6, s[0:3], s28 offen sc1\n"
      "s_mov_b64 exec, -1\n"
      ".Lif17_%=:\n"
      "ds_write_b128 v7, a[124:127] offset:40960\n"
      "ds_write_b128 v7, a[128:131] offset:45056\n"
      "ds_write_b128 v7, a[132:135] offset:49152\n"
      "ds_write_b128 v7, a[136:139] offset:53248\n"
      "v_add_u32 v87, 57344, v7\n"
      "ds_write_b128 v87, a[140:143] offset:0\n"
      "ds_write_b128 v87, a[144:147] offset:4096\n"
      "ds_write_b128 v87, a[148:151] offset:8192\n"
      "ds_write_b128 v87, a[152:155] offset:12288\n"
      "ds_write_b128 v87, a[156:159] offset:16384\n"
      "ds_write_b128 v87, a[160:163] offset:20480\n"
      "ds_write_b128 v87, a[164:167] offset:24576\n"
      "s_branch .Lid14_%=\n"
      ".Lic16_%=:\n"
      "v_mov_b32 v80, 0x3f803f80\n"
      "v_mov_b32 v81, 0x3f803f80\n"
      "v_mov_b32 v82, 0x3f803f80\n"
      "v_mov_b32 v83, 0x3f803f80\n"
      "s_cmp_eq_u32 s16, 0\n"
      "s_cbranch_scc0 .Lif18_%=\n"
      "s_add_u32 s28, s25, s27\n"
      "s_lshl_b32 s29, s15, 8\n"
      "s_add_u32 s28, s28, s29\n"
      "s_mov_b32 exec_lo, 65535\n"
      "s_mov_b32 exec_hi, 0\n"
      "buffer_load_dwordx2 v[92:93], v6, s[0:3], s28 offen sc1\n"
      "s_mov_b64 exec, -1\n"
      ".Lif18_%=:\n"
      "v_mfma_f32_32x32x16_bf16 v[16:31], v[80:83], a[84:87], 0\n"
      "v_mfma_f32_32x32x16_bf16 v[16:31], v[80:83], a[88:91], v[16:31]\n"
      "ds_write_b128 v7, a[124:127] offset:40960\n"
      "v_mfma_f32_32x32x16_bf16 v[16:31], v[80:83], a[92:95], v[16:31]\n"
      "v_mfma_f32_32x32x16_bf16 v[16:31], v[80:83], a[96:99], v[16:31]\n"
      "ds_write_b128 v7, a[128:131] offset:45056\n"
      "v_mfma_f32_32x32x16_bf16 v[16:31], v[80:83], a[100:103], v[16:31]\n"
      "v_mfma_f32_32x32x16_bf16 v[16:31], v[80:83], a[104:107], v[16:31]\n"
      "ds_write_b128 v7, a[132:135] offset:49152\n"
      "v_mfma_f32_32x32x16_bf16 v[16:31], v[80:83], a[108:111], v[16:31]\n"
      "v_mfma_f32_32x32x16_bf16 v[32:47], v[80:83], a[112:115], 0\n"
      "ds_write_b128 v7, a[136:139] offset:53248\n"
      "v_mfma_f32_32x32x16_bf16 v[32:47], v[80:83], a[116:119], v[32:47]\n"
      "v_mfma_f32_32x32x16_bf16 v[32:47], v[80:83], a[120:123], v[32:47]\n"
      "v_add_u32 v87, 57344, v7\n"
      "ds_write_b128 v87, a[140:143] offset:0\n"
      "v_mfma_f32_32x32x16_bf16 v[32:47], v[80:83], a[124:127], v[32:47]\n"
      "v_mfma_f32_32x32x16_bf16 v[32:47], v[80:83], a[128:131], v[32:47]\n"
      "ds_write_b128 v87, a[144:147] offset:4096\n"
      "v_mfma_f32_32x32x16_bf16 v[32:47], v[80:83], a[132:135], v[32:47]\n"
      "v_mfma_f32_32x32x16_bf16 v[32:47], v[80:83], a[136:139], v[32:47]\n"
      "ds_write_b128 v87, a[148:151] offset:8192\n"
      "v_mfma_f32_32x32x16_bf16 v[48:63], v[80:83], a[140:143], 0\n"
      "v_mfma_f32_32x32x16_bf16 v[48:63], v[80:83], a[144:147], v[48:63]\n"
      "ds_write_b128 v87, a[152:155] offset:12288\n"
      "v_mfma_f32_32x32x16_bf16 v[48:63], v[80:83], a[148:151], v[48:63]\n"
      "v_mfma_f32_32x32x16_bf16 v[48:63], v[80:83], a[152:155], v[48:63]\n"
      "ds_write_b128 v87, a[156:159] offset:16384\n"
      "v_mfma_f32_32x32x16_bf16 v[48:63], v[80:83], a[156:159], v[48:63]\n"
      "v_mfma_f32_32x32x16_bf16 v[48:63], v[80:83], a[160:163], v[48:63]\n"
      "ds_write_b128 v87, a[160:163] offset:20480\n"
      "v_mfma_f32_32x32x16_bf16 v[48:63], v[80:83], a[164:167], v[48:63]\n"
      "ds_write_b128 v87, a[164:167] offset:24576\n"
      "ds_write_b32 v8, v16 offset:0\n"
      "ds_write_b32 v8, v32 offset:128\n"
      "s_nop 7 ; hz\n"
      "s_nop 0 ; hz\n"
      "ds_write_b32 v8, v48 offset:256\n"
      ".Lid14_%=:\n"
      "s_cmp_eq_u32 s16, 0\n"
      "s_cbranch_scc0 .Lif22_%=\n"
      "s_add_u32 s28, s25, s27\n"
      "s_lshl_b32 s29, s15, 8\n"
      "s_add_u32 s28, s28, s29\n"
      "s_mov_b32 s71, 0x10000\n"
      "s_mov_b32 exec_lo, 65535\n"
      "s_mov_b32 exec_hi, 0\n"
      "s_branch .Lpc21_%=\n"
      ".Lpl19_%=:\n"
      "buffer_load_dwordx2 v[92:93], v6, s[0:3], s28 offen sc1\n"
      ".Lpc21_%=:\n"
      "s_waitcnt vmcnt(0)\n"
      "v_cmp_eq_u64 vcc, s[12:13], v[92:93]\n"
      "s_nop 0 ; hz\n"
      "s_cmp_eq_u64 vcc, exec\n"
      "s_cbranch_scc1 .Lpo20_%=\n"
      "s_sleep 1\n"
      "s_sub_u32 s71, s71, 1\n"
      "s_cmp_lg_u32 s71, 0\n"
      "s_cbranch_scc1 .Lpl19_%=\n"
      ".Lpo20_%=:\n"
      "s_mov_b64 exec, -1\n"
      ".Lif22_%=:\n"
      "s_add_u32 s27, s27, 4096\n"
      "s_waitcnt vmcnt(0) lgkmcnt(0)\n"
      "s_barrier\n"
      ".Lxhead23_%=:\n"
      "s_mov_b32 s30, s20\n"
      "buffer_load_dwordx4 v[128:131], v0, s[0:3], s30 offen offset:0 sc1\n"
      "buffer_load_dwordx4 v[132:135], v0, s[0:3], s30 offen offset:1024 sc1\n"
      "buffer_load_dwordx4 v[136:139], v0, s[0:3], s30 offen offset:2048 sc1\n"
      "buffer_load_dwordx4 v[140:143], v0, s[0:3], s30 offen offset:3072 sc1\n"
      "v_mov_b64 v[76:77], 0\n"
      "v_mov_b64 v[78:79], 0\n"
      "s_cmp_eq_u32 s17, 1\n"
      "s_cbranch_scc0 .Lif27_%=\n"
      "v_lshrrev_b32 v27, 1, v6\n"
      "v_add_u32 v27, 161792, v27\n"
      "ds_read_b32 v16, v27 offset:0\n"
      "ds_read_b32 v17, v27 offset:128\n"
      "ds_read_b32 v18, v27 offset:256\n"
      "ds_read_b32 v19, v27 offset:384\n"
      "ds_read_b32 v20, v27 offset:512\n"
      "ds_read_b32 v21, v27 offset:640\n"
      "ds_read_b32 v22, v27 offset:768\n"
      "ds_read_b32 v23, v27 offset:896\n"
      "ds_read_b32 v24, v27 offset:1024\n"
      "ds_read_b32 v25, v27 offset:1152\n"
      "ds_read_b32 v26, v27 offset:1280\n"
      "ds_read_b32 v27, v27 offset:1408\n"
      "s_waitcnt lgkmcnt(0)\n"
      "v_add_f32 v16, v16, v19\n"
      "v_add_f32 v22, v22, v25\n"
      "v_add_f32 v17, v17, v20\n"
      "v_add_f32 v23, v23, v26\n"
      "v_add_f32 v18, v18, v21\n"
      "v_add_f32 v24, v24, v27\n"
      "v_add_f32 v16, v16, v22\n"
      "v_add_f32 v17, v17, v23\n"
      "v_add_f32 v18, v18, v24\n"
      "v_cvt_pk_bf16_f32 v19, v16, v16\n"
      "v_cvt_pk_bf16_f32 v20, v17, v17\n"
      "v_cvt_pk_bf16_f32 v21, v18, v18\n"
      "v_and_b32 v22, 0xffff0000, v19\n"
      "v_sub_f32 v22, v16, v22\n"
      "v_and_b32 v23, 0xffff0000, v20\n"
      "v_sub_f32 v23, v17, v23\n"
      "v_and_b32 v24, 0xffff0000, v21\n"
      "v_sub_f32 v24, v18, v24\n"
      "v_cvt_pk_bf16_f32 v22, v22, 0\n"
      "v_cvt_pk_bf16_f32 v23, v23, 0\n"
      "v_cvt_pk_bf16_f32 v24, v24, 0\n"
      "s_mov_b32 exec_hi, 0\n"
      "v_accvgpr_write_b32 a180, v19\n"
      "v_accvgpr_write_b32 a181, v22\n"
      "v_accvgpr_write_b32 a184, v20\n"
      "v_accvgpr_write_b32 a185, v23\n"
      "v_accvgpr_write_b32 a188, v21\n"
      "v_accvgpr_write_b32 a189, v24\n"
      "s_mov_b32 exec_hi, -1\n"
      ".Lif27_%=:\n"
      "s_add_u32 s30, s30, 0x1000\n"
      "buffer_load_dwordx4 v[144:147], v0, s[0:3], s30 offen offset:0 sc1\n"
      "buffer_load_dwordx4 v[148:151], v0, s[0:3], s30 offen offset:1024 sc1\n"
      "buffer_load_dwordx4 v[152:155], v0, s[0:3], s30 offen offset:2048 sc1\n"
      "buffer_load_dwordx4 v[156:159], v0, s[0:3], s30 offen offset:3072 sc1\n"
      "s_add_u32 s30, s30, 0x1000\n"
      "buffer_load_dwordx4 v[160:163], v0, s[0:3], s30 offen offset:0 sc1\n"
      "buffer_load_dwordx4 v[164:167], v0, s[0:3], s30 offen offset:1024 sc1\n"
      "s_bitcmp1_b32 s17, 0\n"
      "s_cbranch_scc1 .Lmixb6_%=\n"
      "ds_read_b128 v[96:99], v1 offset:0\n"
      "ds_read_b128 v[100:103], v1 offset:28672\n"
      "ds_read_b128 v[104:107], v1 offset:1024\n"
      "ds_read_b128 v[108:111], v1 offset:29696\n"
      "ds_read_b128 v[112:115], v1 offset:2048\n"
      "ds_read_b128 v[116:119], v1 offset:30720\n"
      "buffer_load_dwordx4 v[168:171], v0, s[0:3], s30 offen offset:2048 sc1\n"
      "s_waitcnt vmcnt(10) lgkmcnt(4)\n"
      "v_mfma_f32_32x32x16_bf16 v[16:31], v[128:131], v[96:99], 0\n"
      "ds_read_b128 v[120:123], v1 offset:3072\n"
      "ds_read_b128 v[124:127], v1 offset:31744\n"
      "v_cmp_eq_u32 vcc, 224, v6\n"
      "v_mov_b32 v57, 0x3f803f80\n"
      "v_mfma_f32_32x32x16_bf16 v[32:47], v[128:131], v[100:103], 0\n"
      "v_cndmask_b32 v56, 0, v57, vcc\n"
      "v_mov_b32 v57, v56\n"
      "buffer_load_dwordx4 v[172:175], v0, s[0:3], s30 offen offset:3072 sc1\n"
      "buffer_load_dwordx4 v[64:67], v0, s[0:3], s21 offen offset:0 sc1\n"
      "buffer_load_dwordx4 v[68:71], v0, s[0:3], s21 offen offset:1024 sc1\n"
      "buffer_load_dwordx4 v[72:75], v0, s[0:3], s21 offen offset:2048 sc1\n"
      "s_mov_b32 exec_hi, 0\n"
      "buffer_load_dwordx4 v[76:79], v0, s[0:3], s21 offen offset:3072 sc1\n"
      "s_mov_b32 exec_hi, -1\n"
      "s_waitcnt vmcnt(14) lgkmcnt(4)\n"
      "v_mfma_f32_32x32x16_bf16 v[16:31], v[132:135], v[104:107], v[16:31]\n"
      "ds_read_b128 v[96:99], v1 offset:4096\n"
      "ds_read_b128 v[100:103], v1 offset:32768\n"
      "v_mov_b64 v[58:59], v[56:57]\n"
      "v_mov_b64 v[60:61], v[56:57]\n"
      "v_mfma_f32_32x32x16_bf16 v[32:47], v[132:135], v[108:111], v[32:47]\n"
      "v_mov_b64 v[62:63], v[56:57]\n"
      "s_nop 0\n"
      "s_add_u32 s30, s30, 0x1000\n"
      "buffer_load_dwordx4 v[176:179], v0, s[0:3], s30 offen offset:0 sc1\n"
      "s_waitcnt vmcnt(14) lgkmcnt(4)\n"
      "v_mfma_f32_32x32x16_bf16 v[16:31], v[136:139], v[112:115], v[16:31]\n"
      "ds_read_b128 v[104:107], v1 offset:5120\n"
      "ds_read_b128 v[108:111], v1 offset:33792\n"
      "s_lshl_b32 s28, s16, 12\n"
      "s_add_u32 s28, s28, 65536\n"
      "v_mfma_f32_32x32x16_bf16 v[32:47], v[136:139], v[116:119], v[32:47]\n"
      "v_add_u32 v92, s28, v0\n"
      "s_lshl_b32 s28, s16, 10\n"
      "buffer_load_dwordx4 v[180:183], v0, s[0:3], s30 offen offset:1024 sc1\n"
      "s_waitcnt vmcnt(14) lgkmcnt(4)\n"
      "v_mfma_f32_32x32x16_bf16 v[16:31], v[140:143], v[120:123], v[16:31]\n"
      "ds_read_b128 v[112:115], v1 offset:6144\n"
      "ds_read_b128 v[116:119], v1 offset:34816\n"
      "s_add_u32 s28, s28, 65536\n"
      "v_add_u32 v93, s28, v0\n"
      "v_mfma_f32_32x32x16_bf16 v[32:47], v[140:143], v[124:127], v[32:47]\n"
      "s_lshl_b32 s28, s14, 9\n"
      "s_add_u32 s34, s28, s23\n"
      "buffer_load_dwordx4 v[184:187], v0, s[0:3], s30 offen offset:2048 sc1\n"
      "s_waitcnt vmcnt(14) lgkmcnt(4)\n"
      "v_mfma_f32_32x32x16_bf16 v[16:31], v[144:147], v[96:99], v[16:31]\n"
      "ds_read_b128 v[120:123], v1 offset:7168\n"
      "ds_read_b128 v[124:127], v1 offset:35840\n"
      "v_lshrrev_b32 v94, 1, v0\n"
      "v_mfma_f32_32x32x16_bf16 v[32:47], v[144:147], v[100:103], v[32:47]\n"
      "buffer_load_dwordx4 v[188:191], v0, s[0:3], s30 offen offset:3072 sc1\n"
      "s_waitcnt vmcnt(14) lgkmcnt(4)\n"
      "v_mfma_f32_32x32x16_bf16 v[16:31], v[148:151], v[104:107], v[16:31]\n"
      "ds_read_b128 v[96:99], v1 offset:8192\n"
      "ds_read_b128 v[100:103], v1 offset:36864\n"
      "v_mfma_f32_32x32x16_bf16 v[32:47], v[148:151], v[108:111], v[32:47]\n"
      "s_add_u32 s30, s30, 0x1000\n"
      "buffer_load_dwordx4 v[192:195], v0, s[0:3], s30 offen offset:0 sc1\n"
      "s_waitcnt vmcnt(14) lgkmcnt(4)\n"
      "v_mfma_f32_32x32x16_bf16 v[16:31], v[152:155], v[112:115], v[16:31]\n"
      "ds_read_b128 v[104:107], v1 offset:9216\n"
      "ds_read_b128 v[108:111], v1 offset:37888\n"
      "v_mfma_f32_32x32x16_bf16 v[32:47], v[152:155], v[116:119], v[32:47]\n"
      "buffer_load_dwordx4 v[196:199], v0, s[0:3], s30 offen offset:1024 sc1\n"
      "s_waitcnt vmcnt(14) lgkmcnt(4)\n"
      "v_mfma_f32_32x32x16_bf16 v[16:31], v[156:159], v[120:123], v[16:31]\n"
      "ds_read_b128 v[112:115], v1 offset:10240\n"
      "ds_read_b128 v[116:119], v1 offset:38912\n"
      "v_mfma_f32_32x32x16_bf16 v[32:47], v[156:159], v[124:127], v[32:47]\n"
      "buffer_load_dwordx4 v[200:203], v0, s[0:3], s30 offen offset:2048 sc1\n"
      "s_waitcnt vmcnt(14) lgkmcnt(4)\n"
      "v_mfma_f32_32x32x16_bf16 v[16:31], v[160:163], v[96:99], v[16:31]\n"
      "ds_read_b128 v[120:123], v1 offset:11264\n"
      "ds_read_b128 v[124:127], v1 offset:39936\n"
      "v_mfma_f32_32x32x16_bf16 v[32:47], v[160:163], v[100:103], v[32:47]\n"
      "buffer_load_dwordx4 v[204:207], v0, s[0:3], s30 offen offset:3072 sc1\n"
      "s_waitcnt vmcnt(14) lgkmcnt(4)\n"
      "v_mfma_f32_32x32x16_bf16 v[16:31], v[164:167], v[104:107], v[16:31]\n"
      "ds_read_b128 v[96:99], v1 offset:12288\n"
      "ds_read_b128 v[100:103], v1 offset:40960\n"
      "v_mfma_f32_32x32x16_bf16 v[32:47], v[164:167], v[108:111], v[32:47]\n"
      "s_add_u32 s30, s30, 0x1000\n"
      "buffer_load_dwordx4 v[208:211], v0, s[0:3], s30 offen offset:0 sc1\n"
      "s_waitcnt vmcnt(14) lgkmcnt(4)\n"
      "v_mfma_f32_32x32x16_bf16 v[16:31], v[168:171], v[112:115], v[16:31]\n"
      "ds_read_b128 v[104:107], v1 offset:13312\n"
      "ds_read_b128 v[108:111], v1 offset:41984\n"
      "v_mfma_f32_32x32x16_bf16 v[32:47], v[168:171], v[116:119], v[32:47]\n"
      "buffer_load_dwordx4 v[212:215], v0, s[0:3], s30 offen offset:1024 sc1\n"
      "s_waitcnt vmcnt(14) lgkmcnt(4)\n"
      "v_mfma_f32_32x32x16_bf16 v[16:31], v[172:175], v[120:123], v[16:31]\n"
      "ds_read_b128 v[112:115], v1 offset:14336\n"
      "ds_read_b128 v[116:119], v1 offset:43008\n"
      "s_lshl_b32 s28, s16, 8\n"
      "s_add_u32 s28, s28, 159744\n"
      "v_mfma_f32_32x32x16_bf16 v[32:47], v[172:175], v[124:127], v[32:47]\n"
      "v_add_u32 v86, s28, v6\n"
      "v_mov_b32 v82, 0\n"
      "buffer_load_dwordx4 v[216:219], v0, s[0:3], s30 offen offset:2048 sc1\n"
      "s_waitcnt vmcnt(10) lgkmcnt(4)\n"
      "v_mfma_f32_32x32x16_bf16 v[16:31], v[176:179], v[96:99], v[16:31]\n"
      "ds_read_b128 v[120:123], v1 offset:15360\n"
      "ds_read_b128 v[124:127], v1 offset:44032\n"
      "v_mov_b32 v83, 0\n"
      "v_mfma_f32_32x32x16_bf16 v[32:47], v[176:179], v[100:103], v[32:47]\n"
      "v_pk_add_f32 v[64:65], v[64:65], v[68:69]\n"
      "v_pk_add_f32 v[66:67], v[66:67], v[70:71]\n"
      "buffer_load_dwordx4 v[220:223], v0, s[0:3], s30 offen offset:3072 sc1\n"
      "s_waitcnt vmcnt(10) lgkmcnt(4)\n"
      "v_mfma_f32_32x32x16_bf16 v[16:31], v[180:183], v[104:107], v[16:31]\n"
      "ds_read_b128 v[96:99], v1 offset:16384\n"
      "ds_read_b128 v[100:103], v1 offset:45056\n"
      "v_pk_add_f32 v[72:73], v[72:73], v[76:77]\n"
      "v_pk_add_f32 v[74:75], v[74:75], v[78:79]\n"
      "v_mfma_f32_32x32x16_bf16 v[32:47], v[180:183], v[108:111], v[32:47]\n"
      "v_pk_add_f32 v[64:65], v[64:65], v[72:73]\n"
      "v_pk_add_f32 v[66:67], v[66:67], v[74:75]\n"
      "s_add_u32 s30, s30, 0x1000\n"
      "buffer_load_dwordx4 v[224:227], v0, s[0:3], s30 offen offset:0 sc1\n"
      "s_waitcnt vmcnt(10) lgkmcnt(4)\n"
      "v_mfma_f32_32x32x16_bf16 v[16:31], v[184:187], v[112:115], v[16:31]\n"
      "ds_read_b128 v[104:107], v1 offset:17408\n"
      "ds_read_b128 v[108:111], v1 offset:46080\n"
      "v_permlane32_swap_b32 v64, v66\n"
      "v_permlane32_swap_b32 v65, v67\n"
      "v_mfma_f32_32x32x16_bf16 v[32:47], v[184:187], v[116:119], v[32:47]\n"
      "v_pk_add_f32 v[64:65], v[64:65], v[66:67]\n"
      "buffer_load_dwordx4 v[228:231], v0, s[0:3], s30 offen offset:1024 sc1\n"
      "s_waitcnt vmcnt(10) lgkmcnt(4)\n"
      "v_mfma_f32_32x32x16_bf16 v[16:31], v[188:191], v[120:123], v[16:31]\n"
      "ds_read_b128 v[112:115], v1 offset:18432\n"
      "ds_read_b128 v[116:119], v1 offset:47104\n"
      "v_permlane16_swap_b32 v64, v65\n"
      "v_add_f32 v64, v64, v65\n"
      "v_mfma_f32_32x32x16_bf16 v[32:47], v[188:191], v[124:127], v[32:47]\n"
      "ds_write_b32 v5, v64\n"
      "ds_read_b64 v[84:85], v86\n"
      "buffer_load_dwordx4 v[232:235], v0, s[0:3], s30 offen offset:2048 sc1\n"
      "s_waitcnt vmcnt(10) lgkmcnt(6)\n"
      "v_mfma_f32_32x32x16_bf16 v[16:31], v[192:195], v[96:99], v[16:31]\n"
      "ds_read_b128 v[120:123], v1 offset:19456\n"
      "ds_read_b128 v[124:127], v1 offset:48128\n"
      "v_mfma_f32_32x32x16_bf16 v[32:47], v[192:195], v[100:103], v[32:47]\n"
      "buffer_load_dwordx4 v[236:239], v0, s[0:3], s30 offen offset:3072 sc1\n"
      "s_waitcnt vmcnt(10) lgkmcnt(6)\n"
      "v_mfma_f32_32x32x16_bf16 v[16:31], v[196:199], v[104:107], v[16:31]\n"
      "ds_read_b128 v[96:99], v1 offset:20480\n"
      "ds_read_b128 v[100:103], v1 offset:49152\n"
      "s_waitcnt lgkmcnt(4)\n"
      "v_fma_f32 v84, 0, v85, v84\n"
      "v_mfma_f32_32x32x16_bf16 v[32:47], v[196:199], v[108:111], v[32:47]\n"
      "v_mul_f32 v84, 0xbb124925, v84\n"
      "v_mul_f32 v85, 0x3b124925, v85\n"
      "s_lshl_b32 s31, s15, 8\n"
      "s_add_u32 s31, s31, s25\n"
      "s_add_u32 s31, s31, s27\n"
      "s_sub_u32 s31, s31, 3968\n"
      "s_or_b32 s31, s31, s6\n"
      "s_mov_b64 exec, 0xffff\n"
      "buffer_store_dwordx2 v[12:13], v6, s[0:3], s31 offen sc1\n"
      "s_mov_b64 exec, -1\n"
      "s_waitcnt vmcnt(10)\n"
      "v_mfma_f32_32x32x16_bf16 v[16:31], v[200:203], v[112:115], v[16:31]\n"
      "ds_read_b128 v[104:107], v1 offset:21504\n"
      "ds_read_b128 v[108:111], v1 offset:50176\n"
      "v_fma_f32 v85, -v84, v84, v85\n"
      "v_max_f32 v85, 0, v85\n"
      "v_mfma_f32_32x32x16_bf16 v[32:47], v[200:203], v[116:119], v[32:47]\n"
      "v_add_f32 v85, 0x3727c5ac, v85\n"
      "v_rsq_f32 v88, v85\n"
      "s_waitcnt vmcnt(9) lgkmcnt(4)\n"
      "v_mfma_f32_32x32x16_bf16 v[16:31], v[204:207], v[120:123], v[16:31]\n"
      "ds_read_b128 v[112:115], v1 offset:22528\n"
      "ds_read_b128 v[116:119], v1 offset:51200\n"
      "v_cvt_pk_bf16_f32 v81, v84, 0\n"
      "v_lshlrev_b32 v86, 16, v81\n"
      "v_mfma_f32_32x32x16_bf16 v[32:47], v[204:207], v[124:127], v[32:47]\n"
      "v_sub_f32 v86, v84, v86\n"
      "v_cvt_pk_bf16_f32 v80, v84, v86\n"
      "s_waitcnt vmcnt(8) lgkmcnt(4)\n"
      "v_mfma_f32_32x32x16_bf16 v[16:31], v[208:211], v[96:99], v[16:31]\n"
      "ds_read_b128 v[120:123], v1 offset:23552\n"
      "ds_read_b128 v[124:127], v1 offset:52224\n"
      "v_mov_b32 v89, v88\n"
      "ds_write_b32 v9, v88\n"
      "v_mfma_f32_32x32x16_bf16 v[32:47], v[208:211], v[100:103], v[32:47]\n"
      "ds_read_b128 v[64:67], v10 offset:0\n"
      "ds_read_b128 v[68:71], v10 offset:32\n"
      "s_waitcnt vmcnt(7) lgkmcnt(7)\n"
      "v_mfma_f32_32x32x16_bf16 v[16:31], v[212:215], v[104:107], v[16:31]\n"
      "ds_read_b128 v[96:99], v1 offset:24576\n"
      "ds_read_b128 v[100:103], v1 offset:53248\n"
      "ds_read_b128 v[72:75], v10 offset:64\n"
      "ds_read_b128 v[76:79], v10 offset:96\n"
      "v_mfma_f32_32x32x16_bf16 v[32:47], v[212:215], v[108:111], v[32:47]\n"
      "v_mfma_f32_32x32x16_bf16 v[16:31], v[80:83], a[168:171], v[16:31]\n"
      "v_mfma_f32_32x32x16_bf16 v[32:47], v[80:83], a[172:175], v[32:47]\n"
      "s_waitcnt vmcnt(6) lgkmcnt(9)\n"
      "v_mfma_f32_32x32x16_bf16 v[16:31], v[216:219], v[112:115], v[16:31]\n"
      "ds_read_b128 v[104:107], v1 offset:25600\n"
      "ds_read_b128 v[108:111], v1 offset:54272\n"
      "v_mfma_f32_32x32x16_bf16 v[32:47], v[216:219], v[116:119], v[32:47]\n"
      "s_waitcnt vmcnt(5) lgkmcnt(9)\n"
      "v_mfma_f32_32x32x16_bf16 v[16:31], v[220:223], v[120:123], v[16:31]\n"
      "ds_read_b128 v[112:115], v1 offset:26624\n"
      "ds_read_b128 v[116:119], v1 offset:55296\n"
      "v_mfma_f32_32x32x16_bf16 v[32:47], v[220:223], v[124:127], v[32:47]\n"
      "s_waitcnt vmcnt(4) lgkmcnt(6)\n"
      "v_mfma_f32_32x32x16_bf16 v[16:31], v[224:227], v[96:99], v[16:31]\n"
      "ds_read_b128 v[120:123], v1 offset:27648\n"
      "ds_read_b128 v[124:127], v1 offset:56320\n"
      "v_mfma_f32_32x32x16_bf16 v[32:47], v[224:227], v[100:103], v[32:47]\n"
      "s_waitcnt vmcnt(3) lgkmcnt(4)\n"
      "v_mfma_f32_32x32x16_bf16 v[16:31], v[228:231], v[104:107], v[16:31]\n"
      "s_barrier\n"
      "v_mfma_f32_32x32x16_bf16 v[32:47], v[228:231], v[108:111], v[32:47]\n"
      "s_waitcnt vmcnt(2) lgkmcnt(2)\n"
      "v_mfma_f32_32x32x16_bf16 v[16:31], v[232:235], v[112:115], v[16:31]\n"
      "v_mfma_f32_32x32x16_bf16 v[32:47], v[232:235], v[116:119], v[32:47]\n"
      "s_waitcnt vmcnt(1) lgkmcnt(0)\n"
      "v_mfma_f32_32x32x16_bf16 v[16:31], v[236:239], v[120:123], v[16:31]\n"
      "v_mfma_f32_32x32x16_bf16 v[32:47], v[236:239], v[124:127], v[32:47]\n"
      "s_waitcnt lgkmcnt(0)\n"
      ".Lepa3_%=:\n"
      "s_nop 7 ; hz\n"
      "s_nop 1 ; hz\n"
      "v_pk_fma_f32 v[16:17], v[16:17], v[64:65], s[60:61]\n"
      "v_pk_fma_f32 v[18:19], v[18:19], v[66:67], s[60:61]\n"
      "v_pk_fma_f32 v[20:21], v[20:21], v[68:69], s[60:61]\n"
      "v_pk_fma_f32 v[22:23], v[22:23], v[70:71], s[60:61]\n"
      "v_pk_fma_f32 v[112:113], v[16:17], s[62:63], s[62:63] neg_lo:[0,0,1] neg_hi:[0,0,1]\n"
      "v_pk_fma_f32 v[114:115], v[18:19], s[62:63], s[62:63] neg_lo:[0,0,1] neg_hi:[0,0,1]\n"
      "v_pk_fma_f32 v[116:117], v[20:21], s[62:63], s[62:63] neg_lo:[0,0,1] neg_hi:[0,0,1]\n"
      "v_pk_fma_f32 v[118:119], v[22:23], s[62:63], s[62:63] neg_lo:[0,0,1] neg_hi:[0,0,1]\n"
      "v_exp_f32 v112, v112 clamp\n"
      "v_exp_f32 v113, v113 clamp\n"
      "v_exp_f32 v114, v114 clamp\n"
      "v_exp_f32 v115, v115 clamp\n"
      "v_exp_f32 v116, v116 clamp\n"
      "v_exp_f32 v117, v117 clamp\n"
      "v_exp_f32 v118, v118 clamp\n"
      "v_exp_f32 v119, v119 clamp\n"
      "v_pk_mul_f32 v[32:33], v[32:33], v[64:65]\n"
      "v_pk_mul_f32 v[34:35], v[34:35], v[66:67]\n"
      "v_pk_mul_f32 v[36:37], v[36:37], v[68:69]\n"
      "v_pk_mul_f32 v[38:39], v[38:39], v[70:71]\n"
      "v_max_f32 v16, v16, v112\n"
      "v_max_f32 v17, v17, v113\n"
      "v_max_f32 v18, v18, v114\n"
      "v_max_f32 v19, v19, v115\n"
      "v_max_f32 v20, v20, v116\n"
      "v_max_f32 v21, v21, v117\n"
      "v_max_f32 v22, v22, v118\n"
      "v_max_f32 v23, v23, v119\n"
      "s_mov_b32 exec_lo, 0x0fffffff\n"
      "s_mov_b32 exec_hi, 0x0fffffff\n"
      "v_cvt_pk_bf16_f32 v56, v32, v33\n"
      "v_cvt_pk_bf16_f32 v57, v34, v35\n"
      "v_cvt_pk_bf16_f32 v58, v36, v37\n"
      "v_cvt_pk_bf16_f32 v59, v38, v39\n"
      "s_mov_b64 exec, -1\n"
      "v_pk_add_f32 v[16:17], v[16:17], s[64:65]\n"
      "v_pk_add_f32 v[18:19], v[18:19], s[64:65]\n"
      "v_pk_add_f32 v[20:21], v[20:21], s[64:65]\n"
      "v_pk_add_f32 v[22:23], v[22:23], s[64:65]\n"
      "v_cvt_pk_bf16_f32 v48, v16, v17\n"
      "v_cvt_pk_bf16_f32 v49, v18, v19\n"
      "v_cvt_pk_bf16_f32 v50, v20, v21\n"
      "v_cvt_pk_bf16_f32 v51, v22, v23\n"
      "s_nop 1 ; hz\n"
      "v_mfma_f32_32x32x16_bf16 v[96:111], v[48:51], v[56:59], 0\n"
      "v_pk_fma_f32 v[24:25], v[24:25], v[72:73], s[60:61]\n"
      "v_pk_fma_f32 v[26:27], v[26:27], v[74:75], s[60:61]\n"
      "v_pk_fma_f32 v[28:29], v[28:29], v[76:77], s[60:61]\n"
      "v_pk_fma_f32 v[30:31], v[30:31], v[78:79], s[60:61]\n"
      "v_pk_fma_f32 v[120:121], v[24:25], s[62:63], s[62:63] neg_lo:[0,0,1] neg_hi:[0,0,1]\n"
      "v_pk_fma_f32 v[122:123], v[26:27], s[62:63], s[62:63] neg_lo:[0,0,1] neg_hi:[0,0,1]\n"
      "v_pk_fma_f32 v[124:125], v[28:29], s[62:63], s[62:63] neg_lo:[0,0,1] neg_hi:[0,0,1]\n"
      "v_pk_fma_f32 v[126:127], v[30:31], s[62:63], s[62:63] neg_lo:[0,0,1] neg_hi:[0,0,1]\n"
      "v_exp_f32 v120, v120 clamp\n"
      "v_exp_f32 v121, v121 clamp\n"
      "v_exp_f32 v122, v122 clamp\n"
      "v_exp_f32 v123, v123 clamp\n"
      "v_exp_f32 v124, v124 clamp\n"
      "v_exp_f32 v125, v125 clamp\n"
      "v_exp_f32 v126, v126 clamp\n"
      "v_exp_f32 v127, v127 clamp\n"
      "v_pk_mul_f32 v[40:41], v[40:41], v[72:73]\n"
      "v_pk_mul_f32 v[42:43], v[42:43], v[74:75]\n"
      "v_pk_mul_f32 v[44:45], v[44:45], v[76:77]\n"
      "v_pk_mul_f32 v[46:47], v[46:47], v[78:79]\n"
      "v_max_f32 v24, v24, v120\n"
      "v_max_f32 v25, v25, v121\n"
      "v_max_f32 v26, v26, v122\n"
      "v_max_f32 v27, v27, v123\n"
      "v_max_f32 v28, v28, v124\n"
      "v_max_f32 v29, v29, v125\n"
      "v_max_f32 v30, v30, v126\n"
      "v_max_f32 v31, v31, v127\n"
      "s_mov_b32 exec_lo, 0x0fffffff\n"
      "s_mov_b32 exec_hi, 0x0fffffff\n"
      "v_cvt_pk_bf16_f32 v60, v40, v41\n"
      "v_cvt_pk_bf16_f32 v61, v42, v43\n"
      "v_cvt_pk_bf16_f32 v62, v44, v45\n"
      "v_cvt_pk_bf16_f32 v63, v46, v47\n"
      "s_mov_b64 exec, -1\n"
      "v_pk_add_f32 v[24:25], v[24:25], s[64:65]\n"
      "v_pk_add_f32 v[26:27], v[26:27], s[64:65]\n"
      "v_pk_add_f32 v[28:29], v[28:29], s[64:65]\n"
      "v_pk_add_f32 v[30:31], v[30:31], s[64:65]\n"
      "v_cvt_pk_bf16_f32 v52, v24, v25\n"
      "v_cvt_pk_bf16_f32 v53, v26, v27\n"
      "v_cvt_pk_bf16_f32 v54, v28, v29\n"
      "v_cvt_pk_bf16_f32 v55, v30, v31\n"
      "s_nop 1 ; hz\n"
      "v_mfma_f32_32x32x16_bf16 v[96:111], v[52:55], v[60:63], v[96:111]\n"
      "s_nop 7 ; hz\n"
      "s_nop 3 ; hz\n"
      "ds_write_b128 v92, v[96:99] offset:0\n"
      "ds_write_b128 v92, v[100:103] offset:1024\n"
      "ds_write_b128 v92, v[104:107] offset:2048\n"
      "ds_write_b128 v92, v[108:111] offset:3072\n"
      "s_waitcnt lgkmcnt(0)\n"
      "s_barrier\n"
      "ds_read_b128 v[32:35], v93 offset:0\n"
      "ds_read_b128 v[36:39], v93 offset:4096\n"
      "ds_read_b128 v[40:43], v93 offset:8192\n"
      "ds_read_b128 v[44:47], v93 offset:12288\n"
      "ds_read2st64_b32 v[16:17], v1 offset0:0 offset1:16\n"
      "ds_read2st64_b32 v[18:19], v1 offset0:32 offset1:48\n"
      "s_waitcnt lgkmcnt(4)\n"
      "v_pk_add_f32 v[32:33], v[32:33], v[36:37]\n"
      "v_pk_add_f32 v[34:35], v[34:35], v[38:39]\n"
      "s_waitcnt lgkmcnt(2)\n"
      "v_pk_add_f32 v[40:41], v[40:41], v[44:45]\n"
      "v_pk_add_f32 v[42:43], v[42:43], v[46:47]\n"
      "v_pk_add_f32 v[32:33], v[32:33], v[40:41]\n"
      "v_pk_add_f32 v[34:35], v[34:35], v[42:43]\n"
      "s_waitcnt lgkmcnt(0)\n"
      "v_pk_add_f32 v[16:17], v[16:17], v[18:19]\n"
      "v_add_f32 v16, v16, v17\n"
      "v_cmp_class_f32 vcc, v16, s7\n"
      "s_cbranch_vccz .Lck28_%=\n"
      "s_cmp_eq_u32 s5, 0\n"
      "s_cbranch_scc1 .Lslowa24_%=\n"
      ".Lck28_%=:\n"
      "s_mov_b32 s5, 0\n"
      "v_cvt_pk_bf16_f32 v32, v32, v33\n"
      "v_cvt_pk_bf16_f32 v33, v34, v35\n"
      "buffer_store_dwordx2 v[32:33], v94, s[0:3], s34 offen sc1\n"
      "s_cmp_lg_u32 s17, 0\n"
      "s_cbranch_scc1 .Lqa29_%=\n"
      "s_lshl_b32 s28, s16, 10\n"
      "v_add_u32 v95, 57344, v95\n"
      "v_subrev_u32 v87, s28, v95\n"
      "v_permlane32_swap_b32 v224, v226\n"
      "v_permlane32_swap_b32 v225, v227\n"
      "v_permlane32_swap_b32 v228, v230\n"
      "v_permlane32_swap_b32 v229, v231\n"
      "v_permlane32_swap_b32 v232, v234\n"
      "v_permlane32_swap_b32 v233, v235\n"
      "v_permlane32_swap_b32 v236, v238\n"
      "v_permlane32_swap_b32 v237, v239\n"
      "s_waitcnt vmcnt(1)\n"
      "s_barrier\n"
      "ds_read_b64_tr_b16 v[16:17], v91 offset:12288\n"
      "ds_read_b64_tr_b16 v[18:19], v91 offset:12800\n"
      "ds_read_b64_tr_b16 v[20:21], v91 offset:13312\n"
      "ds_read_b64_tr_b16 v[22:23], v91 offset:13824\n"
      "ds_read_b64_tr_b16 v[24:25], v91 offset:14336\n"
      "ds_read_b64_tr_b16 v[26:27], v91 offset:14848\n"
      "v_readfirstlane_b32 s12, v12\n"
      "v_readfirstlane_b32 s13, v13\n"
      "s_add_u32 s12, s12, 1\n"
      "s_addc_u32 s13, s13, 0\n"
      "v_mov_b32 v12, s12\n"
      "v_mov_b32 v13, s13\n"
      "v_mov_b32 v3, v0\n"
      "s_waitcnt lgkmcnt(0)\n"
      "s_mov_b32 exec_lo, 0xf0000000\n"
      "s_mov_b32 exec_hi, 0xf0000000\n"
      "v_mov_b64 v[16:17], 0\n"
      "v_mov_b64 v[18:19], 0\n"
      "v_mov_b64 v[20:21], 0\n"
      "v_mov_b64 v[22:23], 0\n"
      "s_mov_b64 exec, -1\n"
      "buffer_store_dwordx4 v[16:19], v0, s[0:3], s75 offen offset:0 sc1\n"
      "buffer_store_dwordx4 v[20:23], v0, s[0:3], s98 offen offset:0 sc1\n"
      "buffer_store_dwordx4 v[24:27], v0, s[0:3], s99 offen offset:0 sc1\n"
      "ds_read_b64_tr_b16 v[32:33], v87 offset:0\n"
      "ds_read_b64_tr_b16 v[34:35], v87 offset:512\n"
      "ds_read_b64_tr_b16 v[36:37], v87 offset:1024\n"
      "ds_read_b64_tr_b16 v[38:39], v87 offset:1536\n"
      "ds_read_b64_tr_b16 v[40:41], v87 offset:2048\n"
      "ds_read_b64_tr_b16 v[42:43], v87 offset:2560\n"
      "ds_read_b64_tr_b16 v[44:45], v87 offset:3072\n"
      "ds_read_b64_tr_b16 v[46:47], v87 offset:3584\n"
      "s_waitcnt lgkmcnt(6)\n"
      "v_mfma_f32_32x32x16_bf16 v[48:63], v[32:35], v[128:131], 0\n"
      "ds_read_b64_tr_b16 v[112:113], v87 offset:4096\n"
      "ds_read_b64_tr_b16 v[114:115], v87 offset:4608\n"
      "ds_read_b64_tr_b16 a[56:57], v95 offset:0\n"
      "ds_read_b64_tr_b16 a[58:59], v95 offset:512\n"
      "s_waitcnt lgkmcnt(8)\n"
      "v_mfma_f32_32x32x16_bf16 v[48:63], v[36:39], v[132:135], v[48:63]\n"
      "ds_read_b64_tr_b16 v[116:117], v87 offset:5120\n"
      "ds_read_b64_tr_b16 v[118:119], v87 offset:5632\n"
      "ds_read_b64_tr_b16 a[60:61], v95 offset:4096\n"
      "ds_read_b64_tr_b16 a[62:63], v95 offset:4608\n"
      "s_waitcnt lgkmcnt(10)\n"
      "v_mfma_f32_32x32x16_bf16 v[48:63], v[40:43], v[136:139], v[48:63]\n"
      "ds_read_b64_tr_b16 v[120:121], v87 offset:6144\n"
      "ds_read_b64_tr_b16 v[122:123], v87 offset:6656\n"
      "ds_read_b64_tr_b16 a[64:65], v95 offset:8192\n"
      "ds_read_b64_tr_b16 a[66:67], v95 offset:8704\n"
      "s_waitcnt lgkmcnt(12)\n"
      "v_mfma_f32_32x32x16_bf16 v[48:63], v[44:47], v[140:143], v[48:63]\n"
      "ds_read_b64_tr_b16 v[124:125], v87 offset:7168\n"
      "ds_read_b64_tr_b16 v[126:127], v87 offset:7680\n"
      "ds_read_b64_tr_b16 a[68:69], v95 offset:12288\n"
      "ds_read_b64_tr_b16 a[70:71], v95 offset:12800\n"
      "s_waitcnt lgkmcnt(12)\n"
      "v_mfma_f32_32x32x16_bf16 a[208:223], a[192:195], a[56:59], 0\n"
      "v_mfma_f32_32x32x16_bf16 v[48:63], v[112:115], v[144:147], v[48:63]\n"
      "ds_read_b64_tr_b16 v[32:33], v87 offset:8192\n"
      "ds_read_b64_tr_b16 v[34:35], v87 offset:8704\n"
      "ds_read_b64_tr_b16 a[72:73], v95 offset:16384\n"
      "ds_read_b64_tr_b16 a[74:75], v95 offset:16896\n"
      "s_waitcnt lgkmcnt(12)\n"
      "v_mfma_f32_32x32x16_bf16 a[208:223], a[192:195], a[60:63], a[208:223]\n"
      "v_mfma_f32_32x32x16_bf16 v[48:63], v[116:119], v[148:151], v[48:63]\n"
      "ds_read_b64_tr_b16 v[36:37], v87 offset:9216\n"
      "ds_read_b64_tr_b16 v[38:39], v87 offset:9728\n"
      "ds_read_b64_tr_b16 a[76:77], v95 offset:20480\n"
      "ds_read_b64_tr_b16 a[78:79], v95 offset:20992\n"
      "s_waitcnt lgkmcnt(12)\n"
      "v_mfma_f32_32x32x16_bf16 a[208:223], a[192:195], a[64:67], a[208:223]\n"
      "v_mfma_f32_32x32x16_bf16 v[48:63], v[120:123], v[152:155], v[48:63]\n"
      "ds_read_b64_tr_b16 v[40:41], v87 offset:10240\n"
      "ds_read_b64_tr_b16 v[42:43], v87 offset:10752\n"
      "ds_read_b64_tr_b16 a[80:81], v95 offset:24576\n"
      "ds_read_b64_tr_b16 a[82:83], v95 offset:25088\n"
      "s_waitcnt lgkmcnt(12)\n"
      "v_mfma_f32_32x32x16_bf16 a[208:223], a[192:195], a[68:71], a[208:223]\n"
      "v_mfma_f32_32x32x16_bf16 v[48:63], v[124:127], v[156:159], v[48:63]\n"
      "ds_read_b64_tr_b16 v[44:45], v87 offset:11264\n"
      "ds_read_b64_tr_b16 v[46:47], v87 offset:11776\n"
      "s_waitcnt lgkmcnt(10)\n"
      "v_mfma_f32_32x32x16_bf16 a[208:223], a[192:195], a[72:75], a[208:223]\n"
      "v_mfma_f32_32x32x16_bf16 v[48:63], v[32:35], v[160:163], v[48:63]\n"
      "ds_read_b64_tr_b16 v[112:113], v87 offset:12288\n"
      "ds_read_b64_tr_b16 v[114:115], v87 offset:12800\n"
      "s_waitcnt lgkmcnt(8)\n"
      "v_mfma_f32_32x32x16_bf16 a[208:223], a[192:195], a[76:79], a[208:223]\n"
      "v_mfma_f32_32x32x16_bf16 v[48:63], v[36:39], v[164:167], v[48:63]\n"
      "ds_read_b64_tr_b16 v[116:117], v87 offset:13312\n"
      "ds_read_b64_tr_b16 v[118:119], v87 offset:13824\n"
      "s_waitcnt lgkmcnt(6)\n"
      "v_mfma_f32_32x32x16_bf16 a[208:223], a[192:195], a[80:83], a[208:223]\n"
      "v_mfma_f32_32x32x16_bf16 v[48:63], v[40:43], v[168:171], v[48:63]\n"
      "ds_read_b64_tr_b16 v[120:121], v87 offset:14336\n"
      "ds_read_b64_tr_b16 v[122:123], v87 offset:14848\n"
      "s_lshl_b32 s29, s15, 6\n"
      "s_add_u32 s19, s19, s29\n"
      "s_lshl_b32 s28, s15, 3\n"
      "s_lshl_b32 s29, s82, 4\n"
      "s_sub_u32 s28, s28, s29\n"
      "s_cselect_b32 s29, 112, 0\n"
      "s_add_u32 s28, s28, s29\n"
      "s_set_gpr_idx_on s28, gpr_idx(SRC0)\n"
      "s_nop 1\n"
      "v_mov_b32 v241, v128\n"
      "v_mov_b32 v243, v129\n"
      "v_mov_b32 v245, v130\n"
      "v_mov_b32 v247, v131\n"
      "v_mov_b32 v249, v132\n"
      "v_mov_b32 v251, v133\n"
      "v_mov_b32 v253, v134\n"
      "v_mov_b32 v255, v135\n"
      "s_set_gpr_idx_off\n"
      "s_nop 1\n"
      "v_lshlrev_b32 v240, 16, v241\n"
      "v_and_b32 v241, 0xffff0000, v241\n"
      "v_lshlrev_b32 v242, 16, v243\n"
      "v_and_b32 v243, 0xffff0000, v243\n"
      "s_waitcnt lgkmcnt(6)\n"
      "v_mfma_f32_32x32x16_bf16 v[48:63], v[44:47], v[172:175], v[48:63]\n"
      "ds_read_b64_tr_b16 v[124:125], v87 offset:15360\n"
      "ds_read_b64_tr_b16 v[126:127], v87 offset:15872\n"
      "v_lshlrev_b32 v244, 16, v245\n"
      "v_and_b32 v245, 0xffff0000, v245\n"
      "v_lshlrev_b32 v246, 16, v247\n"
      "v_and_b32 v247, 0xffff0000, v247\n"
      "v_lshlrev_b32 v248, 16, v249\n"
      "v_and_b32 v249, 0xffff0000, v249\n"
      "s_waitcnt lgkmcnt(6)\n"
      "v_mfma_f32_32x32x16_bf16 v[48:63], v[112:115], v[176:179], v[48:63]\n"
      "ds_read_b64_tr_b16 v[32:33], v87 offset:16384\n"
      "ds_read_b64_tr_b16 v[34:35], v87 offset:16896\n"
      "v_lshlrev_b32 v250, 16, v251\n"
      "v_and_b32 v251, 0xffff0000, v251\n"
      "v_lshlrev_b32 v252, 16, v253\n"
      "v_and_b32 v253, 0xffff0000, v253\n"
      "v_lshlrev_b32 v254, 16, v255\n"
      "v_and_b32 v255, 0xffff0000, v255\n"
      "s_waitcnt lgkmcnt(6)\n"
      "v_mfma_f32_32x32x16_bf16 v[48:63], v[116:119], v[180:183], v[48:63]\n"
      "ds_read_b64_tr_b16 v[36:37], v87 offset:17408\n"
      "ds_read_b64_tr_b16 v[38:39], v87 offset:17920\n"
      "ds_write_b32 v8, a208 offset:256\n"
      "s_waitcnt lgkmcnt(7)\n"
      "v_mfma_f32_32x32x16_bf16 v[48:63], v[120:123], v[184:187], v[48:63]\n"
      "ds_read_b64_tr_b16 v[40:41], v87 offset:18432\n"
      "ds_read_b64_tr_b16 v[42:43], v87 offset:18944\n"
      "s_waitcnt lgkmcnt(7)\n"
      "v_mfma_f32_32x32x16_bf16 v[48:63], v[124:127], v[188:191], v[48:63]\n"
      "ds_read_b64_tr_b16 v[44:45], v87 offset:19456\n"
      "ds_read_b64_tr_b16 v[46:47], v87 offset:19968\n"
      "s_waitcnt lgkmcnt(4)\n"
      "s_waitcnt vmcnt(0)\n"
      "s_barrier\n"
      "s_cmp_eq_u32 s16, 0\n"
      "s_cbranch_scc0 .Lif31_%=\n"
      "s_lshl_b32 s28, s14, 3\n"
      "s_add_u32 s28, s28, s26\n"
      "s_add_u32 s28, s28, s27\n"
      "s_mov_b64 exec, 0xffff\n"
      "buffer_store_dwordx2 v[12:13], v4, s[0:3], s28 offen sc1\n"
      "s_mov_b64 exec, -1\n"
      ".Lif31_%=:\n"
      "v_lshrrev_b32 v67, 1, v6\n"
      "v_add_u32 v67, 161792, v67\n"
      "ds_read_b32 v64, v67 offset:256\n"
      "ds_read_b32 v65, v67 offset:640\n"
      "ds_read_b32 v66, v67 offset:1024\n"
      "ds_read_b32 v67, v67 offset:1408\n"
      "v_mfma_f32_32x32x16_bf16 v[48:63], v[32:35], v[192:195], v[48:63]\n"
      "ds_read_b64_tr_b16 v[112:113], v87 offset:20480\n"
      "ds_read_b64_tr_b16 v[114:115], v87 offset:20992\n"
      "s_cmp_lt_u32 s15, 14\n"
      "s_cselect_b32 s79, 0, 2147483648\n"
      "s_lshl_b32 s78, s15, 11\n"
      "s_add_u32 s76, s78, s20\n"
      "s_or_b32 s76, s76, s79\n"
      "s_lshl_b32 s77, s15, 8\n"
      "s_add_u32 s77, s77, s21\n"
      "s_or_b32 s77, s77, s79\n"
      "v_mfma_f32_32x32x16_bf16 v[48:63], v[36:39], v[196:199], v[48:63]\n"
      "ds_read_b64_tr_b16 v[116:117], v87 offset:21504\n"
      "ds_read_b64_tr_b16 v[118:119], v87 offset:22016\n"
      "s_add_u32 s78, s78, s22\n"
      "s_lshl_b32 s79, s14, 12\n"
      "s_add_u32 s79, s79, s24\n"
      "s_lshl_b32 s80, s14, 9\n"
      "s_add_u32 s80, s80, s23\n"
      "s_waitcnt lgkmcnt(10)\n"
      "v_mfma_f32_32x32x16_bf16 v[48:63], v[40:43], v[200:203], v[48:63]\n"
      "ds_read_b64_tr_b16 v[120:121], v87 offset:22528\n"
      "ds_read_b64_tr_b16 v[122:123], v87 offset:23040\n"
      "v_mov_b64 v[28:29], -1\n"
      "v_mov_b64 v[30:31], -1\n"
      "s_waitcnt lgkmcnt(10)\n"
      "v_mfma_f32_32x32x16_bf16 v[48:63], v[44:47], v[204:207], v[48:63]\n"
      "ds_read_b64_tr_b16 v[124:125], v87 offset:23552\n"
      "ds_read_b64_tr_b16 v[126:127], v87 offset:24064\n"
      "s_add_u32 s31, s78, 0\n"
      "buffer_store_dwordx4 v[28:31], v0, s[0:3], s31 offen offset:0 sc1\n"
      "s_waitcnt lgkmcnt(6)\n"
      "v_mfma_f32_32x32x16_bf16 v[48:63], v[112:115], v[208:211], v[48:63]\n"
      "ds_read_b64_tr_b16 v[32:33], v87 offset:24576\n"
      "ds_read_b64_tr_b16 v[34:35], v87 offset:25088\n"
      "s_add_u32 s31, s78, 0\n"
      "buffer_store_dwordx4 v[28:31], v0, s[0:3], s31 offen offset:1024 sc1\n"
      "s_waitcnt lgkmcnt(6)\n"
      "v_mfma_f32_32x32x16_bf16 v[48:63], v[116:119], v[212:215], v[48:63]\n"
      "ds_read_b64_tr_b16 v[36:37], v87 offset:25600\n"
      "ds_read_b64_tr_b16 v[38:39], v87 offset:26112\n"
      "s_add_u32 s31, s76, 114688\n"
      "buffer_store_dwordx4 v[28:31], v0, s[0:3], s31 offen offset:0 sc1\n"
      "s_waitcnt lgkmcnt(6)\n"
      "v_mfma_f32_32x32x16_bf16 v[48:63], v[120:123], v[216:219], v[48:63]\n"
      "ds_read_b64_tr_b16 v[40:41], v87 offset:26624\n"
      "ds_read_b64_tr_b16 v[42:43], v87 offset:27136\n"
      "s_add_u32 s31, s76, 114688\n"
      "buffer_store_dwordx4 v[28:31], v0, s[0:3], s31 offen offset:1024 sc1\n"
      "s_waitcnt lgkmcnt(6)\n"
      "v_mfma_f32_32x32x16_bf16 v[48:63], v[124:127], v[220:223], v[48:63]\n"
      "ds_read_b64_tr_b16 v[44:45], v87 offset:27648\n"
      "ds_read_b64_tr_b16 v[46:47], v87 offset:28160\n"
      "s_add_u32 s31, s77, 16384\n"
      "s_mov_b32 exec_lo, 0xffff\n"
      "s_mov_b32 exec_hi, 0\n"
      "buffer_store_dwordx4 v[28:31], v0, s[0:3], s31 offen offset:0 sc1\n"
      "s_mov_b64 exec, -1\n"
      "s_waitcnt lgkmcnt(6)\n"
      "v_mfma_f32_32x32x16_bf16 v[48:63], v[32:35], v[224:227], v[48:63]\n"
      "s_lshr_b32 s28, s16, 1\n"
      "s_lshl_b32 s28, s28, 10\n"
      "s_and_b32 s29, s16, 1\n"
      "s_lshl_b32 s29, s29, 3\n"
      "s_add_u32 s28, s28, s29\n"
      "s_add_u32 s34, s28, 159744\n"
      "s_waitcnt lgkmcnt(4)\n"
      "v_mfma_f32_32x32x16_bf16 v[48:63], v[36:39], v[228:231], v[48:63]\n"
      "v_lshrrev_b32 v93, 9, v0\n"
      "v_lshlrev_b32 v93, 4, v93\n"
      "v_lshl_add_u32 v92, v6, 2, v93\n"
      "v_add_u32 v92, s34, v92\n"
      "v_add_u32 v93, 159744, v0\n"
      "s_waitcnt lgkmcnt(2)\n"
      "v_mfma_f32_32x32x16_bf16 v[48:63], v[40:43], v[232:235], v[48:63]\n"
      "s_mov_b32 s68, 0x358637bd\n"
      "s_mov_b32 s69, 0\n"
      "s_mov_b32 s4, 0\n"
      "s_waitcnt lgkmcnt(0)\n"
      "v_mfma_f32_32x32x16_bf16 v[48:63], v[44:47], v[236:239], v[48:63]\n"
      "s_waitcnt lgkmcnt(0)\n"
      "v_add_f32 v64, v64, v65\n"
      "v_add_f32 v66, v66, v67\n"
      "v_add_f32 v64, v64, v66\n"
      "v_cvt_pk_bf16_f32 v65, v64, v64\n"
      "v_and_b32 v66, 0xffff0000, v65\n"
      "v_sub_f32 v66, v64, v66\n"
      "v_cvt_pk_bf16_f32 v66, v66, 0\n"
      "s_mov_b32 exec_hi, 0\n"
      "v_accvgpr_write_b32 a176, v65\n"
      "v_accvgpr_write_b32 a177, v66\n"
      "s_mov_b32 exec_hi, -1\n"
      "s_cmp_eq_u32 s16, 0\n"
      "s_cbranch_scc0 .Lif32_%=\n"
      "s_add_u32 s28, s26, s27\n"
      "s_lshl_b32 s29, s14, 8\n"
      "s_add_u32 s28, s28, s29\n"
      "s_mov_b32 exec_lo, 65535\n"
      "s_mov_b32 exec_hi, 0\n"
      "buffer_load_dwordx2 v[90:91], v6, s[0:3], s28 offen sc1\n"
      "s_mov_b64 exec, -1\n"
      ".Lif32_%=:\n"
      "v_mfma_f32_32x32x16_bf16 v[48:63], a[176:179], v[80:83], v[48:63]\n"
      "s_branch .Lqb30_%=\n"
      ".Lqa29_%=:\n"
      "s_barrier\n"
      "s_cmp_eq_u32 s16, 0\n"
      "s_cbranch_scc0 .Lif33_%=\n"
      "s_lshl_b32 s28, s14, 3\n"
      "s_add_u32 s28, s28, s26\n"
      "s_add_u32 s28, s28, s27\n"
      "s_mov_b64 exec, 0xffff\n"
      "buffer_store_dwordx2 v[12:13], v4, s[0:3], s28 offen sc1\n"
      "s_mov_b64 exec, -1\n"
      ".Lif33_%=:\n"
      "ds_read_b128 v[32:35], v2 offset:0\n"
      "ds_read_b128 v[36:39], v2 offset:1024\n"
      "ds_read_b128 v[40:43], v2 offset:2048\n"
      "ds_read_b128 v[44:47], v2 offset:3072\n"
      "ds_read_b128 v[112:115], v2 offset:4096\n"
      "v_mfma_f32_32x32x16_bf16 v[48:63], a[176:179], v[80:83], 0\n"
      "s_waitcnt lgkmcnt(4)\n"
      "v_mfma_f32_32x32x16_bf16 v[48:63], v[32:35], v[128:131], v[48:63]\n"
      "ds_read_b128 v[116:119], v2 offset:5120\n"
      "s_waitcnt lgkmcnt(4)\n"
      "v_mfma_f32_32x32x16_bf16 v[48:63], v[36:39], v[132:135], v[48:63]\n"
      "ds_read_b128 v[120:123], v2 offset:6144\n"
      "s_waitcnt lgkmcnt(4)\n"
      "v_mfma_f32_32x32x16_bf16 v[48:63], v[40:43], v[136:139], v[48:63]\n"
      "ds_read_b128 v[124:127], v2 offset:7168\n"
      "s_waitcnt lgkmcnt(4)\n"
      "v_mfma_f32_32x32x16_bf16 v[48:63], v[44:47], v[140:143], v[48:63]\n"
      "ds_read_b128 v[32:35], v2 offset:8192\n"
      "s_waitcnt lgkmcnt(4)\n"
      "v_mfma_f32_32x32x16_bf16 v[48:63], v[112:115], v[144:147], v[48:63]\n"
      "ds_read_b128 v[36:39], v2 offset:9216\n"
      "s_waitcnt lgkmcnt(4)\n"
      "v_mfma_f32_32x32x16_bf16 v[48:63], v[116:119], v[148:151], v[48:63]\n"
      "ds_read_b128 v[40:43], v2 offset:10240\n"
      "s_waitcnt lgkmcnt(4)\n"
      "v_mfma_f32_32x32x16_bf16 v[48:63], v[120:123], v[152:155], v[48:63]\n"
      "ds_read_b128 v[44:47], v2 offset:11264\n"
      "s_waitcnt lgkmcnt(4)\n"
      "v_mfma_f32_32x32x16_bf16 v[48:63], v[124:127], v[156:159], v[48:63]\n"
      "ds_read_b128 v[112:115], v2 offset:12288\n"
      "s_waitcnt lgkmcnt(4)\n"
      "v_mfma_f32_32x32x16_bf16 v[48:63], v[32:35], v[160:163], v[48:63]\n"
      "ds_read_b128 v[116:119], v2 offset:13312\n"
      "s_waitcnt lgkmcnt(4)\n"
      "v_mfma_f32_32x32x16_bf16 v[48:63], v[36:39], v[164:167], v[48:63]\n"
      "ds_read_b128 v[120:123], v2 offset:14336\n"
      "s_waitcnt lgkmcnt(4)\n"
      "v_mfma_f32_32x32x16_bf16 v[48:63], v[40:43], v[168:171], v[48:63]\n"
      "ds_read_b128 v[124:127], v2 offset:15360\n"
      "s_waitcnt lgkmcnt(4)\n"
      "v_mfma_f32_32x32x16_bf16 v[48:63], v[44:47], v[172:175], v[48:63]\n"
      "ds_read_b128 v[32:35], v2 offset:16384\n"
      "s_waitcnt lgkmcnt(4)\n"
      "v_mfma_f32_32x32x16_bf16 v[48:63], v[112:115], v[176:179], v[48:63]\n"
      "ds_read_b128 v[36:39], v2 offset:17408\n"
      "s_waitcnt lgkmcnt(4)\n"
      "v_mfma_f32_32x32x16_bf16 v[48:63], v[116:119], v[180:183], v[48:63]\n"
      "ds_read_b128 v[40:43], v2 offset:18432\n"
      "s_waitcnt lgkmcnt(4)\n"
      "v_mfma_f32_32x32x16_bf16 v[48:63], v[120:123], v[184:187], v[48:63]\n"
      "ds_read_b128 v[44:47], v2 offset:19456\n"
      "s_waitcnt lgkmcnt(4)\n"
      "v_mfma_f32_32x32x16_bf16 v[48:63], v[124:127], v[188:191], v[48:63]\n"
      "ds_read_b128 v[112:115], v2 offset:20480\n"
      "s_waitcnt lgkmcnt(4)\n"
      "v_mfma_f32_32x32x16_bf16 v[48:63], v[32:35], v[192:195], v[48:63]\n"
      "ds_read_b128 v[116:119], v2 offset:21504\n"
      "s_waitcnt lgkmcnt(4)\n"
      "v_mfma_f32_32x32x16_bf16 v[48:63], v[36:39], v[196:199], v[48:63]\n"
      "ds_read_b128 v[120:123], v2 offset:22528\n"
      "s_waitcnt lgkmcnt(4)\n"
      "v_mfma_f32_32x32x16_bf16 v[48:63], v[40:43], v[200:203], v[48:63]\n"
      "ds_read_b128 v[124:127], v2 offset:23552\n"
      "s_waitcnt lgkmcnt(4)\n"
      "v_mfma_f32_32x32x16_bf16 v[48:63], v[44:47], v[204:207], v[48:63]\n"
      "ds_read_b128 v[32:35], v2 offset:24576\n"
      "s_lshr_b32 s28, s16, 1\n"
      "s_lshl_b32 s28, s28, 10\n"
      "s_and_b32 s29, s16, 1\n"
      "s_lshl_b32 s29, s29, 3\n"
      "s_add_u32 s28, s28, s29\n"
      "s_add_u32 s34, s28, 159744\n"
      "s_waitcnt lgkmcnt(4)\n"
      "v_mfma_f32_32x32x16_bf16 v[48:63], v[112:115], v[208:211], v[48:63]\n"
      "ds_read_b128 v[36:39], v2 offset:25600\n"
      "v_lshrrev_b32 v93, 9, v0\n"
      "v_lshlrev_b32 v93, 4, v93\n"
      "v_lshl_add_u32 v92, v6, 2, v93\n"
      "v_add_u32 v92, s34, v92\n"
      "v_add_u32 v93, 159744, v0\n"
      "s_waitcnt lgkmcnt(4)\n"
      "v_mfma_f32_32x32x16_bf16 v[48:63], v[116:119], v[212:215], v[48:63]\n"
      "ds_read_b128 v[40:43], v2 offset:26624\n"
      "s_mov_b32 s68, 0x358637bd\n"
      "s_mov_b32 s69, 0\n"
      "s_mov_b32 s4, 0\n"
      "s_waitcnt lgkmcnt(4)\n"
      "v_mfma_f32_32x32x16_bf16 v[48:63], v[120:123], v[216:219], v[48:63]\n"
      "ds_read_b128 v[44:47], v2 offset:27648\n"
      "s_cmp_eq_u32 s16, 0\n"
      "s_cbranch_scc0 .Lif34_%=\n"
      "s_add_u32 s28, s26, s27\n"
      "s_lshl_b32 s29, s14, 8\n"
      "s_add_u32 s28, s28, s29\n"
      "s_mov_b32 exec_lo, 65535\n"
      "s_mov_b32 exec_hi, 0\n"
      "buffer_load_dwordx2 v[90:91], v6, s[0:3], s28 offen sc1\n"
      "s_mov_b64 exec, -1\n"
      ".Lif34_%=:\n"
      "s_waitcnt lgkmcnt(4)\n"
      "v_mfma_f32_32x32x16_bf16 v[48:63], v[124:127], v[220:223], v[48:63]\n"
      "s_waitcnt lgkmcnt(3)\n"
      "v_mfma_f32_32x32x16_bf16 v[48:63], v[32:35], v[224:227], v[48:63]\n"
      "s_waitcnt lgkmcnt(2)\n"
      "v_mfma_f32_32x32x16_bf16 v[48:63], v[36:39], v[228:231], v[48:63]\n"
      "s_waitcnt lgkmcnt(1)\n"
      "v_mfma_f32_32x32x16_bf16 v[48:63], v[40:43], v[232:235], v[48:63]\n"
      "s_waitcnt lgkmcnt(0)\n"
      "v_mfma_f32_32x32x16_bf16 v[48:63], v[44:47], v[236:239], v[48:63]\n"
      ".Lqb30_%=:\n"
      "s_cmp_eq_u32 s16, 0\n"
      "s_cbranch_scc0 .Lif38_%=\n"
      "s_add_u32 s28, s26, s27\n"
      "s_lshl_b32 s29, s14, 8\n"
      "s_add_u32 s28, s28, s29\n"
      "s_mov_b32 s71, 0x10000\n"
      "s_mov_b32 exec_lo, 65535\n"
      "s_mov_b32 exec_hi, 0\n"
      "s_branch .Lpc37_%=\n"
      ".Lpl35_%=:\n"
      "buffer_load_dwordx2 v[90:91], v6, s[0:3], s28 offen sc1\n"
      ".Lpc37_%=:\n"
      "s_waitcnt vmcnt(0)\n"
      "v_cmp_eq_u64 vcc, s[12:13], v[90:91]\n"
      "s_nop 0 ; hz\n"
      "s_cmp_eq_u64 vcc, exec\n"
      "s_cbranch_scc1 .Lpo36_%=\n"
      "s_sleep 1\n"
      "s_sub_u32 s71, s71, 1\n"
      "s_cmp_lg_u32 s71, 0\n"
      "s_cbranch_scc1 .Lpl35_%=\n"
      ".Lpo36_%=:\n"
      "s_mov_b64 exec, -1\n"
      ".Lif38_%=:\n"
      "s_add_u32 s27, s27, 4096\n"
      "s_waitcnt vmcnt(0) lgkmcnt(0)\n"
      "s_barrier\n"
      ".Lkvhead39_%=:\n"
      "s_mov_b32 s30, s23\n"
      "buffer_load_dwordx4 v[128:131], v0, s[0:3], s30 offen offset:0 sc1\n"
      "buffer_load_dwordx4 v[132:135], v0, s[0:3], s30 offen offset:1024 sc1\n"
      "buffer_load_dwordx4 v[136:139], v0, s[0:3], s30 offen offset:2048 sc1\n"
      "buffer_load_dwordx4 v[140:143], v0, s[0:3], s30 offen offset:3072 sc1\n"
      "s_add_u32 s30, s30, 0x1000\n"
      "buffer_load_dwordx4 v[144:147], v0, s[0:3], s30 offen offset:0 sc1\n"
      "buffer_load_dwordx4 v[148:151], v0, s[0:3], s30 offen offset:1024 sc1\n"
      "buffer_load_dwordx4 v[152:155], v0, s[0:3], s30 offen offset:2048 sc1\n"
      "buffer_load_dwordx4 v[156:159], v0, s[0:3], s30 offen offset:3072 sc1\n"
      "s_cmp_eq_u32 s69, 0\n"
      "s_cbranch_scc0 .Lif40_%=\n"
      "v_mov_b32 v22, 0\n"
      "v_mov_b32 v23, 0\n"
      "v_pk_fma_f32 v[48:49], v[48:49], v[88:89], s[60:61]\n"
      "v_pk_fma_f32 v[50:51], v[50:51], v[88:89], s[60:61]\n"
      "v_pk_fma_f32 v[52:53], v[52:53], v[88:89], s[60:61]\n"
      "v_pk_fma_f32 v[54:55], v[54:55], v[88:89], s[60:61]\n"
      "v_pk_fma_f32 v[56:57], v[56:57], v[88:89], s[60:61]\n"
      "v_pk_fma_f32 v[58:59], v[58:59], v[88:89], s[60:61]\n"
      "v_pk_fma_f32 v[60:61], v[60:61], v[88:89], s[60:61]\n"
      "v_pk_fma_f32 v[62:63], v[62:63], v[88:89], s[60:61]\n"
      "v_pk_fma_f32 v[112:113], v[48:49], s[62:63], s[62:63] neg_lo:[0,0,1] neg_hi:[0,0,1]\n"
      "v_pk_fma_f32 v[114:115], v[50:51], s[62:63], s[62:63] neg_lo:[0,0,1] neg_hi:[0,0,1]\n"
      "v_pk_fma_f32 v[116:117], v[52:53], s[62:63], s[62:63] neg_lo:[0,0,1] neg_hi:[0,0,1]\n"
      "v_pk_fma_f32 v[118:119], v[54:55], s[62:63], s[62:63] neg_lo:[0,0,1] neg_hi:[0,0,1]\n"
      "v_pk_fma_f32 v[120:121], v[56:57], s[62:63], s[62:63] neg_lo:[0,0,1] neg_hi:[0,0,1]\n"
      "v_pk_fma_f32 v[122:123], v[58:59], s[62:63], s[62:63] neg_lo:[0,0,1] neg_hi:[0,0,1]\n"
      "v_pk_fma_f32 v[124:125], v[60:61], s[62:63], s[62:63] neg_lo:[0,0,1] neg_hi:[0,0,1]\n"
      "v_pk_fma_f32 v[126:127], v[62:63], s[62:63], s[62:63] neg_lo:[0,0,1] neg_hi:[0,0,1]\n"
      "v_exp_f32 v112, v112 clamp\n"
      "v_exp_f32 v113, v113 clamp\n"
      "v_exp_f32 v114, v114 clamp\n"
      "v_exp_f32 v115, v115 clamp\n"
      "v_exp_f32 v116, v116 clamp\n"
      "v_exp_f32 v117, v117 clamp\n"
      "v_exp_f32 v118, v118 clamp\n"
      "v_exp_f32 v119, v119 clamp\n"
      "v_exp_f32 v120, v120 clamp\n"
      "v_exp_f32 v121, v121 clamp\n"
      "v_exp_f32 v122, v122 clamp\n"
      "v_exp_f32 v123, v123 clamp\n"
      "v_exp_f32 v124, v124 clamp\n"
      "v_exp_f32 v125, v125 clamp\n"
      "v_exp_f32 v126, v126 clamp\n"
      "v_exp_f32 v127, v127 clamp\n"
      "v_max_f32 v48, v48, v112\n"
      "v_max_f32 v49, v49, v113\n"
      "v_max_f32 v50, v50, v114\n"
      "v_max_f32 v51, v51, v115\n"
      "v_max_f32 v52, v52, v116\n"
      "v_max_f32 v53, v53, v117\n"
      "v_max_f32 v54, v54, v118\n"
      "v_max_f32 v55, v55, v119\n"
      "v_max_f32 v56, v56, v120\n"
      "v_max_f32 v57, v57, v121\n"
      "v_max_f32 v58, v58, v122\n"
      "v_max_f32 v59, v59, v123\n"
      "v_max_f32 v60, v60, v124\n"
      "v_max_f32 v61, v61, v125\n"
      "v_max_f32 v62, v62, v126\n"
      "v_max_f32 v63, v63, v127\n"
      "v_pk_add_f32 v[48:49], v[48:49], s[64:65]\n"
      "v_pk_add_f32 v[50:51], v[50:51], s[64:65]\n"
      "v_pk_add_f32 v[52:53], v[52:53], s[64:65]\n"
      "v_pk_add_f32 v[54:55], v[54:55], s[64:65]\n"
      "v_pk_add_f32 v[56:57], v[56:57], s[64:65]\n"
      "v_pk_add_f32 v[58:59], v[58:59], s[64:65]\n"
      "v_pk_add_f32 v[60:61], v[60:61], s[64:65]\n"
      "v_pk_add_f32 v[62:63], v[62:63], s[64:65]\n"
      "v_cvt_pk_bf16_f32 v16, v48, v49\n"
      "v_cvt_pk_bf16_f32 v17, v50, v51\n"
      "v_cvt_pk_bf16_f32 v18, v52, v53\n"
      "v_cvt_pk_bf16_f32 v19, v54, v55\n"
      "v_cvt_pk_bf16_f32 v20, v56, v57\n"
      "v_cvt_pk_bf16_f32 v21, v58, v59\n"
      "s_mov_b32 exec_hi, 0\n"
      "v_cvt_pk_bf16_f32 v22, v60, v61\n"
      "v_cvt_pk_bf16_f32 v23, v62, v63\n"
      "s_mov_b32 exec_hi, -1\n"
      ".Lif40_%=:\n"
      "s_cmp_lg_u32 s17, 0\n"
      "s_cbranch_scc1 .Lk041_%=\n"
      "s_lshl_b32 s28, s16, 13\n"
      "s_add_u32 s28, s28, 0\n"
      "s_add_u32 s29, s73, 0\n"
      "s_mov_b32 m0, s28\n"
      "s_add_u32 s28, s28, 4096\n"
      "buffer_load_dwordx4 v0, s[0:3], s29 offen offset:0 sc1 lds\n"
      "buffer_load_dwordx4 v0, s[0:3], s29 offen offset:1024 sc1 lds\n"
      "buffer_load_dwordx4 v0, s[0:3], s29 offen offset:2048 sc1 lds\n"
      "buffer_load_dwordx4 v0, s[0:3], s29 offen offset:3072 sc1 lds\n"
      "s_add_u32 s29, s29, 4096\n"
      "s_mov_b32 m0, s28\n"
      "s_add_u32 s28, s28, 4096\n"
      "buffer_load_dwordx4 v0, s[0:3], s29 offen offset:0 sc1 lds\n"
      "buffer_load_dwordx4 v0, s[0:3], s29 offen offset:1024 sc1 lds\n"
      "buffer_load_dwordx4 v0, s[0:3], s29 offen offset:2048 sc1 lds\n"
      "buffer_load_dwordx4 v0, s[0:3], s29 offen offset:3072 sc1 lds\n"
      "s_add_u32 s29, s29, 4096\n"
      "s_add_u32 s31, s72, 0\n"
      "buffer_load_dwordx4 a[84:87], v0, s[0:3], s31 offen sc1\n"
      "s_add_u32 s31, s72, 4096\n"
      "buffer_load_dwordx4 a[88:91], v0, s[0:3], s31 offen sc1\n"
      "s_add_u32 s31, s72, 8192\n"
      "buffer_load_dwordx4 a[92:95], v0, s[0:3], s31 offen sc1\n"
      "s_add_u32 s31, s72, 12288\n"
      "buffer_load_dwordx4 a[96:99], v0, s[0:3], s31 offen sc1\n"
      "s_add_u32 s31, s72, 16384\n"
      "buffer_load_dwordx4 a[100:103], v0, s[0:3], s31 offen sc1\n"
      "s_add_u32 s31, s72, 20480\n"
      "buffer_load_dwordx4 a[104:107], v0, s[0:3], s31 offen sc1\n"
      "s_add_u32 s31, s72, 24576\n"
      "buffer_load_dwordx4 a[108:111], v0, s[0:3], s31 offen sc1\n"
      "s_add_u32 s31, s72, 28672\n"
      "buffer_load_dwordx4 a[112:115], v0, s[0:3], s31 offen sc1\n"
      "s_add_u32 s31, s72, 32768\n"
      "buffer_load_dwordx4 a[116:119], v0, s[0:3], s31 offen sc1\n"
      "s_add_u32 s31, s72, 36864\n"
      "buffer_load_dwordx4 a[120:123], v0, s[0:3], s31 offen sc1\n"
      "s_add_u32 s31, s72, 40960\n"
      "buffer_load_dwordx4 a[124:127], v0, s[0:3], s31 offen sc1\n"
      "s_waitcnt vmcnt(26)\n"
      "v_lshlrev_b32 v32, 16, v128\n"
      "v_and_b32 v33, 0xffff0000, v128\n"
      "v_lshlrev_b32 v34, 16, v129\n"
      "v_and_b32 v35, 0xffff0000, v129\n"
      "v_lshlrev_b32 v36, 16, v130\n"
      "v_and_b32 v37, 0xffff0000, v130\n"
      "v_lshlrev_b32 v38, 16, v131\n"
      "v_and_b32 v39, 0xffff0000, v131\n"
      "s_waitcnt vmcnt(25)\n"
      "v_lshlrev_b32 v168, 16, v132\n"
      "v_and_b32 v169, 0xffff0000, v132\n"
      "v_lshlrev_b32 v170, 16, v133\n"
      "v_and_b32 v171, 0xffff0000, v133\n"
      "v_lshlrev_b32 v172, 16, v134\n"
      "v_and_b32 v173, 0xffff0000, v134\n"
      "v_lshlrev_b32 v174, 16, v135\n"
      "v_and_b32 v175, 0xffff0000, v135\n"
      "v_pk_add_f32 v[32:33], v[32:33], v[168:169]\n"
      "v_pk_add_f32 v[34:35], v[34:35], v[170:171]\n"
      "v_pk_add_f32 v[36:37], v[36:37], v[172:173]\n"
      "v_pk_add_f32 v[38:39], v[38:39], v[174:175]\n"
      "s_waitcnt vmcnt(24)\n"
      "v_lshlrev_b32 v160, 16, v136\n"
      "v_and_b32 v161, 0xffff0000, v136\n"
      "v_lshlrev_b32 v162, 16, v137\n"
      "v_and_b32 v163, 0xffff0000, v137\n"
      "v_lshlrev_b32 v164, 16, v138\n"
      "v_and_b32 v165, 0xffff0000, v138\n"
      "v_lshlrev_b32 v166, 16, v139\n"
      "v_and_b32 v167, 0xffff0000, v139\n"
      "v_pk_add_f32 v[32:33], v[32:33], v[160:161]\n"
      "v_pk_add_f32 v[34:35], v[34:35], v[162:163]\n"
      "v_pk_add_f32 v[36:37], v[36:37], v[164:165]\n"
      "v_pk_add_f32 v[38:39], v[38:39], v[166:167]\n"
      "s_waitcnt vmcnt(23)\n"
      "v_lshlrev_b32 v168, 16, v140\n"
      "v_and_b32 v169, 0xffff0000, v140\n"
      "v_lshlrev_b32 v170, 16, v141\n"
      "v_and_b32 v171, 0xffff0000, v141\n"
      "v_lshlrev_b32 v172, 16, v142\n"
      "v_and_b32 v173, 0xffff0000, v142\n"
      "v_lshlrev_b32 v174, 16, v143\n"
      "v_and_b32 v175, 0xffff0000, v143\n"
      "v_pk_add_f32 v[32:33], v[32:33], v[168:169]\n"
      "v_pk_add_f32 v[34:35], v[34:35], v[170:171]\n"
      "v_pk_add_f32 v[36:37], v[36:37], v[172:173]\n"
      "v_pk_add_f32 v[38:39], v[38:39], v[174:175]\n"
      "s_waitcnt vmcnt(22)\n"
      "v_lshlrev_b32 v160, 16, v144\n"
      "v_and_b32 v161, 0xffff0000, v144\n"
      "v_lshlrev_b32 v162, 16, v145\n"
      "v_and_b32 v163, 0xffff0000, v145\n"
      "v_lshlrev_b32 v164, 16, v146\n"
      "v_and_b32 v165, 0xffff0000, v146\n"
      "v_lshlrev_b32 v166, 16, v147\n"
      "v_and_b32 v167, 0xffff0000, v147\n"
      "v_pk_add_f32 v[32:33], v[32:33], v[160:161]\n"
      "v_pk_add_f32 v[34:35], v[34:35], v[162:163]\n"
      "v_pk_add_f32 v[36:37], v[36:37], v[164:165]\n"
      "v_pk_add_f32 v[38:39], v[38:39], v[166:167]\n"
      "s_waitcnt vmcnt(21)\n"
      "v_lshlrev_b32 v168, 16, v148\n"
      "v_and_b32 v169, 0xffff0000, v148\n"
      "v_lshlrev_b32 v170, 16, v149\n"
      "v_and_b32 v171, 0xffff0000, v149\n"
      "v_lshlrev_b32 v172, 16, v150\n"
      "v_and_b32 v173, 0xffff0000, v150\n"
      "v_lshlrev_b32 v174, 16, v151\n"
      "v_and_b32 v175, 0xffff0000, v151\n"
      "v_pk_add_f32 v[32:33], v[32:33], v[168:169]\n"
      "v_pk_add_f32 v[34:35], v[34:35], v[170:171]\n"
      "v_pk_add_f32 v[36:37], v[36:37], v[172:173]\n"
      "v_pk_add_f32 v[38:39], v[38:39], v[174:175]\n"
      "s_waitcnt vmcnt(20)\n"
      "v_lshlrev_b32 v160, 16, v152\n"
      "v_and_b32 v161, 0xffff0000, v152\n"
      "v_lshlrev_b32 v162, 16, v153\n"
      "v_and_b32 v163, 0xffff0000, v153\n"
      "v_lshlrev_b32 v164, 16, v154\n"
      "v_and_b32 v165, 0xffff0000, v154\n"
      "v_lshlrev_b32 v166, 16, v155\n"
      "v_and_b32 v167, 0xffff0000, v155\n"
      "v_pk_add_f32 v[32:33], v[32:33], v[160:161]\n"
      "v_pk_add_f32 v[34:35], v[34:35], v[162:163]\n"
      "v_pk_add_f32 v[36:37], v[36:37], v[164:165]\n"
      "v_pk_add_f32 v[38:39], v[38:39], v[166:167]\n"
      "s_waitcnt vmcnt(19)\n"
      "v_lshlrev_b32 v168, 16, v156\n"
      "v_and_b32 v169, 0xffff0000, v156\n"
      "v_lshlrev_b32 v170, 16, v157\n"
      "v_and_b32 v171, 0xffff0000, v157\n"
      "v_lshlrev_b32 v172, 16, v158\n"
      "v_and_b32 v173, 0xffff0000, v158\n"
      "v_lshlrev_b32 v174, 16, v159\n"
      "v_and_b32 v175, 0xffff0000, v159\n"
      "v_pk_add_f32 v[32:33], v[32:33], v[168:169]\n"
      "v_pk_add_f32 v[34:35], v[34:35], v[170:171]\n"
      "v_pk_add_f32 v[36:37], v[36:37], v[172:173]\n"
      "v_pk_add_f32 v[38:39], v[38:39], v[174:175]\n"
      "s_branch .Lk142_%=\n"
      ".Lk041_%=:\n"
      "s_waitcnt vmcnt(7)\n"
      "v_lshlrev_b32 v32, 16, v128\n"
      "v_and_b32 v33, 0xffff0000, v128\n"
      "v_lshlrev_b32 v34, 16, v129\n"
      "v_and_b32 v35, 0xffff0000, v129\n"
      "v_lshlrev_b32 v36, 16, v130\n"
      "v_and_b32 v37, 0xffff0000, v130\n"
      "v_lshlrev_b32 v38, 16, v131\n"
      "v_and_b32 v39, 0xffff0000, v131\n"
      "s_waitcnt vmcnt(6)\n"
      "v_lshlrev_b32 v168, 16, v132\n"
      "v_and_b32 v169, 0xffff0000, v132\n"
      "v_lshlrev_b32 v170, 16, v133\n"
      "v_and_b32 v171, 0xffff0000, v133\n"
      "v_lshlrev_b32 v172, 16, v134\n"
      "v_and_b32 v173, 0xffff0000, v134\n"
      "v_lshlrev_b32 v174, 16, v135\n"
      "v_and_b32 v175, 0xffff0000, v135\n"
      "v_pk_add_f32 v[32:33], v[32:33], v[168:169]\n"
      "v_pk_add_f32 v[34:35], v[34:35], v[170:171]\n"
      "v_pk_add_f32 v[36:37], v[36:37], v[172:173]\n"
      "v_pk_add_f32 v[38:39], v[38:39], v[174:175]\n"
      "s_waitcnt vmcnt(5)\n"
      "v_lshlrev_b32 v160, 16, v136\n"
      "v_and_b32 v161, 0xffff0000, v136\n"
      "v_lshlrev_b32 v162, 16, v137\n"
      "v_and_b32 v163, 0xffff0000, v137\n"
      "v_lshlrev_b32 v164, 16, v138\n"
      "v_and_b32 v165, 0xffff0000, v138\n"
      "v_lshlrev_b32 v166, 16, v139\n"
      "v_and_b32 v167, 0xffff0000, v139\n"
      "v_pk_add_f32 v[32:33], v[32:33], v[160:161]\n"
      "v_pk_add_f32 v[34:35], v[34:35], v[162:163]\n"
      "v_pk_add_f32 v[36:37], v[36:37], v[164:165]\n"
      "v_pk_add_f32 v[38:39], v[38:39], v[166:167]\n"
      "s_waitcnt vmcnt(4)\n"
      "v_lshlrev_b32 v168, 16, v140\n"
      "v_and_b32 v169, 0xffff0000, v140\n"
      "v_lshlrev_b32 v170, 16, v141\n"
      "v_and_b32 v171, 0xffff0000, v141\n"
      "v_lshlrev_b32 v172, 16, v142\n"
      "v_and_b32 v173, 0xffff0000, v142\n"
      "v_lshlrev_b32 v174, 16, v143\n"
      "v_and_b32 v175, 0xffff0000, v143\n"
      "v_pk_add_f32 v[32:33], v[32:33], v[168:169]\n"
      "v_pk_add_f32 v[34:35], v[34:35], v[170:171]\n"
      "v_pk_add_f32 v[36:37], v[36:37], v[172:173]\n"
      "v_pk_add_f32 v[38:39], v[38:39], v[174:175]\n"
      "s_waitcnt vmcnt(3)\n"
      "v_lshlrev_b32 v160, 16, v144\n"
      "v_and_b32 v161, 0xffff0000, v144\n"
      "v_lshlrev_b32 v162, 16, v145\n"
      "v_and_b32 v163, 0xffff0000, v145\n"
      "v_lshlrev_b32 v164, 16, v146\n"
      "v_and_b32 v165, 0xffff0000, v146\n"
      "v_lshlrev_b32 v166, 16, v147\n"
      "v_and_b32 v167, 0xffff0000, v147\n"
      "v_pk_add_f32 v[32:33], v[32:33], v[160:161]\n"
      "v_pk_add_f32 v[34:35], v[34:35], v[162:163]\n"
      "v_pk_add_f32 v[36:37], v[36:37], v[164:165]\n"
      "v_pk_add_f32 v[38:39], v[38:39], v[166:167]\n"
      "s_waitcnt vmcnt(2)\n"
      "v_lshlrev_b32 v168, 16, v148\n"
      "v_and_b32 v169, 0xffff0000, v148\n"
      "v_lshlrev_b32 v170, 16, v149\n"
      "v_and_b32 v171, 0xffff0000, v149\n"
      "v_lshlrev_b32 v172, 16, v150\n"
      "v_and_b32 v173, 0xffff0000, v150\n"
      "v_lshlrev_b32 v174, 16, v151\n"
      "v_and_b32 v175, 0xffff0000, v151\n"
      "v_pk_add_f32 v[32:33], v[32:33], v[168:169]\n"
      "v_pk_add_f32 v[34:35], v[34:35], v[170:171]\n"
      "v_pk_add_f32 v[36:37], v[36:37], v[172:173]\n"
      "v_pk_add_f32 v[38:39], v[38:39], v[174:175]\n"
      "s_waitcnt vmcnt(1)\n"
      "v_lshlrev_b32 v160, 16, v152\n"
      "v_and_b32 v161, 0xffff0000, v152\n"
      "v_lshlrev_b32 v162, 16, v153\n"
      "v_and_b32 v163, 0xffff0000, v153\n"
      "v_lshlrev_b32 v164, 16, v154\n"
      "v_and_b32 v165, 0xffff0000, v154\n"
      "v_lshlrev_b32 v166, 16, v155\n"
      "v_and_b32 v167, 0xffff0000, v155\n"
      "v_pk_add_f32 v[32:33], v[32:33], v[160:161]\n"
      "v_pk_add_f32 v[34:35], v[34:35], v[162:163]\n"
      "v_pk_add_f32 v[36:37], v[36:37], v[164:165]\n"
      "v_pk_add_f32 v[38:39], v[38:39], v[166:167]\n"
      "s_waitcnt vmcnt(0)\n"
      "v_lshlrev_b32 v168, 16, v156\n"
      "v_and_b32 v169, 0xffff0000, v156\n"
      "v_lshlrev_b32 v170, 16, v157\n"
      "v_and_b32 v171, 0xffff0000, v157\n"
      "v_lshlrev_b32 v172, 16, v158\n"
      "v_and_b32 v173, 0xffff0000, v158\n"
      "v_lshlrev_b32 v174, 16, v159\n"
      "v_and_b32 v175, 0xffff0000, v159\n"
      "v_pk_add_f32 v[32:33], v[32:33], v[168:169]\n"
      "v_pk_add_f32 v[34:35], v[34:35], v[170:171]\n"
      "v_pk_add_f32 v[36:37], v[36:37], v[172:173]\n"
      "v_pk_add_f32 v[38:39], v[38:39], v[174:175]\n"
      ".Lk142_%=:\n"
      "s_nop 0 ; hz\n"
      "v_permlane32_swap_b32 v32, v36\n"
      "v_permlane32_swap_b32 v33, v37\n"
      "v_permlane32_swap_b32 v34, v38\n"
      "v_permlane32_swap_b32 v35, v39\n"
      "v_pk_add_f32 v[32:33], v[32:33], v[36:37]\n"
      "v_pk_add_f32 v[34:35], v[34:35], v[38:39]\n"
      "v_cvt_pk_bf16_f32 v32, v32, v33\n"
      "v_cvt_pk_bf16_f32 v33, v34, v35\n"
      "ds_write_b64 v92, v[32:33]\n"
      "s_waitcnt lgkmcnt(0)\n"
      "s_barrier\n"
      "ds_read_b128 v[128:131], v93\n"
      "ds_read_b128 v[132:135], v93 offset:1024\n"
      "s_sub_u32 s28, s11, 9\n"
      "s_cmp_lt_u32 s28, 7\n"
      "s_cbranch_scc0 .Lif43_%=\n"
      "s_lshl_b32 s28, s15, 3\n"
      "s_add_u32 s28, s28, s25\n"
      "s_add_u32 s28, s28, s27\n"
      "s_mov_b64 exec, 0xffff\n"
      "buffer_store_dwordx2 v[12:13], v4, s[0:3], s28 offen sc1\n"
      "s_mov_b64 exec, -1\n"
      ".Lif43_%=:\n"
      "s_waitcnt lgkmcnt(1)\n"
      "v_mfma_f32_32x32x16_bf16 v[96:111], v[128:131], v[16:19], 0\n"
      "s_waitcnt lgkmcnt(0)\n"
      "v_mfma_f32_32x32x16_bf16 v[96:111], v[132:135], v[20:23], v[96:111]\n"
      "s_branch .Lsto7_%=\n"
      ".Lmixb6_%=:\n"
      "ds_read_b128 v[96:99], v1 offset:0\n"
      "ds_read_b128 v[100:103], v1 offset:28672\n"
      "ds_read_b128 v[104:107], v1 offset:1024\n"
      "ds_read_b128 v[108:111], v1 offset:29696\n"
      "ds_read_b128 v[112:115], v1 offset:2048\n"
      "ds_read_b128 v[116:119], v1 offset:30720\n"
      "buffer_load_dwordx4 v[168:171], v0, s[0:3], s30 offen offset:2048 sc1\n"
      "s_waitcnt vmcnt(10) lgkmcnt(4)\n"
      "v_mfma_f32_32x32x16_bf16 v[16:31], v[96:99], v[128:131], 0\n"
      "ds_read_b128 v[120:123], v1 offset:3072\n"
      "ds_read_b128 v[124:127], v1 offset:31744\n"
      "v_mov_b32 v54, 0x3f803f80\n"
      "v_mov_b32 v55, 0x3f80\n"
      "v_mfma_f32_32x32x16_bf16 v[32:47], v[128:131], v[100:103], 0\n"
      "v_cmp_eq_u32 vcc, 224, v6\n"
      "v_mov_b32 v57, 0x3f803f80\n"
      "buffer_load_dwordx4 v[172:175], v0, s[0:3], s30 offen offset:3072 sc1\n"
      "buffer_load_dwordx4 v[64:67], v0, s[0:3], s21 offen offset:0 sc1\n"
      "buffer_load_dwordx4 v[68:71], v0, s[0:3], s21 offen offset:1024 sc1\n"
      "buffer_load_dwordx4 v[72:75], v0, s[0:3], s21 offen offset:2048 sc1\n"
      "s_mov_b32 exec_hi, 0\n"
      "buffer_load_dwordx4 v[76:79], v0, s[0:3], s21 offen offset:3072 sc1\n"
      "s_mov_b32 exec_hi, -1\n"
      "s_waitcnt vmcnt(14) lgkmcnt(4)\n"
      "v_mfma_f32_32x32x16_bf16 v[16:31], v[104:107], v[132:135], v[16:31]\n"
      "ds_read_b128 v[96:99], v1 offset:4096\n"
      "ds_read_b128 v[100:103], v1 offset:32768\n"
      "v_cndmask_b32 v56, 0, v57, vcc\n"
      "v_mov_b32 v57, v56\n"
      "v_mfma_f32_32x32x16_bf16 v[32:47], v[132:135], v[108:111], v[32:47]\n"
      "v_mov_b64 v[58:59], v[56:57]\n"
      "v_mov_b64 v[60:61], v[56:57]\n"
      "s_add_u32 s30, s30, 0x1000\n"
      "buffer_load_dwordx4 v[176:179], v0, s[0:3], s30 offen offset:0 sc1\n"
      "s_waitcnt vmcnt(14) lgkmcnt(4)\n"
      "v_mfma_f32_32x32x16_bf16 v[16:31], v[112:115], v[136:139], v[16:31]\n"
      "ds_read_b128 v[104:107], v1 offset:5120\n"
      "ds_read_b128 v[108:111], v1 offset:33792\n"
      "v_mov_b64 v[62:63], v[56:57]\n"
      "s_nop 0\n"
      "v_mfma_f32_32x32x16_bf16 v[32:47], v[136:139], v[116:119], v[32:47]\n"
      "s_lshl_b32 s34, s14, 12\n"
      "s_add_u32 s34, s34, s24\n"
      "buffer_load_dwordx4 v[180:183], v0, s[0:3], s30 offen offset:1024 sc1\n"
      "s_waitcnt vmcnt(14) lgkmcnt(4)\n"
      "v_mfma_f32_32x32x16_bf16 v[16:31], v[120:123], v[140:143], v[16:31]\n"
      "ds_read_b128 v[112:115], v1 offset:6144\n"
      "ds_read_b128 v[116:119], v1 offset:34816\n"
      "v_mfma_f32_32x32x16_bf16 v[32:47], v[140:143], v[124:127], v[32:47]\n"
      "buffer_load_dwordx4 v[184:187], v0, s[0:3], s30 offen offset:2048 sc1\n"
      "s_waitcnt vmcnt(14) lgkmcnt(4)\n"
      "v_mfma_f32_32x32x16_bf16 v[16:31], v[96:99], v[144:147], v[16:31]\n"
      "ds_read_b128 v[120:123], v1 offset:7168\n"
      "ds_read_b128 v[124:127], v1 offset:35840\n"
      "v_mfma_f32_32x32x16_bf16 v[32:47], v[144:147], v[100:103], v[32:47]\n"
      "buffer_load_dwordx4 v[188:191], v0, s[0:3], s30 offen offset:3072 sc1\n"
      "s_waitcnt vmcnt(14) lgkmcnt(4)\n"
      "v_mfma_f32_32x32x16_bf16 v[16:31], v[104:107], v[148:151], v[16:31]\n"
      "ds_read_b128 v[96:99], v1 offset:8192\n"
      "ds_read_b128 v[100:103], v1 offset:36864\n"
      "v_mfma_f32_32x32x16_bf16 v[32:47], v[148:151], v[108:111], v[32:47]\n"
      "s_add_u32 s30, s30, 0x1000\n"
      "buffer_load_dwordx4 v[192:195], v0, s[0:3], s30 offen offset:0 sc1\n"
      "s_waitcnt vmcnt(14) lgkmcnt(4)\n"
      "v_mfma_f32_32x32x16_bf16 v[16:31], v[112:115], v[152:155], v[16:31]\n"
      "ds_read_b128 v[104:107], v1 offset:9216\n"
      "ds_read_b128 v[108:111], v1 offset:37888\n"
      "v_mfma_f32_32x32x16_bf16 v[32:47], v[152:155], v[116:119], v[32:47]\n"
      "buffer_load_dwordx4 v[196:199], v0, s[0:3], s30 offen offset:1024 sc1\n"
      "s_waitcnt vmcnt(14) lgkmcnt(4)\n"
      "v_mfma_f32_32x32x16_bf16 v[16:31], v[120:123], v[156:159], v[16:31]\n"
      "ds_read_b128 v[112:115], v1 offset:10240\n"
      "ds_read_b128 v[116:119], v1 offset:38912\n"
      "v_mfma_f32_32x32x16_bf16 v[32:47], v[156:159], v[124:127], v[32:47]\n"
      "buffer_load_dwordx4 v[200:203], v0, s[0:3], s30 offen offset:2048 sc1\n"
      "s_waitcnt vmcnt(14) lgkmcnt(4)\n"
      "v_mfma_f32_32x32x16_bf16 v[16:31], v[96:99], v[160:163], v[16:31]\n"
      "ds_read_b128 v[120:123], v1 offset:11264\n"
      "ds_read_b128 v[124:127], v1 offset:39936\n"
      "v_mfma_f32_32x32x16_bf16 v[32:47], v[160:163], v[100:103], v[32:47]\n"
      "buffer_load_dwordx4 v[204:207], v0, s[0:3], s30 offen offset:3072 sc1\n"
      "s_waitcnt vmcnt(14) lgkmcnt(4)\n"
      "v_mfma_f32_32x32x16_bf16 v[16:31], v[104:107], v[164:167], v[16:31]\n"
      "ds_read_b128 v[96:99], v1 offset:12288\n"
      "ds_read_b128 v[100:103], v1 offset:40960\n"
      "v_mfma_f32_32x32x16_bf16 v[32:47], v[164:167], v[108:111], v[32:47]\n"
      "s_add_u32 s30, s30, 0x1000\n"
      "buffer_load_dwordx4 v[208:211], v0, s[0:3], s30 offen offset:0 sc1\n"
      "s_waitcnt vmcnt(14) lgkmcnt(4)\n"
      "v_mfma_f32_32x32x16_bf16 v[16:31], v[112:115], v[168:171], v[16:31]\n"
      "ds_read_b128 v[104:107], v1 offset:13312\n"
      "ds_read_b128 v[108:111], v1 offset:41984\n"
      "v_mfma_f32_32x32x16_bf16 v[32:47], v[168:171], v[116:119], v[32:47]\n"
      "buffer_load_dwordx4 v[212:215], v0, s[0:3], s30 offen offset:1024 sc1\n"
      "s_waitcnt vmcnt(14) lgkmcnt(4)\n"
      "v_mfma_f32_32x32x16_bf16 v[16:31], v[120:123], v[172:175], v[16:31]\n"
      "ds_read_b128 v[112:115], v1 offset:14336\n"
      "ds_read_b128 v[116:119], v1 offset:43008\n"
      "s_lshl_b32 s28, s16, 8\n"
      "s_add_u32 s28, s28, 159744\n"
      "v_mfma_f32_32x32x16_bf16 v[32:47], v[172:175], v[124:127], v[32:47]\n"
      "v_add_u32 v86, s28, v6\n"
      "v_mov_b32 v82, 0\n"
      "buffer_load_dwordx4 v[216:219], v0, s[0:3], s30 offen offset:2048 sc1\n"
      "s_waitcnt vmcnt(10) lgkmcnt(4)\n"
      "v_mfma_f32_32x32x16_bf16 v[16:31], v[96:99], v[176:179], v[16:31]\n"
      "ds_read_b128 v[120:123], v1 offset:15360\n"
      "ds_read_b128 v[124:127], v1 offset:44032\n"
      "v_mov_b32 v83, 0\n"
      "v_mfma_f32_32x32x16_bf16 v[32:47], v[176:179], v[100:103], v[32:47]\n"
      "v_pk_add_f32 v[64:65], v[64:65], v[68:69]\n"
      "v_pk_add_f32 v[66:67], v[66:67], v[70:71]\n"
      "buffer_load_dwordx4 v[220:223], v0, s[0:3], s30 offen offset:3072 sc1\n"
      "s_waitcnt vmcnt(10) lgkmcnt(4)\n"
      "v_mfma_f32_32x32x16_bf16 v[16:31], v[104:107], v[180:183], v[16:31]\n"
      "ds_read_b128 v[96:99], v1 offset:16384\n"
      "ds_read_b128 v[100:103], v1 offset:45056\n"
      "v_pk_add_f32 v[72:73], v[72:73], v[76:77]\n"
      "v_pk_add_f32 v[74:75], v[74:75], v[78:79]\n"
      "v_mfma_f32_32x32x16_bf16 v[32:47], v[180:183], v[108:111], v[32:47]\n"
      "v_pk_add_f32 v[64:65], v[64:65], v[72:73]\n"
      "v_pk_add_f32 v[66:67], v[66:67], v[74:75]\n"
      "s_add_u32 s30, s30, 0x1000\n"
      "buffer_load_dwordx4 v[224:227], v0, s[0:3], s30 offen offset:0 sc1\n"
      "s_waitcnt vmcnt(10) lgkmcnt(4)\n"
      "v_mfma_f32_32x32x16_bf16 v[16:31], v[112:115], v[184:187], v[16:31]\n"
      "ds_read_b128 v[104:107], v1 offset:17408\n"
      "ds_read_b128 v[108:111], v1 offset:46080\n"
      "v_permlane32_swap_b32 v64, v66\n"
      "v_permlane32_swap_b32 v65, v67\n"
      "v_mfma_f32_32x32x16_bf16 v[32:47], v[184:187], v[116:119], v[32:47]\n"
      "v_pk_add_f32 v[64:65], v[64:65], v[66:67]\n"
      "buffer_load_dwordx4 v[228:231], v0, s[0:3], s30 offen offset:1024 sc1\n"
      "s_waitcnt vmcnt(10) lgkmcnt(4)\n"
      "v_mfma_f32_32x32x16_bf16 v[16:31], v[120:123], v[188:191], v[16:31]\n"
      "ds_read_b128 v[112:115], v1 offset:18432\n"
      "ds_read_b128 v[116:119], v1 offset:47104\n"
      "v_permlane16_swap_b32 v64, v65\n"
      "v_add_f32 v64, v64, v65\n"
      "v_mfma_f32_32x32x16_bf16 v[32:47], v[188:191], v[124:127], v[32:47]\n"
      "ds_write_b32 v5, v64\n"
      "ds_read_b64 v[84:85], v86\n"
      "buffer_load_dwordx4 v[232:235], v0, s[0:3], s30 offen offset:2048 sc1\n"
      "s_waitcnt vmcnt(10) lgkmcnt(6)\n"
      "v_mfma_f32_32x32x16_bf16 v[16:31], v[96:99], v[192:195], v[16:31]\n"
      "ds_read_b128 v[120:123], v1 offset:19456\n"
      "ds_read_b128 v[124:127], v1 offset:48128\n"
      "v_mfma_f32_32x32x16_bf16 v[32:47], v[192:195], v[100:103], v[32:47]\n"
      "buffer_load_dwordx4 v[236:239], v0, s[0:3], s30 offen offset:3072 sc1\n"
      "s_waitcnt vmcnt(10) lgkmcnt(6)\n"
      "v_mfma_f32_32x32x16_bf16 v[16:31], v[104:107], v[196:199], v[16:31]\n"
      "ds_read_b128 v[96:99], v1 offset:20480\n"
      "ds_read_b128 v[100:103], v1 offset:49152\n"
      "s_waitcnt lgkmcnt(4)\n"
      "v_fma_f32 v84, 0, v85, v84\n"
      "v_mfma_f32_32x32x16_bf16 v[32:47], v[196:199], v[108:111], v[32:47]\n"
      "v_mul_f32 v84, 0xbb124925, v84\n"
      "v_mul_f32 v85, 0x3b124925, v85\n"
      "s_lshl_b32 s31, s15, 8\n"
      "s_add_u32 s31, s31, s25\n"
      "s_add_u32 s31, s31, s27\n"
      "s_sub_u32 s31, s31, 3968\n"
      "s_or_b32 s31, s31, s6\n"
      "s_mov_b64 exec, 0xffff\n"
      "buffer_store_dwordx2 v[12:13], v6, s[0:3], s31 offen sc1\n"
      "s_mov_b64 exec, -1\n"
      "s_waitcnt vmcnt(10)\n"
      "v_mfma_f32_32x32x16_bf16 v[16:31], v[112:115], v[200:203], v[16:31]\n"
      "ds_read_b128 v[104:107], v1 offset:21504\n"
      "ds_read_b128 v[108:111], v1 offset:50176\n"
      "v_fma_f32 v85, -v84, v84, v85\n"
      "v_max_f32 v85, 0, v85\n"
      "v_mfma_f32_32x32x16_bf16 v[32:47], v[200:203], v[116:119], v[32:47]\n"
      "v_add_f32 v85, 0x3727c5ac, v85\n"
      "v_rsq_f32 v88, v85\n"
      "s_waitcnt vmcnt(9) lgkmcnt(4)\n"
      "v_mfma_f32_32x32x16_bf16 v[16:31], v[120:123], v[204:207], v[16:31]\n"
      "ds_read_b128 v[112:115], v1 offset:22528\n"
      "ds_read_b128 v[116:119], v1 offset:51200\n"
      "v_cvt_pk_bf16_f32 v81, v84, 0\n"
      "v_lshlrev_b32 v86, 16, v81\n"
      "v_mfma_f32_32x32x16_bf16 v[32:47], v[204:207], v[124:127], v[32:47]\n"
      "v_sub_f32 v86, v84, v86\n"
      "v_cvt_pk_bf16_f32 v80, v84, v86\n"
      "s_waitcnt vmcnt(8) lgkmcnt(4)\n"
      "v_mfma_f32_32x32x16_bf16 v[16:31], v[96:99], v[208:211], v[16:31]\n"
      "ds_read_b128 v[120:123], v1 offset:23552\n"
      "ds_read_b128 v[124:127], v1 offset:52224\n"
      "v_mov_b32 v89, v88\n"
      "ds_write_b32 v9, v88\n"
      "v_mfma_f32_32x32x16_bf16 v[32:47], v[208:211], v[100:103], v[32:47]\n"
      "ds_read_b128 v[64:67], v10 offset:0\n"
      "ds_read_b128 v[68:71], v10 offset:32\n"
      "s_waitcnt vmcnt(7) lgkmcnt(7)\n"
      "v_mfma_f32_32x32x16_bf16 v[16:31], v[104:107], v[212:215], v[16:31]\n"
      "ds_read_b128 v[96:99], v1 offset:24576\n"
      "ds_read_b128 v[100:103], v1 offset:53248\n"
      "ds_read_b128 v[72:75], v10 offset:64\n"
      "ds_read_b128 v[76:79], v10 offset:96\n"
      "v_mfma_f32_32x32x16_bf16 v[32:47], v[212:215], v[108:111], v[32:47]\n"
      "v_mfma_f32_32x32x16_bf16 v[16:31], a[180:183], v[80:83], v[16:31]\n"
      "v_mfma_f32_32x32x16_bf16 v[32:47], v[80:83], a[184:187], v[32:47]\n"
      "s_waitcnt vmcnt(6) lgkmcnt(9)\n"
      "v_mfma_f32_32x32x16_bf16 v[16:31], v[112:115], v[216:219], v[16:31]\n"
      "ds_read_b128 v[104:107], v1 offset:25600\n"
      "ds_read_b128 v[108:111], v1 offset:54272\n"
      "v_mfma_f32_32x32x16_bf16 v[32:47], v[216:219], v[116:119], v[32:47]\n"
      "s_waitcnt vmcnt(5) lgkmcnt(9)\n"
      "v_mfma_f32_32x32x16_bf16 v[16:31], v[120:123], v[220:223], v[16:31]\n"
      "ds_read_b128 v[112:115], v1 offset:26624\n"
      "ds_read_b128 v[116:119], v1 offset:55296\n"
      "v_mfma_f32_32x32x16_bf16 v[32:47], v[220:223], v[124:127], v[32:47]\n"
      "s_waitcnt vmcnt(4) lgkmcnt(6)\n"
      "v_mfma_f32_32x32x16_bf16 v[16:31], v[96:99], v[224:227], v[16:31]\n"
      "ds_read_b128 v[120:123], v1 offset:27648\n"
      "ds_read_b128 v[124:127], v1 offset:56320\n"
      "v_mfma_f32_32x32x16_bf16 v[32:47], v[224:227], v[100:103], v[32:47]\n"
      "s_waitcnt vmcnt(3) lgkmcnt(4)\n"
      "v_mfma_f32_32x32x16_bf16 v[16:31], v[104:107], v[228:231], v[16:31]\n"
      "v_mfma_f32_32x32x16_bf16 v[32:47], v[228:231], v[108:111], v[32:47]\n"
      "s_waitcnt vmcnt(2) lgkmcnt(2)\n"
      "v_mfma_f32_32x32x16_bf16 v[16:31], v[112:115], v[232:235], v[16:31]\n"
      "v_mfma_f32_32x32x16_bf16 v[32:47], v[232:235], v[116:119], v[32:47]\n"
      "s_waitcnt vmcnt(1) lgkmcnt(0)\n"
      "v_mfma_f32_32x32x16_bf16 v[16:31], v[120:123], v[236:239], v[16:31]\n"
      "v_mfma_f32_32x32x16_bf16 v[32:47], v[236:239], v[124:127], v[32:47]\n"
      "s_waitcnt lgkmcnt(0)\n"
      "s_nop 7 ; hz\n"
      "s_nop 1 ; hz\n"
      "v_pk_mul_f32 v[16:17], v[16:17], v[88:89]\n"
      "v_pk_mul_f32 v[18:19], v[18:19], v[88:89]\n"
      "v_pk_mul_f32 v[20:21], v[20:21], v[88:89]\n"
      "v_pk_mul_f32 v[22:23], v[22:23], v[88:89]\n"
      "v_pk_mul_f32 v[24:25], v[24:25], v[88:89]\n"
      "v_pk_mul_f32 v[26:27], v[26:27], v[88:89]\n"
      "v_pk_mul_f32 v[28:29], v[28:29], v[88:89]\n"
      "v_pk_mul_f32 v[30:31], v[30:31], v[88:89]\n"
      "v_cmp_class_f32 vcc, v16, s7\n"
      "s_cbranch_vccz .Lck44_%=\n"
      "s_cmp_eq_u32 s5, 0\n"
      "s_cbranch_scc1 .Lslowb25_%=\n"
      ".Lck44_%=:\n"
      "s_mov_b32 s5, 0\n"
      "v_cvt_pk_bf16_f32 v48, v16, v17\n"
      "v_cvt_pk_bf16_f32 v49, v18, v19\n"
      "v_cvt_pk_bf16_f32 v50, v20, v21\n"
      "v_cvt_pk_bf16_f32 v51, v22, v23\n"
      "v_cvt_pk_bf16_f32 v52, v24, v25\n"
      "v_cvt_pk_bf16_f32 v53, v26, v27\n"
      "s_mov_b32 exec_hi, 0\n"
      "v_cvt_pk_bf16_f32 v54, v28, v29\n"
      "v_cvt_pk_bf16_f32 v55, v30, v31\n"
      "s_mov_b32 exec_hi, -1\n"
      "buffer_store_dwordx4 v[48:51], v0, s[0:3], s34 offen offset:0 sc1\n"
      "buffer_store_dwordx4 v[52:55], v0, s[0:3], s34 offen offset:1024 sc1\n"
      "v_pk_mul_f32 v[32:33], v[32:33], v[64:65]\n"
      "v_pk_mul_f32 v[34:35], v[34:35], v[66:67]\n"
      "v_pk_mul_f32 v[36:37], v[36:37], v[68:69]\n"
      "v_pk_mul_f32 v[38:39], v[38:39], v[70:71]\n"
      "v_pk_mul_f32 v[40:41], v[40:41], v[72:73]\n"
      "v_pk_mul_f32 v[42:43], v[42:43], v[74:75]\n"
      "v_pk_mul_f32 v[44:45], v[44:45], v[76:77]\n"
      "v_pk_mul_f32 v[46:47], v[46:47], v[78:79]\n"
      "s_mov_b32 exec_lo, 0x0fffffff\n"
      "s_mov_b32 exec_hi, 0x0fffffff\n"
      "v_cvt_pk_bf16_f32 v56, v32, v33\n"
      "v_cvt_pk_bf16_f32 v57, v34, v35\n"
      "v_cvt_pk_bf16_f32 v58, v36, v37\n"
      "v_cvt_pk_bf16_f32 v59, v38, v39\n"
      "v_cvt_pk_bf16_f32 v60, v40, v41\n"
      "v_cvt_pk_bf16_f32 v61, v42, v43\n"
      "v_cvt_pk_bf16_f32 v62, v44, v45\n"
      "v_cvt_pk_bf16_f32 v63, v46, v47\n"
      "s_mov_b64 exec, -1\n"
      "v_permlane32_swap_b32 v56, v58\n"
      "v_permlane32_swap_b32 v57, v59\n"
      "v_permlane32_swap_b32 v60, v62\n"
      "v_permlane32_swap_b32 v61, v63\n"
      "buffer_store_dwordx4 v[56:59], v0, s[0:3], s34 offen offset:2048 sc1\n"
      "buffer_store_dwordx4 v[60:63], v0, s[0:3], s34 offen offset:3072 sc1\n"
      "s_barrier\n"
      "s_cmp_eq_u32 s16, 0\n"
      "s_cbranch_scc0 .Lif45_%=\n"
      "s_lshl_b32 s28, s14, 3\n"
      "s_add_u32 s28, s28, s26\n"
      "s_add_u32 s28, s28, s27\n"
      "s_mov_b64 exec, 0xffff\n"
      "buffer_store_dwordx2 v[12:13], v4, s[0:3], s28 offen sc1\n"
      "s_mov_b64 exec, -1\n"
      ".Lif45_%=:\n"
      "ds_read_b128 v[32:35], v2 offset:0\n"
      "ds_read_b128 v[36:39], v2 offset:1024\n"
      "ds_read_b128 v[40:43], v2 offset:2048\n"
      "ds_read_b128 v[44:47], v2 offset:3072\n"
      "ds_read_b128 v[112:115], v2 offset:4096\n"
      "v_mfma_f32_32x32x16_bf16 v[16:31], a[188:191], v[80:83], 0\n"
      "s_waitcnt lgkmcnt(4)\n"
      "v_mfma_f32_32x32x16_bf16 v[16:31], v[32:35], v[128:131], v[16:31]\n"
      "ds_read_b128 v[116:119], v2 offset:5120\n"
      "s_waitcnt lgkmcnt(4)\n"
      "v_mfma_f32_32x32x16_bf16 v[16:31], v[36:39], v[132:135], v[16:31]\n"
      "ds_read_b128 v[120:123], v2 offset:6144\n"
      "v_mov_b64 v[64:65], 0\n"
      "v_mov_b64 v[66:67], 0\n"
      "v_mov_b64 v[68:69], 0\n"
      "s_waitcnt lgkmcnt(4)\n"
      "v_mfma_f32_32x32x16_bf16 v[16:31], v[40:43], v[136:139], v[16:31]\n"
      "ds_read_b128 v[124:127], v2 offset:7168\n"
      "v_mov_b64 v[70:71], 0\n"
      "v_mov_b64 v[72:73], 0\n"
      "v_mov_b64 v[74:75], 0\n"
      "s_waitcnt lgkmcnt(4)\n"
      "v_mfma_f32_32x32x16_bf16 v[16:31], v[44:47], v[140:143], v[16:31]\n"
      "ds_read_b128 v[32:35], v2 offset:8192\n"
      "v_mov_b64 v[76:77], 0\n"
      "v_mov_b64 v[78:79], 0\n"
      "v_mov_b64 v[80:81], 0\n"
      "s_waitcnt lgkmcnt(4)\n"
      "v_mfma_f32_32x32x16_bf16 v[16:31], v[112:115], v[144:147], v[16:31]\n"
      "ds_read_b128 v[36:39], v2 offset:9216\n"
      "v_mov_b64 v[82:83], 0\n"
      "v_mov_b64 v[84:85], 0\n"
      "v_mov_b64 v[86:87], 0\n"
      "s_waitcnt lgkmcnt(4)\n"
      "v_mfma_f32_32x32x16_bf16 v[16:31], v[116:119], v[148:151], v[16:31]\n"
      "ds_read_b128 v[40:43], v2 offset:10240\n"
      "v_mov_b64 v[90:91], 0\n"
      "v_mov_b64 v[92:93], 0\n"
      "v_mov_b64 v[94:95], 0\n"
      "s_waitcnt lgkmcnt(4)\n"
      "v_mfma_f32_32x32x16_bf16 v[16:31], v[120:123], v[152:155], v[16:31]\n"
      "ds_read_b128 v[44:47], v2 offset:11264\n"
      "v_mov_b64 v[96:97], 0\n"
      "v_mov_b64 v[98:99], 0\n"
      "v_mov_b64 v[100:101], 0\n"
      "s_waitcnt lgkmcnt(4)\n"
      "v_mfma_f32_32x32x16_bf16 v[16:31], v[124:127], v[156:159], v[16:31]\n"
      "ds_read_b128 v[112:115], v2 offset:12288\n"
      "v_mov_b64 v[102:103], 0\n"
      "v_mov_b64 v[104:105], 0\n"
      "v_mov_b64 v[106:107], 0\n"
      "s_waitcnt lgkmcnt(4)\n"
      "v_mfma_f32_32x32x16_bf16 v[16:31], v[32:35], v[160:163], v[16:31]\n"
      "ds_read_b128 v[116:119], v2 offset:13312\n"
      "v_mov_b64 v[108:109], 0\n"
      "v_mov_b64 v[110:111], 0\n"
      "s_waitcnt lgkmcnt(4)\n"
      "v_mfma_f32_32x32x16_bf16 v[16:31], v[36:39], v[164:167], v[16:31]\n"
      "ds_read_b128 v[120:123], v2 offset:14336\n"
      "s_waitcnt lgkmcnt(4)\n"
      "v_mfma_f32_32x32x16_bf16 v[16:31], v[40:43], v[168:171], v[16:31]\n"
      "ds_read_b128 v[124:127], v2 offset:15360\n"
      "s_waitcnt lgkmcnt(4)\n"
      "v_mfma_f32_32x32x16_bf16 v[16:31], v[44:47], v[172:175], v[16:31]\n"
      "ds_read_b128 v[32:35], v2 offset:16384\n"
      "s_waitcnt lgkmcnt(4)\n"
      "v_mfma_f32_32x32x16_bf16 v[16:31], v[112:115], v[176:179], v[16:31]\n"
      "ds_read_b128 v[36:39], v2 offset:17408\n"
      "v_mov_b64 v[160:161], v[48:49]\n"
      "v_mov_b64 v[162:163], v[50:51]\n"
      "v_mov_b64 v[164:165], v[52:53]\n"
      "v_mov_b64 v[166:167], v[54:55]\n"
      "s_waitcnt lgkmcnt(4)\n"
      "v_mfma_f32_32x32x16_bf16 v[16:31], v[116:119], v[180:183], v[16:31]\n"
      "ds_read_b128 v[40:43], v2 offset:18432\n"
      "v_mov_b64 v[168:169], v[56:57]\n"
      "v_mov_b64 v[170:171], v[58:59]\n"
      "v_mov_b64 v[172:173], v[60:61]\n"
      "v_mov_b64 v[174:175], v[62:63]\n"
      "s_waitcnt lgkmcnt(4)\n"
      "v_mfma_f32_32x32x16_bf16 v[16:31], v[120:123], v[184:187], v[16:31]\n"
      "ds_read_b128 v[44:47], v2 offset:19456\n"
      "v_mov_b64 v[48:49], 0\n"
      "v_mov_b64 v[50:51], 0\n"
      "v_mov_b64 v[52:53], 0\n"
      "v_mov_b64 v[54:55], 0\n"
      "s_waitcnt lgkmcnt(4)\n"
      "v_mfma_f32_32x32x16_bf16 v[16:31], v[124:127], v[188:191], v[16:31]\n"
      "ds_read_b128 v[112:115], v2 offset:20480\n"
      "v_mov_b64 v[56:57], 0\n"
      "v_mov_b64 v[58:59], 0\n"
      "v_mov_b64 v[60:61], 0\n"
      "v_mov_b64 v[62:63], 0\n"
      "s_waitcnt lgkmcnt(4)\n"
      "v_mfma_f32_32x32x16_bf16 v[16:31], v[32:35], v[192:195], v[16:31]\n"
      "ds_read_b128 v[116:119], v2 offset:21504\n"
      "s_waitcnt lgkmcnt(4)\n"
      "v_mfma_f32_32x32x16_bf16 v[16:31], v[36:39], v[196:199], v[16:31]\n"
      "ds_read_b128 v[120:123], v2 offset:22528\n"
      "s_lshl_b32 s28, s16, 11\n"
      "s_add_u32 s28, s28, 151552\n"
      "v_add_u32 v184, s28, v0\n"
      "v_add_u32 v185, s24, v0\n"
      "v_mov_b64 v[182:183], 0\n"
      "s_waitcnt lgkmcnt(4)\n"
      "v_mfma_f32_32x32x16_bf16 v[16:31], v[40:43], v[200:203], v[16:31]\n"
      "ds_read_b128 v[124:127], v2 offset:23552\n"
      "s_add_u32 s28, s16, 1\n"
      "s_and_b32 s28, s28, 3\n"
      "s_lshl_b32 s29, s28, 11\n"
      "s_add_u32 s29, s29, 151552\n"
      "v_add_u32 v192, s29, v0\n"
      "s_waitcnt lgkmcnt(4)\n"
      "v_mfma_f32_32x32x16_bf16 v[16:31], v[44:47], v[204:207], v[16:31]\n"
      "ds_read_b128 v[32:35], v2 offset:24576\n"
      "s_mul_i32 s28, s28, 12288\n"
      "s_add_u32 s28, s28, 94208\n"
      "v_add_u32 v188, s28, v0\n"
      "s_waitcnt lgkmcnt(4)\n"
      "v_mfma_f32_32x32x16_bf16 v[16:31], v[112:115], v[208:211], v[16:31]\n"
      "ds_read_b128 v[36:39], v2 offset:25600\n"
      "s_add_u32 s28, s16, 2\n"
      "s_and_b32 s28, s28, 3\n"
      "s_lshl_b32 s29, s28, 11\n"
      "s_add_u32 s29, s29, 151552\n"
      "v_add_u32 v193, s29, v0\n"
      "s_waitcnt lgkmcnt(4)\n"
      "v_mfma_f32_32x32x16_bf16 v[16:31], v[116:119], v[212:215], v[16:31]\n"
      "ds_read_b128 v[40:43], v2 offset:26624\n"
      "s_mul_i32 s28, s28, 12288\n"
      "s_add_u32 s28, s28, 98304\n"
      "v_add_u32 v189, s28, v0\n"
      "s_waitcnt lgkmcnt(4)\n"
      "v_mfma_f32_32x32x16_bf16 v[16:31], v[120:123], v[216:219], v[16:31]\n"
      "ds_read_b128 v[44:47], v2 offset:27648\n"
      "s_add_u32 s28, s16, 3\n"
      "s_and_b32 s28, s28, 3\n"
      "s_lshl_b32 s29, s28, 11\n"
      "s_add_u32 s29, s29, 151552\n"
      "v_add_u32 v194, s29, v0\n"
      "s_waitcnt lgkmcnt(4)\n"
      "v_mfma_f32_32x32x16_bf16 v[16:31], v[124:127], v[220:223], v[16:31]\n"
      "s_mul_i32 s28, s28, 12288\n"
      "s_add_u32 s28, s28, 102400\n"
      "v_add_u32 v190, s28, v0\n"
      "s_waitcnt lgkmcnt(3)\n"
      "v_mfma_f32_32x32x16_bf16 v[16:31], v[32:35], v[224:227], v[16:31]\n"
      "s_add_u32 s28, s16, 4\n"
      "s_and_b32 s28, s28, 3\n"
      "s_lshl_b32 s29, s28, 11\n"
      "s_add_u32 s29, s29, 151552\n"
      "v_add_u32 v195, s29, v0\n"
      "s_waitcnt lgkmcnt(2)\n"
      "v_mfma_f32_32x32x16_bf16 v[16:31], v[36:39], v[228:231], v[16:31]\n"
      "s_mul_i32 s28, s28, 12288\n"
      "s_add_u32 s28, s28, 94208\n"
      "v_add_u32 v191, s28, v0\n"
      "s_waitcnt lgkmcnt(1)\n"
      "v_mfma_f32_32x32x16_bf16 v[16:31], v[40:43], v[232:235], v[16:31]\n"
      "v_mul_f32 v198, 0x3e8b97f6, v88\n"
      "v_mul_f32 v199, 0x3e8b97f6, v88\n"
      "v_mov_b64 v[88:89], 0\n"
      "s_waitcnt lgkmcnt(0)\n"
      "v_mfma_f32_32x32x16_bf16 v[16:31], v[44:47], v[236:239], v[16:31]\n"
      "s_cmp_eq_u32 s16, 0\n"
      "s_cbranch_scc0 .Lif46_%=\n"
      "s_add_u32 s28, s26, s27\n"
      "s_lshl_b32 s29, s14, 8\n"
      "s_add_u32 s28, s28, s29\n"
      "s_mov_b32 exec_lo, 65535\n"
      "s_mov_b32 exec_hi, 0\n"
      "buffer_load_dwordx2 v[196:197], v6, s[0:3], s28 offen sc1\n"
      "s_mov_b64 exec, -1\n"
      ".Lif46_%=:\n"
      "v_mov_b64 v[232:233], -1\n"
      "v_mov_b64 v[234:235], -1\n"
      "s_nop 7 ; hz\n"
      "v_pk_mul_f32 v[16:17], v[16:17], v[198:199]\n"
      "v_pk_mul_f32 v[18:19], v[18:19], v[198:199]\n"
      "v_pk_mul_f32 v[20:21], v[20:21], v[198:199]\n"
      "v_pk_mul_f32 v[22:23], v[22:23], v[198:199]\n"
      "v_pk_mul_f32 v[24:25], v[24:25], v[198:199]\n"
      "v_pk_mul_f32 v[26:27], v[26:27], v[198:199]\n"
      "v_pk_mul_f32 v[28:29], v[28:29], v[198:199]\n"
      "v_pk_mul_f32 v[30:31], v[30:31], v[198:199]\n"
      "v_cvt_pk_bf16_f32 v176, v16, v17\n"
      "v_cvt_pk_bf16_f32 v177, v18, v19\n"
      "v_cvt_pk_bf16_f32 v178, v20, v21\n"
      "v_cvt_pk_bf16_f32 v179, v22, v23\n"
      "v_cvt_pk_bf16_f32 v180, v24, v25\n"
      "v_cvt_pk_bf16_f32 v181, v26, v27\n"
      "s_mov_b32 exec_hi, 0\n"
      "v_cvt_pk_bf16_f32 v182, v28, v29\n"
      "v_cvt_pk_bf16_f32 v183, v30, v31\n"
      "s_mov_b32 exec_hi, -1\n"
      "v_mfma_f32_32x32x16_bf16 v[112:127], v[160:163], v[176:179], 0\n"
      "v_mfma_f32_32x32x16_bf16 v[112:127], v[164:167], v[180:183], v[112:127]\n"
      "ds_write_b128 v184, v[176:179]\n"
      "s_nop 7 ; hz\n"
      "s_nop 2 ; hz\n"
      "v_max3_f32 v186, v112, v113, v114\n"
      "v_max3_f32 v187, v115, v116, v117\n"
      "v_max3_f32 v186, v186, v118, v119\n"
      "v_max3_f32 v187, v187, v120, v121\n"
      "v_max3_f32 v186, v186, v122, v123\n"
      "v_max3_f32 v187, v187, v124, v125\n"
      "v_max3_f32 v186, v186, v126, v127\n"
      "v_max_f32 v186, v186, v187\n"
      "s_mov_b32 s69, 0\n"
      "s_mov_b32 s68, 0\n"
      "s_mov_b32 s4, 0x4d800000\n"
      "v_mov_b32 v187, v186\n"
      "s_nop 1 ; hz\n"
      "v_permlane32_swap_b32 v186, v187\n"
      "v_max_f32 v186, v186, v187\n"
      "v_sub_f32 v186, 0, v186\n"
      "v_cvt_pk_bf16_f32 v187, v186, v186\n"
      "v_and_b32 v187, 0xffff0000, v187\n"
      "v_sub_f32 v187, v186, v187\n"
      "s_mov_b32 exec_lo, 0\n"
      "v_cvt_pk_bf16_f32 v182, v186, v187\n"
      "v_and_b32 v186, 0xffff0000, v182\n"
      "v_sub_f32 v186, v187, v186\n"
      "v_cvt_pk_bf16_f32 v183, v186, 0\n"
      "s_mov_b64 exec, -1\n"
      "ds_write_b128 v184, v[180:183] offset:1024\n"
      "v_mfma_f32_32x32x16_bf16 v[112:127], v[160:163], v[176:179], 0\n"
      "v_mfma_f32_32x32x16_bf16 v[112:127], v[164:167], v[180:183], v[112:127]\n"
      "s_cmp_eq_u32 s16, 0\n"
      "s_cbranch_scc0 .Lif50_%=\n"
      "s_add_u32 s28, s26, s27\n"
      "s_lshl_b32 s29, s14, 8\n"
      "s_add_u32 s28, s28, s29\n"
      "s_mov_b32 s71, 0x10000\n"
      "s_mov_b32 exec_lo, 65535\n"
      "s_mov_b32 exec_hi, 0\n"
      "s_branch .Lpc49_%=\n"
      ".Lpl47_%=:\n"
      "buffer_load_dwordx2 v[196:197], v6, s[0:3], s28 offen sc1\n"
      ".Lpc49_%=:\n"
      "s_waitcnt vmcnt(0)\n"
      "v_cmp_eq_u64 vcc, s[12:13], v[196:197]\n"
      "s_nop 0 ; hz\n"
      "s_cmp_eq_u64 vcc, exec\n"
      "s_cbranch_scc1 .Lpo48_%=\n"
      "s_sleep 1\n"
      "s_sub_u32 s71, s71, 1\n"
      "s_cmp_lg_u32 s71, 0\n"
      "s_cbranch_scc1 .Lpl47_%=\n"
      ".Lpo48_%=:\n"
      "s_mov_b64 exec, -1\n"
      ".Lif50_%=:\n"
      "s_add_u32 s27, s27, 4096\n"
      "s_waitcnt vmcnt(0) lgkmcnt(0)\n"
      "s_barrier\n"
      ".Latt51_%=:\n"
      "s_lshl_b32 s30, s14, 12\n"
      "s_add_u32 s30, s30, 0x1000\n"
      "s_and_b32 s30, s30, 0xffff\n"
      "buffer_load_dwordx4 a[192:195], v185, s[0:3], s30 offen offset:0 sc1\n"
      "buffer_load_dwordx4 a[196:199], v185, s[0:3], s30 offen offset:1024 sc1\n"
      "ds_read_b128 v[16:19], v192\n"
      "ds_read_b128 v[20:23], v192 offset:1024\n"
      "ds_read_b128 v[24:27], v193\n"
      "ds_read_b128 v[28:31], v193 offset:1024\n"
      "ds_read_b128 v[32:35], v194\n"
      "ds_read_b128 v[36:39], v194 offset:1024\n"
      "s_add_u32 s28, s17, s69\n"
      "s_cmp_eq_u32 s28, 1\n"
      "s_cselect_b64 vcc, -1, 0\n"
      "buffer_load_dwordx4 a[200:203], v185, s[0:3], s30 offen offset:2048 sc1\n"
      "buffer_load_dwordx4 a[204:207], v185, s[0:3], s30 offen offset:3072 sc1\n"
      "v_exp_f32 v112, v112\n"
      "v_exp_f32 v113, v113\n"
      "v_exp_f32 v114, v114\n"
      "v_exp_f32 v115, v115\n"
      "s_add_u32 s30, s30, 0x1000\n"
      "s_and_b32 s30, s30, 0xffff\n"
      "buffer_load_dwordx4 a[208:211], v185, s[0:3], s30 offen offset:0 sc1\n"
      "v_exp_f32 v116, v116\n"
      "v_exp_f32 v117, v117\n"
      "v_exp_f32 v118, v118\n"
      "v_exp_f32 v119, v119\n"
      "s_waitcnt lgkmcnt(4)\n"
      "v_mfma_f32_32x32x16_bf16 v[128:143], v[160:163], v[16:19], 0\n"
      "buffer_load_dwordx4 a[212:215], v185, s[0:3], s30 offen offset:1024 sc1\n"
      "v_exp_f32 v120, v120\n"
      "v_exp_f32 v121, v121\n"
      "v_exp_f32 v122, v122\n"
      "v_exp_f32 v123, v123\n"
      "v_mfma_f32_32x32x16_bf16 v[128:143], v[164:167], v[20:23], v[128:143]\n"
      "s_waitcnt lgkmcnt(0)\n"
      "v_exp_f32 v124, v124\n"
      "v_exp_f32 v125, v125\n"
      "v_exp_f32 v126, v126\n"
      "v_exp_f32 v127, v127\n"
      "v_smfmac_f32_32x32x32_bf16 v[96:111], v[168:171], v[112:119], v11\n"
      "v_mfma_f32_32x32x16_bf16 v[144:159], v[160:163], v[24:27], 0\n"
      "buffer_load_dwordx4 a[216:219], v185, s[0:3], s30 offen offset:2048 sc1\n"
      "s_nop 3 ; hz\n"
      "v_exp_f32 v128, v128\n"
      "v_exp_f32 v129, v129\n"
      "v_exp_f32 v130, v130\n"
      "v_exp_f32 v131, v131\n"
      "v_mfma_f32_32x32x16_bf16 v[144:159], v[164:167], v[28:31], v[144:159]\n"
      "buffer_load_dwordx4 a[220:223], v185, s[0:3], s30 offen offset:3072 sc1\n"
      "v_exp_f32 v132, v132\n"
      "v_exp_f32 v133, v133\n"
      "v_exp_f32 v134, v134\n"
      "v_exp_f32 v135, v135\n"
      "v_smfmac_f32_32x32x32_bf16 v[96:111], v[172:175], v[120:127], v11\n"
      "v_exp_f32 v136, v136\n"
      "v_exp_f32 v137, v137\n"
      "v_exp_f32 v138, v138\n"
      "v_exp_f32 v139, v139\n"
      "v_smfmac_f32_32x32x32_bf16 v[48:63], v[168:171], v[128:135], v11\n"
      "v_exp_f32 v140, v140\n"
      "v_exp_f32 v141, v141\n"
      "v_exp_f32 v142, v142\n"
      "v_exp_f32 v143, v143\n"
      "v_mfma_f32_32x32x16_bf16 v[112:127], v[160:163], v[32:35], 0\n"
      "s_add_u32 s30, s30, 0x1000\n"
      "s_and_b32 s30, s30, 0xffff\n"
      "buffer_load_dwordx4 a[224:227], v185, s[0:3], s30 offen offset:0 sc1\n"
      "v_exp_f32 v144, v144\n"
      "v_exp_f32 v145, v145\n"
      "v_exp_f32 v146, v146\n"
      "v_exp_f32 v147, v147\n"
      "v_mfma_f32_32x32x16_bf16 v[112:127], v[164:167], v[36:39], v[112:127]\n"
      "buffer_load_dwordx4 a[228:231], v185, s[0:3], s30 offen offset:1024 sc1\n"
      "v_exp_f32 v148, v148\n"
      "v_exp_f32 v149, v149\n"
      "v_exp_f32 v150, v150\n"
      "v_exp_f32 v151, v151\n"
      "v_smfmac_f32_32x32x32_bf16 v[48:63], v[172:175], v[136:143], v11\n"
      "v_exp_f32 v152, v152\n"
      "v_exp_f32 v153, v153\n"
      "v_exp_f32 v154, v154\n"
      "v_exp_f32 v155, v155\n"
      "v_smfmac_f32_32x32x32_bf16 v[64:79], v[168:171], v[144:151], v11\n"
      "v_exp_f32 v156, v156\n"
      "v_exp_f32 v157, v157\n"
      "v_exp_f32 v158, v158\n"
      "v_exp_f32 v159, v159\n"
      "s_waitcnt vmcnt(8)\n"
      "v_mfma_f32_32x32x16_bf16 v[128:143], a[192:195], v[16:19], 0\n"
      "buffer_load_dwordx4 a[232:235], v185, s[0:3], s30 offen offset:2048 sc1\n"
      "v_exp_f32 v112, v112\n"
      "v_exp_f32 v113, v113\n"
      "v_exp_f32 v114, v114\n"
      "v_exp_f32 v115, v115\n"
      "v_mfma_f32_32x32x16_bf16 v[128:143], a[196:199], v[20:23], v[128:143]\n"
      "buffer_load_dwordx4 a[236:239], v185, s[0:3], s30 offen offset:3072 sc1\n"
      "v_exp_f32 v116, v116\n"
      "v_exp_f32 v117, v117\n"
      "v_exp_f32 v118, v118\n"
      "v_exp_f32 v119, v119\n"
      "v_smfmac_f32_32x32x32_bf16 v[64:79], v[172:175], v[152:159], v11\n"
      "v_exp_f32 v120, v120\n"
      "v_exp_f32 v121, v121\n"
      "v_exp_f32 v122, v122\n"
      "v_exp_f32 v123, v123\n"
      "v_smfmac_f32_32x32x32_bf16 v[80:95], v[168:171], v[112:119], v11\n"
      "v_exp_f32 v124, v124\n"
      "v_exp_f32 v125, v125\n"
      "v_exp_f32 v126, v126\n"
      "v_exp_f32 v127, v127\n"
      "v_mfma_f32_32x32x16_bf16 v[144:159], a[192:195], v[24:27], 0\n"
      "s_add_u32 s30, s30, 0x1000\n"
      "s_and_b32 s30, s30, 0xffff\n"
      "buffer_load_dwordx4 a[240:243], v185, s[0:3], s30 offen offset:0 sc1\n"
      "v_exp_f32 v128, v128\n"
      "v_exp_f32 v129, v129\n"
      "v_exp_f32 v130, v130\n"
      "v_exp_f32 v131, v131\n"
      "v_mfma_f32_32x32x16_bf16 v[144:159], a[196:199], v[28:31], v[144:159]\n"
      "buffer_load_dwordx4 a[244:247], v185, s[0:3], s30 offen offset:1024 sc1\n"
      "v_exp_f32 v132, v132\n"
      "v_exp_f32 v133, v133\n"
      "v_exp_f32 v134, v134\n"
      "v_exp_f32 v135, v135\n"
      "v_smfmac_f32_32x32x32_bf16 v[80:95], v[172:175], v[120:127], v11\n"
      "v_exp_f32 v136, v136\n"
      "v_exp_f32 v137, v137\n"
      "v_exp_f32 v138, v138\n"
      "v_exp_f32 v139, v139\n"
      "s_waitcnt vmcnt(10)\n"
      "v_smfmac_f32_32x32x32_bf16 v[48:63], a[200:203], v[128:135], v11\n"
      "v_exp_f32 v140, v140\n"
      "v_exp_f32 v141, v141\n"
      "v_exp_f32 v142, v142\n"
      "v_exp_f32 v143, v143\n"
      "v_mfma_f32_32x32x16_bf16 v[112:127], a[192:195], v[32:35], 0\n"
      "buffer_load_dwordx4 a[248:251], v185, s[0:3], s30 offen offset:2048 sc1\n"
      "v_exp_f32 v144, v144\n"
      "v_exp_f32 v145, v145\n"
      "v_exp_f32 v146, v146\n"
      "v_exp_f32 v147, v147\n"
      "v_mfma_f32_32x32x16_bf16 v[112:127], a[196:199], v[36:39], v[112:127]\n"
      "buffer_load_dwordx4 a[252:255], v185, s[0:3], s30 offen offset:3072 sc1\n"
      "v_exp_f32 v148, v148\n"
      "v_exp_f32 v149, v149\n"
      "v_exp_f32 v150, v150\n"
      "v_exp_f32 v151, v151\n"
      "v_smfmac_f32_32x32x32_bf16 v[48:63], a[204:207], v[136:143], v11\n"
      "v_exp_f32 v152, v152\n"
      "v_exp_f32 v153, v153\n"
      "v_exp_f32 v154, v154\n"
      "v_exp_f32 v155, v155\n"
      "v_smfmac_f32_32x32x32_bf16 v[64:79], a[200:203], v[144:151], v11\n"
      "v_exp_f32 v156, v156\n"
      "v_exp_f32 v157, v157\n"
      "v_exp_f32 v158, v158\n"
      "v_exp_f32 v159, v159\n"
      "v_mfma_f32_32x32x16_bf16 v[128:143], a[192:195], v[176:179], 0\n"
      "s_add_u32 s30, s30, 0x1000\n"
      "s_and_b32 s30, s30, 0xffff\n"
      "buffer_load_dwordx4 v[200:203], v185, s[0:3], s30 offen offset:0 sc1\n"
      "v_exp_f32 v112, v112\n"
      "v_exp_f32 v113, v113\n"
      "v_exp_f32 v114, v114\n"
      "v_exp_f32 v115, v115\n"
      "v_mfma_f32_32x32x16_bf16 v[128:143], a[196:199], v[180:183], v[128:143]\n"
      "buffer_load_dwordx4 v[204:207], v185, s[0:3], s30 offen offset:1024 sc1\n"
      "v_exp_f32 v116, v116\n"
      "v_exp_f32 v117, v117\n"
      "v_exp_f32 v118, v118\n"
      "v_exp_f32 v119, v119\n"
      "v_smfmac_f32_32x32x32_bf16 v[64:79], a[204:207], v[152:159], v11\n"
      "v_exp_f32 v120, v120\n"
      "v_exp_f32 v121, v121\n"
      "v_exp_f32 v122, v122\n"
      "v_exp_f32 v123, v123\n"
      "v_smfmac_f32_32x32x32_bf16 v[80:95], a[200:203], v[112:119], v11\n"
      "v_exp_f32 v124, v124\n"
      "v_exp_f32 v125, v125\n"
      "v_exp_f32 v126, v126\n"
      "v_exp_f32 v127, v127\n"
      "s_waitcnt vmcnt(10)\n"
      "v_mfma_f32_32x32x16_bf16 v[144:159], a[208:211], v[16:19], 0\n"
      "buffer_load_dwordx4 v[208:211], v185, s[0:3], s30 offen offset:2048 sc1\n"
      "v_exp_f32 v128, v128\n"
      "v_exp_f32 v129, v129\n"
      "v_exp_f32 v130, v130\n"
      "v_exp_f32 v131, v131\n"
      "v_mfma_f32_32x32x16_bf16 v[144:159], a[212:215], v[20:23], v[144:159]\n"
      "v_exp_f32 v132, v132\n"
      "v_exp_f32 v133, v133\n"
      "v_exp_f32 v134, v134\n"
      "v_exp_f32 v135, v135\n"
      "v_smfmac_f32_32x32x32_bf16 v[80:95], a[204:207], v[120:127], v11\n"
      "v_exp_f32 v136, v136\n"
      "v_exp_f32 v137, v137\n"
      "v_exp_f32 v138, v138\n"
      "v_exp_f32 v139, v139\n"
      "v_smfmac_f32_32x32x32_bf16 v[96:111], a[200:203], v[128:135], v11\n"
      "v_exp_f32 v140, v140\n"
      "v_exp_f32 v141, v141\n"
      "v_exp_f32 v142, v142\n"
      "v_exp_f32 v143, v143\n"
      "v_mfma_f32_32x32x16_bf16 v[112:127], a[208:211], v[24:27], 0\n"
      "buffer_load_dwordx4 v[212:215], v185, s[0:3], s30 offen offset:3072 sc1\n"
      "v_exp_f32 v144, v144\n"
      "v_exp_f32 v145, v145\n"
      "v_exp_f32 v146, v146\n"
      "v_exp_f32 v147, v147\n"
      "v_mfma_f32_32x32x16_bf16 v[112:127], a[212:215], v[28:31], v[112:127]\n"
      "v_exp_f32 v148, v148\n"
      "v_exp_f32 v149, v149\n"
      "v_exp_f32 v150, v150\n"
      "v_exp_f32 v151, v151\n"
      "v_smfmac_f32_32x32x32_bf16 v[96:111], a[204:207], v[136:143], v11\n"
      "v_exp_f32 v152, v152\n"
      "v_exp_f32 v153, v153\n"
      "v_exp_f32 v154, v154\n"
      "v_exp_f32 v155, v155\n"
      "v_smfmac_f32_32x32x32_bf16 v[48:63], a[216:219], v[144:151], v11\n"
      "v_exp_f32 v156, v156\n"
      "v_exp_f32 v157, v157\n"
      "v_exp_f32 v158, v158\n"
      "v_exp_f32 v159, v159\n"
      "v_mfma_f32_32x32x16_bf16 v[128:143], a[208:211], v[32:35], 0\n"
      "s_add_u32 s30, s30, 0x1000\n"
      "s_and_b32 s30, s30, 0xffff\n"
      "buffer_load_dwordx4 v[216:219], v185, s[0:3], s30 offen offset:0 sc1\n"
      "v_exp_f32 v112, v112\n"
      "v_exp_f32 v113, v113\n"
      "v_exp_f32 v114, v114\n"
      "v_exp_f32 v115, v115\n"
      "v_mfma_f32_32x32x16_bf16 v[128:143], a[212:215], v[36:39], v[128:143]\n"
      "v_exp_f32 v116, v116\n"
      "v_exp_f32 v117, v117\n"
      "v_exp_f32 v118, v118\n"
      "v_exp_f32 v119, v119\n"
      "v_smfmac_f32_32x32x32_bf16 v[48:63], a[220:223], v[152:159], v11\n"
      "v_exp_f32 v120, v120\n"
      "v_exp_f32 v121, v121\n"
      "v_exp_f32 v122, v122\n"
      "v_exp_f32 v123, v123\n"
      "v_smfmac_f32_32x32x32_bf16 v[64:79], a[216:219], v[112:119], v11\n"
      "v_exp_f32 v124, v124\n"
      "v_exp_f32 v125, v125\n"
      "v_exp_f32 v126, v126\n"
      "v_exp_f32 v127, v127\n"
      "v_mfma_f32_32x32x16_bf16 v[144:159], a[208:211], v[176:179], 0\n"
      "buffer_load_dwordx4 v[220:223], v185, s[0:3], s30 offen offset:1024 sc1\n"
      "v_exp_f32 v128, v128\n"
      "v_exp_f32 v129, v129\n"
      "v_exp_f32 v130, v130\n"
      "v_exp_f32 v131, v131\n"
      "v_mfma_f32_32x32x16_bf16 v[144:159], a[212:215], v[180:183], v[144:159]\n"
      "s_cbranch_vccz .Lps52_%=\n"
      "s_add_u32 s31, s78, 131072\n"
      "buffer_store_dwordx4 v[232:235], v0, s[0:3], s31 offen offset:0 sc1\n"
      "s_add_u32 s31, s78, 131072\n"
      "buffer_store_dwordx4 v[232:235], v0, s[0:3], s31 offen offset:1024 sc1\n"
      "s_add_u32 s31, s76, 229376\n"
      "buffer_store_dwordx4 v[232:235], v0, s[0:3], s31 offen offset:0 sc1\n"
      "s_lshl_b32 s28, s16, 13\n"
      "s_add_u32 s28, s28, 32768\n"
      "s_mov_b32 m0, s28\n"
      "s_add_u32 s29, s73, 32768\n"
      "buffer_load_dwordx4 v0, s[0:3], s29 offen offset:0 sc1 lds\n"
      "buffer_load_dwordx4 v0, s[0:3], s29 offen offset:1024 sc1 lds\n"
      ".Lps52_%=:\n"
      "v_exp_f32 v132, v132\n"
      "v_exp_f32 v133, v133\n"
      "v_exp_f32 v134, v134\n"
      "v_exp_f32 v135, v135\n"
      "v_smfmac_f32_32x32x32_bf16 v[64:79], a[220:223], v[120:127], v11\n"
      "v_exp_f32 v136, v136\n"
      "v_exp_f32 v137, v137\n"
      "v_exp_f32 v138, v138\n"
      "v_exp_f32 v139, v139\n"
      "v_smfmac_f32_32x32x32_bf16 v[80:95], a[216:219], v[128:135], v11\n"
      "v_exp_f32 v140, v140\n"
      "v_exp_f32 v141, v141\n"
      "v_exp_f32 v142, v142\n"
      "v_exp_f32 v143, v143\n"
      "s_waitcnt vmcnt(10)\n"
      "v_mfma_f32_32x32x16_bf16 v[112:127], a[224:227], v[16:19], 0\n"
      "buffer_load_dwordx4 v[224:227], v185, s[0:3], s30 offen offset:2048 sc1\n"
      "v_exp_f32 v144, v144\n"
      "v_exp_f32 v145, v145\n"
      "v_exp_f32 v146, v146\n"
      "v_exp_f32 v147, v147\n"
      "v_mfma_f32_32x32x16_bf16 v[112:127], a[228:231], v[20:23], v[112:127]\n"
      "v_exp_f32 v148, v148\n"
      "v_exp_f32 v149, v149\n"
      "v_exp_f32 v150, v150\n"
      "v_exp_f32 v151, v151\n"
      "v_smfmac_f32_32x32x32_bf16 v[80:95], a[220:223], v[136:143], v11\n"
      "v_exp_f32 v152, v152\n"
      "v_exp_f32 v153, v153\n"
      "v_exp_f32 v154, v154\n"
      "v_exp_f32 v155, v155\n"
      "v_smfmac_f32_32x32x32_bf16 v[96:111], a[216:219], v[144:151], v11\n"
      "v_exp_f32 v156, v156\n"
      "v_exp_f32 v157, v157\n"
      "v_exp_f32 v158, v158\n"
      "v_exp_f32 v159, v159\n"
      "v_mfma_f32_32x32x16_bf16 v[128:143], a[224:227], v[24:27], 0\n"
      "buffer_load_dwordx4 v[228:231], v185, s[0:3], s30 offen offset:3072 sc1\n"
      "v_exp_f32 v112, v112\n"
      "v_exp_f32 v113, v113\n"
      "v_exp_f32 v114, v114\n"
      "v_exp_f32 v115, v115\n"
      "v_mfma_f32_32x32x16_bf16 v[128:143], a[228:231], v[28:31], v[128:143]\n"
      "v_exp_f32 v116, v116\n"
      "v_exp_f32 v117, v117\n"
      "v_exp_f32 v118, v118\n"
      "v_exp_f32 v119, v119\n"
      "v_smfmac_f32_32x32x32_bf16 v[96:111], a[220:223], v[152:159], v11\n"
      "v_exp_f32 v120, v120\n"
      "v_exp_f32 v121, v121\n"
      "v_exp_f32 v122, v122\n"
      "v_exp_f32 v123, v123\n"
      "v_smfmac_f32_32x32x32_bf16 v[48:63], a[232:235], v[112:119], v11\n"
      "v_exp_f32 v124, v124\n"
      "v_exp_f32 v125, v125\n"
      "v_exp_f32 v126, v126\n"
      "v_exp_f32 v127, v127\n"
      "v_mfma_f32_32x32x16_bf16 v[144:159], a[224:227], v[32:35], 0\n"
      "s_add_u32 s30, s30, 0x1000\n"
      "s_and_b32 s30, s30, 0xffff\n"
      "buffer_load_dwordx4 a[192:195], v185, s[0:3], s30 offen offset:0 sc1\n"
      "v_exp_f32 v128, v128\n"
      "v_exp_f32 v129, v129\n"
      "v_exp_f32 v130, v130\n"
      "v_exp_f32 v131, v131\n"
      "v_mfma_f32_32x32x16_bf16 v[144:159], a[228:231], v[36:39], v[144:159]\n"
      "v_exp_f32 v132, v132\n"
      "v_exp_f32 v133, v133\n"
      "v_exp_f32 v134, v134\n"
      "v_exp_f32 v135, v135\n"
      "v_smfmac_f32_32x32x32_bf16 v[48:63], a[236:239], v[120:127], v11\n"
      "v_exp_f32 v136, v136\n"
      "v_exp_f32 v137, v137\n"
      "v_exp_f32 v138, v138\n"
      "v_exp_f32 v139, v139\n"
      "v_smfmac_f32_32x32x32_bf16 v[64:79], a[232:235], v[128:135], v11\n"
      "v_exp_f32 v140, v140\n"
      "v_exp_f32 v141, v141\n"
      "v_exp_f32 v142, v142\n"
      "v_exp_f32 v143, v143\n"
      "v_mfma_f32_32x32x16_bf16 v[112:127], a[224:227], v[176:179], 0\n"
      "buffer_load_dwordx4 a[196:199], v185, s[0:3], s30 offen offset:1024 sc1\n"
      "v_exp_f32 v144, v144\n"
      "v_exp_f32 v145, v145\n"
      "v_exp_f32 v146, v146\n"
      "v_exp_f32 v147, v147\n"
      "v_mfma_f32_32x32x16_bf16 v[112:127], a[228:231], v[180:183], v[112:127]\n"
      "v_exp_f32 v148, v148\n"
      "v_exp_f32 v149, v149\n"
      "v_exp_f32 v150, v150\n"
      "v_exp_f32 v151, v151\n"
      "v_smfmac_f32_32x32x32_bf16 v[64:79], a[236:239], v[136:143], v11\n"
      "v_exp_f32 v152, v152\n"
      "v_exp_f32 v153, v153\n"
      "v_exp_f32 v154, v154\n"
      "v_exp_f32 v155, v155\n"
      "v_smfmac_f32_32x32x32_bf16 v[80:95], a[232:235], v[144:151], v11\n"
      "v_exp_f32 v156, v156\n"
      "v_exp_f32 v157, v157\n"
      "v_exp_f32 v158, v158\n"
      "v_exp_f32 v159, v159\n"
      "s_waitcnt vmcnt(10)\n"
      "v_mfma_f32_32x32x16_bf16 v[128:143], a[240:243], v[16:19], 0\n"
      "buffer_load_dwordx4 a[200:203], v185, s[0:3], s30 offen offset:2048 sc1\n"
      "v_exp_f32 v112, v112\n"
      "v_exp_f32 v113, v113\n"
      "v_exp_f32 v114, v114\n"
      "v_exp_f32 v115, v115\n"
      "v_mfma_f32_32x32x16_bf16 v[128:143], a[244:247], v[20:23], v[128:143]\n"
      "v_exp_f32 v116, v116\n"
      "v_exp_f32 v117, v117\n"
      "v_exp_f32 v118, v118\n"
      "v_exp_f32 v119, v119\n"
      "v_smfmac_f32_32x32x32_bf16 v[80:95], a[236:239], v[152:159], v11\n"
      "v_exp_f32 v120, v120\n"
      "v_exp_f32 v121, v121\n"
      "v_exp_f32 v122, v122\n"
      "v_exp_f32 v123, v123\n"
      "v_smfmac_f32_32x32x32_bf16 v[96:111], a[232:235], v[112:119], v11\n"
      "v_exp_f32 v124, v124\n"
      "v_exp_f32 v125, v125\n"
      "v_exp_f32 v126, v126\n"
      "v_exp_f32 v127, v127\n"
      "v_mfma_f32_32x32x16_bf16 v[144:159], a[240:243], v[24:27], 0\n"
      "buffer_load_dwordx4 a[204:207], v185, s[0:3], s30 offen offset:3072 sc1\n"
      "v_exp_f32 v128, v128\n"
      "v_exp_f32 v129, v129\n"
      "v_exp_f32 v130, v130\n"
      "v_exp_f32 v131, v131\n"
      "v_mfma_f32_32x32x16_bf16 v[144:159], a[244:247], v[28:31], v[144:159]\n"
      "s_cbranch_vccz .Lps53_%=\n"
      "s_add_u32 s31, s76, 229376\n"
      "buffer_store_dwordx4 v[232:235], v0, s[0:3], s31 offen offset:1024 sc1\n"
      "s_add_u32 s31, s77, 32768\n"
      "s_mov_b32 exec_lo, 0xffff\n"
      "s_mov_b32 exec_hi, 0\n"
      "buffer_store_dwordx4 v[232:235], v0, s[0:3], s31 offen offset:0 sc1\n"
      "s_mov_b64 exec, -1\n"
      "s_add_u32 s31, s80, 32768\n"
      "s_mov_b32 exec_lo, 0xffffffff\n"
      "s_mov_b32 exec_hi, 0\n"
      "buffer_store_dwordx4 v[232:235], v0, s[0:3], s31 offen offset:0 sc1\n"
      "s_mov_b64 exec, -1\n"
      "buffer_load_dwordx4 v0, s[0:3], s29 offen offset:2048 sc1 lds\n"
      "buffer_load_dwordx4 v0, s[0:3], s29 offen offset:3072 sc1 lds\n"
      ".Lps53_%=:\n"
      "v_exp_f32 v132, v132\n"
      "v_exp_f32 v133, v133\n"
      "v_exp_f32 v134, v134\n"
      "v_exp_f32 v135, v135\n"
      "v_smfmac_f32_32x32x32_bf16 v[96:111], a[236:239], v[120:127], v11\n"
      "v_exp_f32 v136, v136\n"
      "v_exp_f32 v137, v137\n"
      "v_exp_f32 v138, v138\n"
      "v_exp_f32 v139, v139\n"
      "v_smfmac_f32_32x32x32_bf16 v[48:63], a[248:251], v[128:135], v11\n"
      "v_exp_f32 v140, v140\n"
      "v_exp_f32 v141, v141\n"
      "v_exp_f32 v142, v142\n"
      "v_exp_f32 v143, v143\n"
      "v_mfma_f32_32x32x16_bf16 v[112:127], a[240:243], v[32:35], 0\n"
      "s_add_u32 s30, s30, 0x1000\n"
      "s_and_b32 s30, s30, 0xffff\n"
      "buffer_load_dwordx4 a[208:211], v185, s[0:3], s30 offen offset:0 sc1\n"
      "v_exp_f32 v144, v144\n"
      "v_exp_f32 v145, v145\n"
      "v_exp_f32 v146, v146\n"
      "v_exp_f32 v147, v147\n"
      "v_mfma_f32_32x32x16_bf16 v[112:127], a[244:247], v[36:39], v[112:127]\n"
      "v_exp_f32 v148, v148\n"
      "v_exp_f32 v149, v149\n"
      "v_exp_f32 v150, v150\n"
      "v_exp_f32 v151, v151\n"
      "v_smfmac_f32_32x32x32_bf16 v[48:63], a[252:255], v[136:143], v11\n"
      "v_exp_f32 v152, v152\n"
      "v_exp_f32 v153, v153\n"
      "v_exp_f32 v154, v154\n"
      "v_exp_f32 v155, v155\n"
      "v_smfmac_f32_32x32x32_bf16 v[64:79], a[248:251], v[144:151], v11\n"
      "v_exp_f32 v156, v156\n"
      "v_exp_f32 v157, v157\n"
      "v_exp_f32 v158, v158\n"
      "v_exp_f32 v159, v159\n"
      "v_mfma_f32_32x32x16_bf16 v[128:143], a[240:243], v[176:179], 0\n"
      "buffer_load_dwordx4 a[212:215], v185, s[0:3], s30 offen offset:1024 sc1\n"
      "v_exp_f32 v112, v112\n"
      "v_exp_f32 v113, v113\n"
      "v_exp_f32 v114, v114\n"
      "v_exp_f32 v115, v115\n"
      "v_mfma_f32_32x32x16_bf16 v[128:143], a[244:247], v[180:183], v[128:143]\n"
      "v_exp_f32 v116, v116\n"
      "v_exp_f32 v117, v117\n"
      "v_exp_f32 v118, v118\n"
      "v_exp_f32 v119, v119\n"
      "v_smfmac_f32_32x32x32_bf16 v[64:79], a[252:255], v[152:159], v11\n"
      "v_exp_f32 v120, v120\n"
      "v_exp_f32 v121, v121\n"
      "v_exp_f32 v122, v122\n"
      "v_exp_f32 v123, v123\n"
      "v_smfmac_f32_32x32x32_bf16 v[80:95], a[248:251], v[112:119], v11\n"
      "v_exp_f32 v124, v124\n"
      "v_exp_f32 v125, v125\n"
      "v_exp_f32 v126, v126\n"
      "v_exp_f32 v127, v127\n"
      "s_waitcnt vmcnt(10)\n"
      "v_mfma_f32_32x32x16_bf16 v[144:159], v[200:203], v[16:19], 0\n"
      "buffer_load_dwordx4 a[216:219], v185, s[0:3], s30 offen offset:2048 sc1\n"
      "v_exp_f32 v128, v128\n"
      "v_exp_f32 v129, v129\n"
      "v_exp_f32 v130, v130\n"
      "v_exp_f32 v131, v131\n"
      "v_mfma_f32_32x32x16_bf16 v[144:159], v[204:207], v[20:23], v[144:159]\n"
      "v_exp_f32 v132, v132\n"
      "v_exp_f32 v133, v133\n"
      "v_exp_f32 v134, v134\n"
      "v_exp_f32 v135, v135\n"
      "v_smfmac_f32_32x32x32_bf16 v[80:95], a[252:255], v[120:127], v11\n"
      "v_exp_f32 v136, v136\n"
      "v_exp_f32 v137, v137\n"
      "v_exp_f32 v138, v138\n"
      "v_exp_f32 v139, v139\n"
      "v_smfmac_f32_32x32x32_bf16 v[96:111], a[248:251], v[128:135], v11\n"
      "v_exp_f32 v140, v140\n"
      "v_exp_f32 v141, v141\n"
      "v_exp_f32 v142, v142\n"
      "v_exp_f32 v143, v143\n"
      "v_mfma_f32_32x32x16_bf16 v[112:127], v[200:203], v[24:27], 0\n"
      "buffer_load_dwordx4 a[220:223], v185, s[0:3], s30 offen offset:3072 sc1\n"
      "v_exp_f32 v144, v144\n"
      "v_exp_f32 v145, v145\n"
      "v_exp_f32 v146, v146\n"
      "v_exp_f32 v147, v147\n"
      "v_mfma_f32_32x32x16_bf16 v[112:127], v[204:207], v[28:31], v[112:127]\n"
      "v_exp_f32 v148, v148\n"
      "v_exp_f32 v149, v149\n"
      "v_exp_f32 v150, v150\n"
      "v_exp_f32 v151, v151\n"
      "v_smfmac_f32_32x32x32_bf16 v[96:111], a[252:255], v[136:143], v11\n"
      "v_exp_f32 v152, v152\n"
      "v_exp_f32 v153, v153\n"
      "v_exp_f32 v154, v154\n"
      "v_exp_f32 v155, v155\n"
      "v_smfmac_f32_32x32x32_bf16 v[48:63], v[208:211], v[144:151], v11\n"
      "v_exp_f32 v156, v156\n"
      "v_exp_f32 v157, v157\n"
      "v_exp_f32 v158, v158\n"
      "v_exp_f32 v159, v159\n"
      "v_mfma_f32_32x32x16_bf16 v[128:143], v[200:203], v[32:35], 0\n"
      "s_add_u32 s30, s30, 0x1000\n"
      "s_and_b32 s30, s30, 0xffff\n"
      "buffer_load_dwordx4 a[224:227], v185, s[0:3], s30 offen offset:0 sc1\n"
      "v_exp_f32 v112, v112\n"
      "v_exp_f32 v113, v113\n"
      "v_exp_f32 v114, v114\n"
      "v_exp_f32 v115, v115\n"
      "v_mfma_f32_32x32x16_bf16 v[128:143], v[204:207], v[36:39], v[128:143]\n"
      "v_exp_f32 v116, v116\n"
      "v_exp_f32 v117, v117\n"
      "v_exp_f32 v118, v118\n"
      "v_exp_f32 v119, v119\n"
      "v_smfmac_f32_32x32x32_bf16 v[48:63], v[212:215], v[152:159], v11\n"
      "v_exp_f32 v120, v120\n"
      "v_exp_f32 v121, v121\n"
      "v_exp_f32 v122, v122\n"
      "v_exp_f32 v123, v123\n"
      "v_smfmac_f32_32x32x32_bf16 v[64:79], v[208:211], v[112:119], v11\n"
      "v_exp_f32 v124, v124\n"
      "v_exp_f32 v125, v125\n"
      "v_exp_f32 v126, v126\n"
      "v_exp_f32 v127, v127\n"
      "v_mfma_f32_32x32x16_bf16 v[144:159], v[200:203], v[176:179], 0\n"
      "buffer_load_dwordx4 a[228:231], v185, s[0:3], s30 offen offset:1024 sc1\n"
      "v_exp_f32 v128, v128\n"
      "v_exp_f32 v129, v129\n"
      "v_exp_f32 v130, v130\n"
      "v_exp_f32 v131, v131\n"
      "v_mfma_f32_32x32x16_bf16 v[144:159], v[204:207], v[180:183], v[144:159]\n"
      "s_cbranch_vccz .Lps54_%=\n"
      "s_add_u32 s31, s78, 262144\n"
      "buffer_store_dwordx4 v[232:235], v0, s[0:3], s31 offen offset:0 sc1\n"
      "s_add_u32 s31, s78, 262144\n"
      "buffer_store_dwordx4 v[232:235], v0, s[0:3], s31 offen offset:1024 sc1\n"
      "s_add_u32 s31, s79, 262144\n"
      "buffer_store_dwordx4 v[232:235], v0, s[0:3], s31 offen offset:0 sc1\n"
      "s_lshl_b32 s28, s16, 13\n"
      "s_add_u32 s28, s28, 36864\n"
      "s_mov_b32 m0, s28\n"
      "s_add_u32 s29, s73, 36864\n"
      "buffer_load_dwordx4 v0, s[0:3], s29 offen offset:0 sc1 lds\n"
      "buffer_load_dwordx4 v0, s[0:3], s29 offen offset:1024 sc1 lds\n"
      ".Lps54_%=:\n"
      "v_exp_f32 v132, v132\n"
      "v_exp_f32 v133, v133\n"
      "v_exp_f32 v134, v134\n"
      "v_exp_f32 v135, v135\n"
      "v_smfmac_f32_32x32x32_bf16 v[64:79], v[212:215], v[120:127], v11\n"
      "v_exp_f32 v136, v136\n"
      "v_exp_f32 v137, v137\n"
      "v_exp_f32 v138, v138\n"
      "v_exp_f32 v139, v139\n"
      "v_smfmac_f32_32x32x32_bf16 v[80:95], v[208:211], v[128:135], v11\n"
      "v_exp_f32 v140, v140\n"
      "v_exp_f32 v141, v141\n"
      "v_exp_f32 v142, v142\n"
      "v_exp_f32 v143, v143\n"
      "s_waitcnt vmcnt(10)\n"
      "v_mfma_f32_32x32x16_bf16 v[112:127], v[216:219], v[16:19], 0\n"
      "buffer_load_dwordx4 a[232:235], v185, s[0:3], s30 offen offset:2048 sc1\n"
      "v_exp_f32 v144, v144\n"
      "v_exp_f32 v145, v145\n"
      "v_exp_f32 v146, v146\n"
      "v_exp_f32 v147, v147\n"
      "v_mfma_f32_32x32x16_bf16 v[112:127], v[220:223], v[20:23], v[112:127]\n"
      "v_exp_f32 v148, v148\n"
      "v_exp_f32 v149, v149\n"
      "v_exp_f32 v150, v150\n"
      "v_exp_f32 v151, v151\n"
      "v_smfmac_f32_32x32x32_bf16 v[80:95], v[212:215], v[136:143], v11\n"
      "v_exp_f32 v152, v152\n"
      "v_exp_f32 v153, v153\n"
      "v_exp_f32 v154, v154\n"
      "v_exp_f32 v155, v155\n"
      "v_smfmac_f32_32x32x32_bf16 v[96:111], v[208:211], v[144:151], v11\n"
      "v_exp_f32 v156, v156\n"
      "v_exp_f32 v157, v157\n"
      "v_exp_f32 v158, v158\n"
      "v_exp_f32 v159, v159\n"
      "v_mfma_f32_32x32x16_bf16 v[128:143], v[216:219], v[24:27], 0\n"
      "buffer_load_dwordx4 a[236:239], v185, s[0:3], s30 offen offset:3072 sc1\n"
      "v_exp_f32 v112, v112\n"
      "v_exp_f32 v113, v113\n"
      "v_exp_f32 v114, v114\n"
      "v_exp_f32 v115, v115\n"
      "v_mfma_f32_32x32x16_bf16 v[128:143], v[220:223], v[28:31], v[128:143]\n"
      "v_exp_f32 v116, v116\n"
      "v_exp_f32 v117, v117\n"
      "v_exp_f32 v118, v118\n"
      "v_exp_f32 v119, v119\n"
      "v_smfmac_f32_32x32x32_bf16 v[96:111], v[212:215], v[152:159], v11\n"
      "v_exp_f32 v120, v120\n"
      "v_exp_f32 v121, v121\n"
      "v_exp_f32 v122, v122\n"
      "v_exp_f32 v123, v123\n"
      "v_smfmac_f32_32x32x32_bf16 v[48:63], v[224:227], v[112:119], v11\n"
      "v_exp_f32 v124, v124\n"
      "v_exp_f32 v125, v125\n"
      "v_exp_f32 v126, v126\n"
      "v_exp_f32 v127, v127\n"
      "v_mfma_f32_32x32x16_bf16 v[144:159], v[216:219], v[32:35], 0\n"
      "s_add_u32 s30, s30, 0x1000\n"
      "s_and_b32 s30, s30, 0xffff\n"
      "buffer_load_dwordx4 a[240:243], v185, s[0:3], s30 offen offset:0 sc1\n"
      "v_exp_f32 v128, v128\n"
      "v_exp_f32 v129, v129\n"
      "v_exp_f32 v130, v130\n"
      "v_exp_f32 v131, v131\n"
      "v_mfma_f32_32x32x16_bf16 v[144:159], v[220:223], v[36:39], v[144:159]\n"
      "v_exp_f32 v132, v132\n"
      "v_exp_f32 v133, v133\n"
      "v_exp_f32 v134, v134\n"
      "v_exp_f32 v135, v135\n"
      "v_smfmac_f32_32x32x32_bf16 v[48:63], v[228:231], v[120:127], v11\n"
      "v_exp_f32 v136, v136\n"
      "v_exp_f32 v137, v137\n"
      "v_exp_f32 v138, v138\n"
      "v_exp_f32 v139, v139\n"
      "v_smfmac_f32_32x32x32_bf16 v[64:79], v[224:227], v[128:135], v11\n"
      "v_exp_f32 v140, v140\n"
      "v_exp_f32 v141, v141\n"
      "v_exp_f32 v142, v142\n"
      "v_exp_f32 v143, v143\n"
      "v_mfma_f32_32x32x16_bf16 v[112:127], v[216:219], v[176:179], 0\n"
      "buffer_load_dwordx4 a[244:247], v185, s[0:3], s30 offen offset:1024 sc1\n"
      "v_exp_f32 v144, v144\n"
      "v_exp_f32 v145, v145\n"
      "v_exp_f32 v146, v146\n"
      "v_exp_f32 v147, v147\n"
      "v_mfma_f32_32x32x16_bf16 v[112:127], v[220:223], v[180:183], v[112:127]\n"
      "v_exp_f32 v148, v148\n"
      "v_exp_f32 v149, v149\n"
      "v_exp_f32 v150, v150\n"
      "v_exp_f32 v151, v151\n"
      "v_smfmac_f32_32x32x32_bf16 v[64:79], v[228:231], v[136:143], v11\n"
      "v_exp_f32 v152, v152\n"
      "v_exp_f32 v153, v153\n"
      "v_exp_f32 v154, v154\n"
      "v_exp_f32 v155, v155\n"
      "v_smfmac_f32_32x32x32_bf16 v[80:95], v[224:227], v[144:151], v11\n"
      "v_exp_f32 v156, v156\n"
      "v_exp_f32 v157, v157\n"
      "v_exp_f32 v158, v158\n"
      "v_exp_f32 v159, v159\n"
      "s_waitcnt vmcnt(10)\n"
      "v_mfma_f32_32x32x16_bf16 v[128:143], a[192:195], v[16:19], 0\n"
      "buffer_load_dwordx4 a[248:251], v185, s[0:3], s30 offen offset:2048 sc1\n"
      "v_exp_f32 v112, v112\n"
      "v_exp_f32 v113, v113\n"
      "v_exp_f32 v114, v114\n"
      "v_exp_f32 v115, v115\n"
      "v_mfma_f32_32x32x16_bf16 v[128:143], a[196:199], v[20:23], v[128:143]\n"
      "v_exp_f32 v116, v116\n"
      "v_exp_f32 v117, v117\n"
      "v_exp_f32 v118, v118\n"
      "v_exp_f32 v119, v119\n"
      "v_smfmac_f32_32x32x32_bf16 v[80:95], v[228:231], v[152:159], v11\n"
      "v_exp_f32 v120, v120\n"
      "v_exp_f32 v121, v121\n"
      "v_exp_f32 v122, v122\n"
      "v_exp_f32 v123, v123\n"
      "v_smfmac_f32_32x32x32_bf16 v[96:111], v[224:227], v[112:119], v11\n"
      "v_exp_f32 v124, v124\n"
      "v_exp_f32 v125, v125\n"
      "v_exp_f32 v126, v126\n"
      "v_exp_f32 v127, v127\n"
      "v_mfma_f32_32x32x16_bf16 v[144:159], a[192:195], v[24:27], 0\n"
      "buffer_load_dwordx4 a[252:255], v185, s[0:3], s30 offen offset:3072 sc1\n"
      "v_exp_f32 v128, v128\n"
      "v_exp_f32 v129, v129\n"
      "v_exp_f32 v130, v130\n"
      "v_exp_f32 v131, v131\n"
      "v_mfma_f32_32x32x16_bf16 v[144:159], a[196:199], v[28:31], v[144:159]\n"
      "s_cbranch_vccz .Lps55_%=\n"
      "s_add_u32 s31, s79, 262144\n"
      "buffer_store_dwordx4 v[232:235], v0, s[0:3], s31 offen offset:1024 sc1\n"
      "s_add_u32 s31, s79, 262144\n"
      "buffer_store_dwordx4 v[232:235], v0, s[0:3], s31 offen offset:2048 sc1\n"
      "s_add_u32 s31, s79, 262144\n"
      "buffer_store_dwordx4 v[232:235], v0, s[0:3], s31 offen offset:3072 sc1\n"
      "buffer_load_dwordx4 v0, s[0:3], s29 offen offset:2048 sc1 lds\n"
      "buffer_load_dwordx4 v0, s[0:3], s29 offen offset:3072 sc1 lds\n"
      ".Lps55_%=:\n"
      "v_exp_f32 v132, v132\n"
      "v_exp_f32 v133, v133\n"
      "v_exp_f32 v134, v134\n"
      "v_exp_f32 v135, v135\n"
      "v_smfmac_f32_32x32x32_bf16 v[96:111], v[228:231], v[120:127], v11\n"
      "v_exp_f32 v136, v136\n"
      "v_exp_f32 v137, v137\n"
      "v_exp_f32 v138, v138\n"
      "v_exp_f32 v139, v139\n"
      "v_smfmac_f32_32x32x32_bf16 v[48:63], a[200:203], v[128:135], v11\n"
      "v_exp_f32 v140, v140\n"
      "v_exp_f32 v141, v141\n"
      "v_exp_f32 v142, v142\n"
      "v_exp_f32 v143, v143\n"
      "v_mfma_f32_32x32x16_bf16 v[112:127], a[192:195], v[32:35], 0\n"
      "s_add_u32 s30, s30, 0x1000\n"
      "s_and_b32 s30, s30, 0xffff\n"
      "buffer_load_dwordx4 v[200:203], v185, s[0:3], s30 offen offset:0 sc1\n"
      "v_exp_f32 v144, v144\n"
      "v_exp_f32 v145, v145\n"
      "v_exp_f32 v146, v146\n"
      "v_exp_f32 v147, v147\n"
      "v_mfma_f32_32x32x16_bf16 v[112:127], a[196:199], v[36:39], v[112:127]\n"
      "v_exp_f32 v148, v148\n"
      "v_exp_f32 v149, v149\n"
      "v_exp_f32 v150, v150\n"
      "v_exp_f32 v151, v151\n"
      "v_smfmac_f32_32x32x32_bf16 v[48:63], a[204:207], v[136:143], v11\n"
      "v_exp_f32 v152, v152\n"
      "v_exp_f32 v153, v153\n"
      "v_exp_f32 v154, v154\n"
      "v_exp_f32 v155, v155\n"
      "v_smfmac_f32_32x32x32_bf16 v[64:79], a[200:203], v[144:151], v11\n"
      "v_exp_f32 v156, v156\n"
      "v_exp_f32 v157, v157\n"
      "v_exp_f32 v158, v158\n"
      "v_exp_f32 v159, v159\n"
      "v_mfma_f32_32x32x16_bf16 v[128:143], a[192:195], v[176:179], 0\n"
      "buffer_load_dwordx4 v[204:207], v185, s[0:3], s30 offen offset:1024 sc1\n"
      "v_exp_f32 v112, v112\n"
      "v_exp_f32 v113, v113\n"
      "v_exp_f32 v114, v114\n"
      "v_exp_f32 v115, v115\n"
      "v_mfma_f32_32x32x16_bf16 v[128:143], a[196:199], v[180:183], v[128:143]\n"
      "v_exp_f32 v116, v116\n"
      "v_exp_f32 v117, v117\n"
      "v_exp_f32 v118, v118\n"
      "v_exp_f32 v119, v119\n"
      "v_smfmac_f32_32x32x32_bf16 v[64:79], a[204:207], v[152:159], v11\n"
      "v_exp_f32 v120, v120\n"
      "v_exp_f32 v121, v121\n"
      "v_exp_f32 v122, v122\n"
      "v_exp_f32 v123, v123\n"
      "v_smfmac_f32_32x32x32_bf16 v[80:95], a[200:203], v[112:119], v11\n"
      "v_exp_f32 v124, v124\n"
      "v_exp_f32 v125, v125\n"
      "v_exp_f32 v126, v126\n"
      "v_exp_f32 v127, v127\n"
      "s_waitcnt vmcnt(10)\n"
      "v_mfma_f32_32x32x16_bf16 v[144:159], a[208:211], v[16:19], 0\n"
      "buffer_load_dwordx4 v[208:211], v185, s[0:3], s30 offen offset:2048 sc1\n"
      "v_exp_f32 v128, v128\n"
      "v_exp_f32 v129, v129\n"
      "v_exp_f32 v130, v130\n"
      "v_exp_f32 v131, v131\n"
      "v_mfma_f32_32x32x16_bf16 v[144:159], a[212:215], v[20:23], v[144:159]\n"
      "v_exp_f32 v132, v132\n"
      "v_exp_f32 v133, v133\n"
      "v_exp_f32 v134, v134\n"
      "v_exp_f32 v135, v135\n"
      "v_smfmac_f32_32x32x32_bf16 v[80:95], a[204:207], v[120:127], v11\n"
      "v_exp_f32 v136, v136\n"
      "v_exp_f32 v137, v137\n"
      "v_exp_f32 v138, v138\n"
      "v_exp_f32 v139, v139\n"
      "v_smfmac_f32_32x32x32_bf16 v[96:111], a[200:203], v[128:135], v11\n"
      "v_exp_f32 v140, v140\n"
      "v_exp_f32 v141, v141\n"
      "v_exp_f32 v142, v142\n"
      "v_exp_f32 v143, v143\n"
      "v_mfma_f32_32x32x16_bf16 v[112:127], a[208:211], v[24:27], 0\n"
      "buffer_load_dwordx4 v[212:215], v185, s[0:3], s30 offen offset:3072 sc1\n"
      "v_exp_f32 v144, v144\n"
      "v_exp_f32 v145, v145\n"
      "v_exp_f32 v146, v146\n"
      "v_exp_f32 v147, v147\n"
      "v_mfma_f32_32x32x16_bf16 v[112:127], a[212:215], v[28:31], v[112:127]\n"
      "s_add_u32 s30, s30, 0x1000\n"
      "s_and_b32 s30, s30, 0xffff\n"
      "buffer_load_dwordx4 v[216:219], v185, s[0:3], s30 offen offset:0 sc1\n"
      "v_exp_f32 v148, v148\n"
      "v_exp_f32 v149, v149\n"
      "v_exp_f32 v150, v150\n"
      "v_exp_f32 v151, v151\n"
      "v_smfmac_f32_32x32x32_bf16 v[96:111], a[204:207], v[136:143], v11\n"
      "v_exp_f32 v152, v152\n"
      "v_exp_f32 v153, v153\n"
      "v_exp_f32 v154, v154\n"
      "v_exp_f32 v155, v155\n"
      "v_smfmac_f32_32x32x32_bf16 v[48:63], a[216:219], v[144:151], v11\n"
      "v_exp_f32 v156, v156\n"
      "v_exp_f32 v157, v157\n"
      "v_exp_f32 v158, v158\n"
      "v_exp_f32 v159, v159\n"
      "v_mfma_f32_32x32x16_bf16 v[128:143], a[208:211], v[32:35], 0\n"
      "buffer_load_dwordx4 v[220:223], v185, s[0:3], s30 offen offset:1024 sc1\n"
      "v_exp_f32 v112, v112\n"
      "v_exp_f32 v113, v113\n"
      "v_exp_f32 v114, v114\n"
      "v_exp_f32 v115, v115\n"
      "v_mfma_f32_32x32x16_bf16 v[128:143], a[212:215], v[36:39], v[128:143]\n"
      "buffer_load_dwordx4 v[224:227], v185, s[0:3], s30 offen offset:2048 sc1\n"
      "v_exp_f32 v116, v116\n"
      "v_exp_f32 v117, v117\n"
      "v_exp_f32 v118, v118\n"
      "v_exp_f32 v119, v119\n"
      "v_smfmac_f32_32x32x32_bf16 v[48:63], a[220:223], v[152:159], v11\n"
      "v_exp_f32 v120, v120\n"
      "v_exp_f32 v121, v121\n"
      "v_exp_f32 v122, v122\n"
      "v_exp_f32 v123, v123\n"
      "v_smfmac_f32_32x32x32_bf16 v[64:79], a[216:219], v[112:119], v11\n"
      "v_exp_f32 v124, v124\n"
      "v_exp_f32 v125, v125\n"
      "v_exp_f32 v126, v126\n"
      "v_exp_f32 v127, v127\n"
      "v_mfma_f32_32x32x16_bf16 v[144:159], a[208:211], v[176:179], 0\n"
      "buffer_load_dwordx4 v[228:231], v185, s[0:3], s30 offen offset:3072 sc1\n"
      "v_exp_f32 v128, v128\n"
      "v_exp_f32 v129, v129\n"
      "v_exp_f32 v130, v130\n"
      "v_exp_f32 v131, v131\n"
      "v_mfma_f32_32x32x16_bf16 v[144:159], a[212:215], v[180:183], v[144:159]\n"
      "s_add_u32 s30, s30, 0x1000\n"
      "s_and_b32 s30, s30, 0xffff\n"
      "buffer_load_dwordx4 a[192:195], v185, s[0:3], s30 offen offset:0 sc1\n"
      "s_cbranch_vccz .Lps56_%=\n"
      "s_add_u32 s31, s78, 393216\n"
      "buffer_store_dwordx4 v[232:235], v0, s[0:3], s31 offen offset:0 sc1\n"
      "s_add_u32 s31, s78, 393216\n"
      "buffer_store_dwordx4 v[232:235], v0, s[0:3], s31 offen offset:1024 sc1\n"
      "s_add_u32 s31, s76, 344064\n"
      "buffer_store_dwordx4 v[232:235], v0, s[0:3], s31 offen offset:0 sc1\n"
      ".Lps56_%=:\n"
      "v_exp_f32 v132, v132\n"
      "v_exp_f32 v133, v133\n"
      "v_exp_f32 v134, v134\n"
      "v_exp_f32 v135, v135\n"
      "v_smfmac_f32_32x32x32_bf16 v[64:79], a[220:223], v[120:127], v11\n"
      "v_exp_f32 v136, v136\n"
      "v_exp_f32 v137, v137\n"
      "v_exp_f32 v138, v138\n"
      "v_exp_f32 v139, v139\n"
      "v_smfmac_f32_32x32x32_bf16 v[80:95], a[216:219], v[128:135], v11\n"
      "v_exp_f32 v140, v140\n"
      "v_exp_f32 v141, v141\n"
      "v_exp_f32 v142, v142\n"
      "v_exp_f32 v143, v143\n"
      "s_waitcnt vmcnt(13)\n"
      "v_mfma_f32_32x32x16_bf16 v[112:127], a[224:227], v[16:19], 0\n"
      "buffer_load_dwordx4 a[196:199], v185, s[0:3], s30 offen offset:1024 sc1\n"
      "v_exp_f32 v144, v144\n"
      "v_exp_f32 v145, v145\n"
      "v_exp_f32 v146, v146\n"
      "v_exp_f32 v147, v147\n"
      "v_mfma_f32_32x32x16_bf16 v[112:127], a[228:231], v[20:23], v[112:127]\n"
      "buffer_load_dwordx4 a[200:203], v185, s[0:3], s30 offen offset:2048 sc1\n"
      "v_exp_f32 v148, v148\n"
      "v_exp_f32 v149, v149\n"
      "v_exp_f32 v150, v150\n"
      "v_exp_f32 v151, v151\n"
      "v_smfmac_f32_32x32x32_bf16 v[80:95], a[220:223], v[136:143], v11\n"
      "v_exp_f32 v152, v152\n"
      "v_exp_f32 v153, v153\n"
      "v_exp_f32 v154, v154\n"
      "v_exp_f32 v155, v155\n"
      "v_smfmac_f32_32x32x32_bf16 v[96:111], a[216:219], v[144:151], v11\n"
      "v_exp_f32 v156, v156\n"
      "v_exp_f32 v157, v157\n"
      "v_exp_f32 v158, v158\n"
      "v_exp_f32 v159, v159\n"
      "v_mfma_f32_32x32x16_bf16 v[128:143], a[224:227], v[24:27], 0\n"
      "buffer_load_dwordx4 a[204:207], v185, s[0:3], s30 offen offset:3072 sc1\n"
      "v_exp_f32 v112, v112\n"
      "v_exp_f32 v113, v113\n"
      "v_exp_f32 v114, v114\n"
      "v_exp_f32 v115, v115\n"
      "v_mfma_f32_32x32x16_bf16 v[128:143], a[228:231], v[28:31], v[128:143]\n"
      "v_exp_f32 v116, v116\n"
      "v_exp_f32 v117, v117\n"
      "v_exp_f32 v118, v118\n"
      "v_exp_f32 v119, v119\n"
      "v_smfmac_f32_32x32x32_bf16 v[96:111], a[220:223], v[152:159], v11\n"
      "v_exp_f32 v120, v120\n"
      "v_exp_f32 v121, v121\n"
      "v_exp_f32 v122, v122\n"
      "v_exp_f32 v123, v123\n"
      "v_smfmac_f32_32x32x32_bf16 v[48:63], a[232:235], v[112:119], v11\n"
      "v_exp_f32 v124, v124\n"
      "v_exp_f32 v125, v125\n"
      "v_exp_f32 v126, v126\n"
      "v_exp_f32 v127, v127\n"
      "v_mfma_f32_32x32x16_bf16 v[144:159], a[224:227], v[32:35], 0\n"
      "s_add_u32 s30, s30, 0x1000\n"
      "s_and_b32 s30, s30, 0xffff\n"
      "buffer_load_dwordx4 a[208:211], v185, s[0:3], s30 offen offset:0 sc1\n"
      "v_exp_f32 v128, v128\n"
      "v_exp_f32 v129, v129\n"
      "v_exp_f32 v130, v130\n"
      "v_exp_f32 v131, v131\n"
      "v_mfma_f32_32x32x16_bf16 v[144:159], a[228:231], v[36:39], v[144:159]\n"
      "buffer_load_dwordx4 a[212:215], v185, s[0:3], s30 offen offset:1024 sc1\n"
      "v_exp_f32 v132, v132\n"
      "v_exp_f32 v133, v133\n"
      "v_exp_f32 v134, v134\n"
      "v_exp_f32 v135, v135\n"
      "v_smfmac_f32_32x32x32_bf16 v[48:63], a[236:239], v[120:127], v11\n"
      "v_exp_f32 v136, v136\n"
      "v_exp_f32 v137, v137\n"
      "v_exp_f32 v138, v138\n"
      "v_exp_f32 v139, v139\n"
      "v_smfmac_f32_32x32x32_bf16 v[64:79], a[232:235], v[128:135], v11\n"
      "v_exp_f32 v140, v140\n"
      "v_exp_f32 v141, v141\n"
      "v_exp_f32 v142, v142\n"
      "v_exp_f32 v143, v143\n"
      "v_mfma_f32_32x32x16_bf16 v[112:127], a[224:227], v[176:179], 0\n"
      "buffer_load_dwordx4 a[216:219], v185, s[0:3], s30 offen offset:2048 sc1\n"
      "v_exp_f32 v144, v144\n"
      "v_exp_f32 v145, v145\n"
      "v_exp_f32 v146, v146\n"
      "v_exp_f32 v147, v147\n"
      "v_mfma_f32_32x32x16_bf16 v[112:127], a[228:231], v[180:183], v[112:127]\n"
      "buffer_load_dwordx4 a[220:223], v185, s[0:3], s30 offen offset:3072 sc1\n"
      "v_exp_f32 v148, v148\n"
      "v_exp_f32 v149, v149\n"
      "v_exp_f32 v150, v150\n"
      "v_exp_f32 v151, v151\n"
      "v_smfmac_f32_32x32x32_bf16 v[64:79], a[236:239], v[136:143], v11\n"
      "v_exp_f32 v152, v152\n"
      "v_exp_f32 v153, v153\n"
      "v_exp_f32 v154, v154\n"
      "v_exp_f32 v155, v155\n"
      "v_smfmac_f32_32x32x32_bf16 v[80:95], a[232:235], v[144:151], v11\n"
      "v_exp_f32 v156, v156\n"
      "v_exp_f32 v157, v157\n"
      "v_exp_f32 v158, v158\n"
      "v_exp_f32 v159, v159\n"
      "s_waitcnt vmcnt(16)\n"
      "v_mfma_f32_32x32x16_bf16 v[128:143], a[240:243], v[16:19], 0\n"
      "v_exp_f32 v112, v112\n"
      "v_exp_f32 v113, v113\n"
      "v_exp_f32 v114, v114\n"
      "v_exp_f32 v115, v115\n"
      "v_mfma_f32_32x32x16_bf16 v[128:143], a[244:247], v[20:23], v[128:143]\n"
      "v_exp_f32 v116, v116\n"
      "v_exp_f32 v117, v117\n"
      "v_exp_f32 v118, v118\n"
      "v_exp_f32 v119, v119\n"
      "v_smfmac_f32_32x32x32_bf16 v[80:95], a[236:239], v[152:159], v11\n"
      "v_exp_f32 v120, v120\n"
      "v_exp_f32 v121, v121\n"
      "v_exp_f32 v122, v122\n"
      "v_exp_f32 v123, v123\n"
      "v_smfmac_f32_32x32x32_bf16 v[96:111], a[232:235], v[112:119], v11\n"
      "v_exp_f32 v124, v124\n"
      "v_exp_f32 v125, v125\n"
      "v_exp_f32 v126, v126\n"
      "v_exp_f32 v127, v127\n"
      "v_mfma_f32_32x32x16_bf16 v[144:159], a[240:243], v[24:27], 0\n"
      "v_exp_f32 v128, v128\n"
      "v_exp_f32 v129, v129\n"
      "v_exp_f32 v130, v130\n"
      "v_exp_f32 v131, v131\n"
      "v_mfma_f32_32x32x16_bf16 v[144:159], a[244:247], v[28:31], v[144:159]\n"
      "s_cbranch_vccz .Lps57_%=\n"
      "s_add_u32 s31, s76, 344064\n"
      "buffer_store_dwordx4 v[232:235], v0, s[0:3], s31 offen offset:1024 sc1\n"
      "s_add_u32 s31, s77, 49152\n"
      "s_mov_b32 exec_lo, 0xffff\n"
      "s_mov_b32 exec_hi, 0\n"
      "buffer_store_dwordx4 v[232:235], v0, s[0:3], s31 offen offset:0 sc1\n"
      "s_mov_b64 exec, -1\n"
      ".Lps57_%=:\n"
      "v_exp_f32 v132, v132\n"
      "v_exp_f32 v133, v133\n"
      "v_exp_f32 v134, v134\n"
      "v_exp_f32 v135, v135\n"
      "v_smfmac_f32_32x32x32_bf16 v[96:111], a[236:239], v[120:127], v11\n"
      "v_exp_f32 v136, v136\n"
      "v_exp_f32 v137, v137\n"
      "v_exp_f32 v138, v138\n"
      "v_exp_f32 v139, v139\n"
      "v_smfmac_f32_32x32x32_bf16 v[48:63], a[248:251], v[128:135], v11\n"
      "v_exp_f32 v140, v140\n"
      "v_exp_f32 v141, v141\n"
      "v_exp_f32 v142, v142\n"
      "v_exp_f32 v143, v143\n"
      "v_mfma_f32_32x32x16_bf16 v[112:127], a[240:243], v[32:35], 0\n"
      "s_add_u32 s30, s30, 0x1000\n"
      "s_and_b32 s30, s30, 0xffff\n"
      "buffer_load_dwordx4 a[224:227], v185, s[0:3], s30 offen offset:0 sc1\n"
      "v_exp_f32 v144, v144\n"
      "v_exp_f32 v145, v145\n"
      "v_exp_f32 v146, v146\n"
      "v_exp_f32 v147, v147\n"
      "v_mfma_f32_32x32x16_bf16 v[112:127], a[244:247], v[36:39], v[112:127]\n"
      "buffer_load_dwordx4 a[228:231], v185, s[0:3], s30 offen offset:1024 sc1\n"
      "v_exp_f32 v148, v148\n"
      "v_exp_f32 v149, v149\n"
      "v_exp_f32 v150, v150\n"
      "v_exp_f32 v151, v151\n"
      "v_smfmac_f32_32x32x32_bf16 v[48:63], a[252:255], v[136:143], v11\n"
      "v_exp_f32 v152, v152\n"
      "v_exp_f32 v153, v153\n"
      "v_exp_f32 v154, v154\n"
      "v_exp_f32 v155, v155\n"
      "v_smfmac_f32_32x32x32_bf16 v[64:79], a[248:251], v[144:151], v11\n"
      "v_exp_f32 v156, v156\n"
      "v_exp_f32 v157, v157\n"
      "v_exp_f32 v158, v158\n"
      "v_exp_f32 v159, v159\n"
      "v_mfma_f32_32x32x16_bf16 v[128:143], a[240:243], v[176:179], 0\n"
      "buffer_load_dwordx4 a[232:235], v185, s[0:3], s30 offen offset:2048 sc1\n"
      "v_exp_f32 v112, v112\n"
      "v_exp_f32 v113, v113\n"
      "v_exp_f32 v114, v114\n"
      "v_exp_f32 v115, v115\n"
      "v_mfma_f32_32x32x16_bf16 v[128:143], a[244:247], v[180:183], v[128:143]\n"
      "buffer_load_dwordx4 a[236:239], v185, s[0:3], s30 offen offset:3072 sc1\n"
      "v_exp_f32 v116, v116\n"
      "v_exp_f32 v117, v117\n"
      "v_exp_f32 v118, v118\n"
      "v_exp_f32 v119, v119\n"
      "v_smfmac_f32_32x32x32_bf16 v[64:79], a[252:255], v[152:159], v11\n"
      "v_exp_f32 v120, v120\n"
      "v_exp_f32 v121, v121\n"
      "v_exp_f32 v122, v122\n"
      "v_exp_f32 v123, v123\n"
      "v_smfmac_f32_32x32x32_bf16 v[80:95], a[248:251], v[112:119], v11\n"
      "v_exp_f32 v124, v124\n"
      "v_exp_f32 v125, v125\n"
      "v_exp_f32 v126, v126\n"
      "v_exp_f32 v127, v127\n"
      "s_waitcnt vmcnt(16)\n"
      "v_mfma_f32_32x32x16_bf16 v[144:159], v[200:203], v[16:19], 0\n"
      "v_exp_f32 v128, v128\n"
      "v_exp_f32 v129, v129\n"
      "v_exp_f32 v130, v130\n"
      "v_exp_f32 v131, v131\n"
      "v_mfma_f32_32x32x16_bf16 v[144:159], v[204:207], v[20:23], v[144:159]\n"
      "v_exp_f32 v132, v132\n"
      "v_exp_f32 v133, v133\n"
      "v_exp_f32 v134, v134\n"
      "v_exp_f32 v135, v135\n"
      "v_smfmac_f32_32x32x32_bf16 v[80:95], a[252:255], v[120:127], v11\n"
      "v_exp_f32 v136, v136\n"
      "v_exp_f32 v137, v137\n"
      "v_exp_f32 v138, v138\n"
      "v_exp_f32 v139, v139\n"
      "v_smfmac_f32_32x32x32_bf16 v[96:111], a[248:251], v[128:135], v11\n"
      "v_exp_f32 v140, v140\n"
      "v_exp_f32 v141, v141\n"
      "v_exp_f32 v142, v142\n"
      "v_exp_f32 v143, v143\n"
      "v_mfma_f32_32x32x16_bf16 v[112:127], v[200:203], v[24:27], 0\n"
      "v_exp_f32 v144, v144\n"
      "v_exp_f32 v145, v145\n"
      "v_exp_f32 v146, v146\n"
      "v_exp_f32 v147, v147\n"
      "v_mfma_f32_32x32x16_bf16 v[112:127], v[204:207], v[28:31], v[112:127]\n"
      "v_exp_f32 v148, v148\n"
      "v_exp_f32 v149, v149\n"
      "v_exp_f32 v150, v150\n"
      "v_exp_f32 v151, v151\n"
      "v_smfmac_f32_32x32x32_bf16 v[96:111], a[252:255], v[136:143], v11\n"
      "v_exp_f32 v152, v152\n"
      "v_exp_f32 v153, v153\n"
      "v_exp_f32 v154, v154\n"
      "v_exp_f32 v155, v155\n"
      "v_smfmac_f32_32x32x32_bf16 v[48:63], v[208:211], v[144:151], v11\n"
      "v_exp_f32 v156, v156\n"
      "v_exp_f32 v157, v157\n"
      "v_exp_f32 v158, v158\n"
      "v_exp_f32 v159, v159\n"
      "v_mfma_f32_32x32x16_bf16 v[128:143], v[200:203], v[32:35], 0\n"
      "v_exp_f32 v112, v112\n"
      "v_exp_f32 v113, v113\n"
      "v_exp_f32 v114, v114\n"
      "v_exp_f32 v115, v115\n"
      "v_mfma_f32_32x32x16_bf16 v[128:143], v[204:207], v[36:39], v[128:143]\n"
      "v_exp_f32 v116, v116\n"
      "v_exp_f32 v117, v117\n"
      "v_exp_f32 v118, v118\n"
      "v_exp_f32 v119, v119\n"
      "v_smfmac_f32_32x32x32_bf16 v[48:63], v[212:215], v[152:159], v11\n"
      "v_exp_f32 v120, v120\n"
      "v_exp_f32 v121, v121\n"
      "v_exp_f32 v122, v122\n"
      "v_exp_f32 v123, v123\n"
      "v_smfmac_f32_32x32x32_bf16 v[64:79], v[208:211], v[112:119], v11\n"
      "v_exp_f32 v124, v124\n"
      "v_exp_f32 v125, v125\n"
      "v_exp_f32 v126, v126\n"
      "v_exp_f32 v127, v127\n"
      "v_mfma_f32_32x32x16_bf16 v[144:159], v[200:203], v[176:179], 0\n"
      "v_exp_f32 v128, v128\n"
      "v_exp_f32 v129, v129\n"
      "v_exp_f32 v130, v130\n"
      "v_exp_f32 v131, v131\n"
      "v_mfma_f32_32x32x16_bf16 v[144:159], v[204:207], v[180:183], v[144:159]\n"
      "v_exp_f32 v132, v132\n"
      "v_exp_f32 v133, v133\n"
      "v_exp_f32 v134, v134\n"
      "v_exp_f32 v135, v135\n"
      "v_smfmac_f32_32x32x32_bf16 v[64:79], v[212:215], v[120:127], v11\n"
      "v_exp_f32 v136, v136\n"
      "v_exp_f32 v137, v137\n"
      "v_exp_f32 v138, v138\n"
      "v_exp_f32 v139, v139\n"
      "v_smfmac_f32_32x32x32_bf16 v[80:95], v[208:211], v[128:135], v11\n"
      "v_exp_f32 v140, v140\n"
      "v_exp_f32 v141, v141\n"
      "v_exp_f32 v142, v142\n"
      "v_exp_f32 v143, v143\n"
      "s_waitcnt vmcnt(12)\n"
      "v_mfma_f32_32x32x16_bf16 v[112:127], v[216:219], v[16:19], 0\n"
      "v_exp_f32 v144, v144\n"
      "v_exp_f32 v145, v145\n"
      "v_exp_f32 v146, v146\n"
      "v_exp_f32 v147, v147\n"
      "v_mfma_f32_32x32x16_bf16 v[112:127], v[220:223], v[20:23], v[112:127]\n"
      "v_exp_f32 v148, v148\n"
      "v_exp_f32 v149, v149\n"
      "v_exp_f32 v150, v150\n"
      "v_exp_f32 v151, v151\n"
      "v_smfmac_f32_32x32x32_bf16 v[80:95], v[212:215], v[136:143], v11\n"
      "v_exp_f32 v152, v152\n"
      "v_exp_f32 v153, v153\n"
      "v_exp_f32 v154, v154\n"
      "v_exp_f32 v155, v155\n"
      "v_smfmac_f32_32x32x32_bf16 v[96:111], v[208:211], v[144:151], v11\n"
      "v_exp_f32 v156, v156\n"
      "v_exp_f32 v157, v157\n"
      "v_exp_f32 v158, v158\n"
      "v_exp_f32 v159, v159\n"
      "s_waitcnt vmcnt(8)\n"
      "v_mfma_f32_32x32x16_bf16 v[128:143], a[192:195], v[16:19], 0\n"
      "v_exp_f32 v112, v112\n"
      "v_exp_f32 v113, v113\n"
      "v_exp_f32 v114, v114\n"
      "v_exp_f32 v115, v115\n"
      "v_mfma_f32_32x32x16_bf16 v[128:143], a[196:199], v[20:23], v[128:143]\n"
      "v_exp_f32 v116, v116\n"
      "v_exp_f32 v117, v117\n"
      "v_exp_f32 v118, v118\n"
      "v_exp_f32 v119, v119\n"
      "v_smfmac_f32_32x32x32_bf16 v[96:111], v[212:215], v[152:159], v11\n"
      "v_exp_f32 v120, v120\n"
      "v_exp_f32 v121, v121\n"
      "v_exp_f32 v122, v122\n"
      "v_exp_f32 v123, v123\n"
      "v_smfmac_f32_32x32x32_bf16 v[48:63], v[224:227], v[112:119], v11\n"
      "v_exp_f32 v124, v124\n"
      "v_exp_f32 v125, v125\n"
      "v_exp_f32 v126, v126\n"
      "v_exp_f32 v127, v127\n"
      "s_waitcnt vmcnt(4)\n"
      "v_mfma_f32_32x32x16_bf16 v[144:159], a[208:211], v[16:19], 0\n"
      "v_exp_f32 v128, v128\n"
      "v_exp_f32 v129, v129\n"
      "v_exp_f32 v130, v130\n"
      "v_exp_f32 v131, v131\n"
      "v_mfma_f32_32x32x16_bf16 v[144:159], a[212:215], v[20:23], v[144:159]\n"
      "v_exp_f32 v132, v132\n"
      "v_exp_f32 v133, v133\n"
      "v_exp_f32 v134, v134\n"
      "v_exp_f32 v135, v135\n"
      "v_smfmac_f32_32x32x32_bf16 v[48:63], v[228:231], v[120:127], v11\n"
      "v_exp_f32 v136, v136\n"
      "v_exp_f32 v137, v137\n"
      "v_exp_f32 v138, v138\n"
      "v_exp_f32 v139, v139\n"
      "v_smfmac_f32_32x32x32_bf16 v[48:63], a[200:203], v[128:135], v11\n"
      "v_exp_f32 v140, v140\n"
      "v_exp_f32 v141, v141\n"
      "v_exp_f32 v142, v142\n"
      "v_exp_f32 v143, v143\n"
      "s_waitcnt vmcnt(0)\n"
      "v_mfma_f32_32x32x16_bf16 v[112:127], a[224:227], v[16:19], 0\n"
      "v_exp_f32 v144, v144\n"
      "v_exp_f32 v145, v145\n"
      "v_exp_f32 v146, v146\n"
      "v_exp_f32 v147, v147\n"
      "v_mfma_f32_32x32x16_bf16 v[112:127], a[228:231], v[20:23], v[112:127]\n"
      "v_exp_f32 v148, v148\n"
      "v_exp_f32 v149, v149\n"
      "v_exp_f32 v150, v150\n"
      "v_exp_f32 v151, v151\n"
      "v_smfmac_f32_32x32x32_bf16 v[48:63], a[204:207], v[136:143], v11\n"
      "v_exp_f32 v152, v152\n"
      "v_exp_f32 v153, v153\n"
      "v_exp_f32 v154, v154\n"
      "v_exp_f32 v155, v155\n"
      "v_smfmac_f32_32x32x32_bf16 v[48:63], a[216:219], v[144:151], v11\n"
      "v_exp_f32 v156, v156\n"
      "v_exp_f32 v157, v157\n"
      "v_exp_f32 v158, v158\n"
      "v_exp_f32 v159, v159\n"
      "v_mfma_f32_32x32x16_bf16 v[128:143], v[216:219], v[24:27], 0\n"
      "v_exp_f32 v112, v112\n"
      "v_exp_f32 v113, v113\n"
      "v_exp_f32 v114, v114\n"
      "v_exp_f32 v115, v115\n"
      "v_mfma_f32_32x32x16_bf16 v[128:143], v[220:223], v[28:31], v[128:143]\n"
      "v_exp_f32 v116, v116\n"
      "v_exp_f32 v117, v117\n"
      "v_exp_f32 v118, v118\n"
      "v_exp_f32 v119, v119\n"
      "v_smfmac_f32_32x32x32_bf16 v[48:63], a[220:223], v[152:159], v11\n"
      "v_exp_f32 v120, v120\n"
      "v_exp_f32 v121, v121\n"
      "v_exp_f32 v122, v122\n"
      "v_exp_f32 v123, v123\n"
      "v_smfmac_f32_32x32x32_bf16 v[48:63], a[232:235], v[112:119], v11\n"
      "v_exp_f32 v124, v124\n"
      "v_exp_f32 v125, v125\n"
      "v_exp_f32 v126, v126\n"
      "v_exp_f32 v127, v127\n"
      "v_mfma_f32_32x32x16_bf16 v[144:159], a[192:195], v[24:27], 0\n"
      "v_exp_f32 v128, v128\n"
      "v_exp_f32 v129, v129\n"
      "v_exp_f32 v130, v130\n"
      "v_exp_f32 v131, v131\n"
      "v_mfma_f32_32x32x16_bf16 v[144:159], a[196:199], v[28:31], v[144:159]\n"
      "v_exp_f32 v132, v132\n"
      "v_exp_f32 v133, v133\n"
      "v_exp_f32 v134, v134\n"
      "v_exp_f32 v135, v135\n"
      "v_smfmac_f32_32x32x32_bf16 v[48:63], a[236:239], v[120:127], v11\n"
      "v_exp_f32 v136, v136\n"
      "v_exp_f32 v137, v137\n"
      "v_exp_f32 v138, v138\n"
      "v_exp_f32 v139, v139\n"
      "v_smfmac_f32_32x32x32_bf16 v[64:79], v[224:227], v[128:135], v11\n"
      "v_exp_f32 v140, v140\n"
      "v_exp_f32 v141, v141\n"
      "v_exp_f32 v142, v142\n"
      "v_exp_f32 v143, v143\n"
      "v_mfma_f32_32x32x16_bf16 v[112:127], a[208:211], v[24:27], 0\n"
      "v_exp_f32 v144, v144\n"
      "v_exp_f32 v145, v145\n"
      "v_exp_f32 v146, v146\n"
      "v_exp_f32 v147, v147\n"
      "v_mfma_f32_32x32x16_bf16 v[112:127], a[212:215], v[28:31], v[112:127]\n"
      "ds_write_b128 v188, v[48:51] offset:0\n"
      "v_exp_f32 v148, v148\n"
      "v_exp_f32 v149, v149\n"
      "v_exp_f32 v150, v150\n"
      "v_exp_f32 v151, v151\n"
      "v_smfmac_f32_32x32x32_bf16 v[64:79], v[228:231], v[136:143], v11\n"
      "v_exp_f32 v152, v152\n"
      "v_exp_f32 v153, v153\n"
      "v_exp_f32 v154, v154\n"
      "v_exp_f32 v155, v155\n"
      "v_smfmac_f32_32x32x32_bf16 v[64:79], a[200:203], v[144:151], v11\n"
      "v_exp_f32 v156, v156\n"
      "v_exp_f32 v157, v157\n"
      "v_exp_f32 v158, v158\n"
      "v_exp_f32 v159, v159\n"
      "v_mfma_f32_32x32x16_bf16 v[128:143], a[224:227], v[24:27], 0\n"
      "ds_write_b128 v188, v[52:55] offset:1024\n"
      "v_exp_f32 v112, v112\n"
      "v_exp_f32 v113, v113\n"
      "v_exp_f32 v114, v114\n"
      "v_exp_f32 v115, v115\n"
      "v_mfma_f32_32x32x16_bf16 v[128:143], a[228:231], v[28:31], v[128:143]\n"
      "v_exp_f32 v116, v116\n"
      "v_exp_f32 v117, v117\n"
      "v_exp_f32 v118, v118\n"
      "v_exp_f32 v119, v119\n"
      "v_smfmac_f32_32x32x32_bf16 v[64:79], a[204:207], v[152:159], v11\n"
      "v_exp_f32 v120, v120\n"
      "v_exp_f32 v121, v121\n"
      "v_exp_f32 v122, v122\n"
      "v_exp_f32 v123, v123\n"
      "v_smfmac_f32_32x32x32_bf16 v[64:79], a[216:219], v[112:119], v11\n"
      "ds_write_b128 v188, v[56:59] offset:2048\n"
      "v_exp_f32 v124, v124\n"
      "v_exp_f32 v125, v125\n"
      "v_exp_f32 v126, v126\n"
      "v_exp_f32 v127, v127\n"
      "v_mfma_f32_32x32x16_bf16 v[144:159], v[216:219], v[32:35], 0\n"
      "v_exp_f32 v128, v128\n"
      "v_exp_f32 v129, v129\n"
      "v_exp_f32 v130, v130\n"
      "v_exp_f32 v131, v131\n"
      "v_mfma_f32_32x32x16_bf16 v[144:159], v[220:223], v[36:39], v[144:159]\n"
      "v_exp_f32 v132, v132\n"
      "v_exp_f32 v133, v133\n"
      "v_exp_f32 v134, v134\n"
      "v_exp_f32 v135, v135\n"
      "v_smfmac_f32_32x32x32_bf16 v[64:79], a[220:223], v[120:127], v11\n"
      "ds_write_b128 v188, v[60:63] offset:3072\n"
      "v_exp_f32 v136, v136\n"
      "v_exp_f32 v137, v137\n"
      "v_exp_f32 v138, v138\n"
      "v_exp_f32 v139, v139\n"
      "v_smfmac_f32_32x32x32_bf16 v[64:79], a[232:235], v[128:135], v11\n"
      "v_exp_f32 v140, v140\n"
      "v_exp_f32 v141, v141\n"
      "v_exp_f32 v142, v142\n"
      "v_exp_f32 v143, v143\n"
      "v_mfma_f32_32x32x16_bf16 v[112:127], a[192:195], v[32:35], 0\n"
      "v_exp_f32 v144, v144\n"
      "v_exp_f32 v145, v145\n"
      "v_exp_f32 v146, v146\n"
      "v_exp_f32 v147, v147\n"
      "v_mfma_f32_32x32x16_bf16 v[112:127], a[196:199], v[36:39], v[112:127]\n"
      "v_exp_f32 v148, v148\n"
      "v_exp_f32 v149, v149\n"
      "v_exp_f32 v150, v150\n"
      "v_exp_f32 v151, v151\n"
      "v_smfmac_f32_32x32x32_bf16 v[64:79], a[236:239], v[136:143], v11\n"
      "v_exp_f32 v152, v152\n"
      "v_exp_f32 v153, v153\n"
      "v_exp_f32 v154, v154\n"
      "v_exp_f32 v155, v155\n"
      "v_smfmac_f32_32x32x32_bf16 v[80:95], v[224:227], v[144:151], v11\n"
      "v_exp_f32 v156, v156\n"
      "v_exp_f32 v157, v157\n"
      "v_exp_f32 v158, v158\n"
      "v_exp_f32 v159, v159\n"
      "v_mfma_f32_32x32x16_bf16 v[128:143], a[208:211], v[32:35], 0\n"
      "v_exp_f32 v112, v112\n"
      "v_exp_f32 v113, v113\n"
      "v_exp_f32 v114, v114\n"
      "v_exp_f32 v115, v115\n"
      "v_mfma_f32_32x32x16_bf16 v[128:143], a[212:215], v[36:39], v[128:143]\n"
      "ds_write_b128 v189, v[64:67] offset:0\n"
      "v_exp_f32 v116, v116\n"
      "v_exp_f32 v117, v117\n"
      "v_exp_f32 v118, v118\n"
      "v_exp_f32 v119, v119\n"
      "v_smfmac_f32_32x32x32_bf16 v[80:95], v[228:231], v[152:159], v11\n"
      "v_exp_f32 v120, v120\n"
      "v_exp_f32 v121, v121\n"
      "v_exp_f32 v122, v122\n"
      "v_exp_f32 v123, v123\n"
      "v_smfmac_f32_32x32x32_bf16 v[80:95], a[200:203], v[112:119], v11\n"
      "v_exp_f32 v124, v124\n"
      "v_exp_f32 v125, v125\n"
      "v_exp_f32 v126, v126\n"
      "v_exp_f32 v127, v127\n"
      "v_mfma_f32_32x32x16_bf16 v[144:159], a[224:227], v[32:35], 0\n"
      "ds_write_b128 v189, v[68:71] offset:1024\n"
      "v_exp_f32 v128, v128\n"
      "v_exp_f32 v129, v129\n"
      "v_exp_f32 v130, v130\n"
      "v_exp_f32 v131, v131\n"
      "v_mfma_f32_32x32x16_bf16 v[144:159], a[228:231], v[36:39], v[144:159]\n"
      "v_exp_f32 v132, v132\n"
      "v_exp_f32 v133, v133\n"
      "v_exp_f32 v134, v134\n"
      "v_exp_f32 v135, v135\n"
      "v_smfmac_f32_32x32x32_bf16 v[80:95], a[204:207], v[120:127], v11\n"
      "v_exp_f32 v136, v136\n"
      "v_exp_f32 v137, v137\n"
      "v_exp_f32 v138, v138\n"
      "v_exp_f32 v139, v139\n"
      "v_smfmac_f32_32x32x32_bf16 v[80:95], a[216:219], v[128:135], v11\n"
      "ds_write_b128 v189, v[72:75] offset:2048\n"
      "v_exp_f32 v140, v140\n"
      "v_exp_f32 v141, v141\n"
      "v_exp_f32 v142, v142\n"
      "v_exp_f32 v143, v143\n"
      "v_mfma_f32_32x32x16_bf16 v[112:127], v[216:219], v[176:179], 0\n"
      "v_exp_f32 v144, v144\n"
      "v_exp_f32 v145, v145\n"
      "v_exp_f32 v146, v146\n"
      "v_exp_f32 v147, v147\n"
      "v_mfma_f32_32x32x16_bf16 v[112:127], v[220:223], v[180:183], v[112:127]\n"
      "v_exp_f32 v148, v148\n"
      "v_exp_f32 v149, v149\n"
      "v_exp_f32 v150, v150\n"
      "v_exp_f32 v151, v151\n"
      "v_smfmac_f32_32x32x32_bf16 v[80:95], a[220:223], v[136:143], v11\n"
      "ds_write_b128 v189, v[76:79] offset:3072\n"
      "v_exp_f32 v152, v152\n"
      "v_exp_f32 v153, v153\n"
      "v_exp_f32 v154, v154\n"
      "v_exp_f32 v155, v155\n"
      "v_smfmac_f32_32x32x32_bf16 v[80:95], a[232:235], v[144:151], v11\n"
      "v_exp_f32 v156, v156\n"
      "v_exp_f32 v157, v157\n"
      "v_exp_f32 v158, v158\n"
      "v_exp_f32 v159, v159\n"
      "v_mfma_f32_32x32x16_bf16 v[128:143], a[192:195], v[176:179], 0\n"
      "v_exp_f32 v112, v112\n"
      "v_exp_f32 v113, v113\n"
      "v_exp_f32 v114, v114\n"
      "v_exp_f32 v115, v115\n"
      "v_mfma_f32_32x32x16_bf16 v[128:143], a[196:199], v[180:183], v[128:143]\n"
      "v_exp_f32 v116, v116\n"
      "v_exp_f32 v117, v117\n"
      "v_exp_f32 v118, v118\n"
      "v_exp_f32 v119, v119\n"
      "v_smfmac_f32_32x32x32_bf16 v[80:95], a[236:239], v[152:159], v11\n"
      "v_exp_f32 v120, v120\n"
      "v_exp_f32 v121, v121\n"
      "v_exp_f32 v122, v122\n"
      "v_exp_f32 v123, v123\n"
      "v_smfmac_f32_32x32x32_bf16 v[96:111], v[224:227], v[112:119], v11\n"
      "v_exp_f32 v124, v124\n"
      "v_exp_f32 v125, v125\n"
      "v_exp_f32 v126, v126\n"
      "v_exp_f32 v127, v127\n"
      "v_mfma_f32_32x32x16_bf16 v[144:159], a[208:211], v[176:179], 0\n"
      "v_exp_f32 v128, v128\n"
      "v_exp_f32 v129, v129\n"
      "v_exp_f32 v130, v130\n"
      "v_exp_f32 v131, v131\n"
      "v_mfma_f32_32x32x16_bf16 v[144:159], a[212:215], v[180:183], v[144:159]\n"
      "ds_write_b128 v190, v[80:83] offset:0\n"
      "v_exp_f32 v132, v132\n"
      "v_exp_f32 v133, v133\n"
      "v_exp_f32 v134, v134\n"
      "v_exp_f32 v135, v135\n"
      "v_smfmac_f32_32x32x32_bf16 v[96:111], v[228:231], v[120:127], v11\n"
      "ds_write_b128 v190, v[84:87] offset:1024\n"
      "v_exp_f32 v136, v136\n"
      "v_exp_f32 v137, v137\n"
      "v_exp_f32 v138, v138\n"
      "v_exp_f32 v139, v139\n"
      "v_smfmac_f32_32x32x32_bf16 v[96:111], a[200:203], v[128:135], v11\n"
      "ds_write_b128 v190, v[88:91] offset:2048\n"
      "v_exp_f32 v140, v140\n"
      "v_exp_f32 v141, v141\n"
      "v_exp_f32 v142, v142\n"
      "v_exp_f32 v143, v143\n"
      "v_mfma_f32_32x32x16_bf16 v[112:127], a[224:227], v[176:179], 0\n"
      "ds_write_b128 v190, v[92:95] offset:3072\n"
      "v_exp_f32 v144, v144\n"
      "v_exp_f32 v145, v145\n"
      "v_exp_f32 v146, v146\n"
      "v_exp_f32 v147, v147\n"
      "v_mfma_f32_32x32x16_bf16 v[112:127], a[228:231], v[180:183], v[112:127]\n"
      "v_exp_f32 v148, v148\n"
      "v_exp_f32 v149, v149\n"
      "v_exp_f32 v150, v150\n"
      "v_exp_f32 v151, v151\n"
      "v_smfmac_f32_32x32x32_bf16 v[96:111], a[204:207], v[136:143], v11\n"
      "s_waitcnt lgkmcnt(0)\n"
      "s_barrier\n"
      "ds_read_b128 v[60:63], v191 offset:3072\n"
      "ds_read_b128 v[76:79], v191 offset:7168\n"
      "ds_read_b128 v[92:95], v191 offset:11264\n"
      "ds_read_b128 v[48:51], v191 offset:0\n"
      "ds_read_b128 v[64:67], v191 offset:4096\n"
      "ds_read_b128 v[80:83], v191 offset:8192\n"
      "ds_read_b128 v[52:55], v191 offset:1024\n"
      "ds_read_b128 v[68:71], v191 offset:5120\n"
      "ds_read_b128 v[84:87], v191 offset:9216\n"
      "ds_read_b128 v[56:59], v191 offset:2048\n"
      "ds_read_b128 v[72:75], v191 offset:6144\n"
      "ds_read_b128 v[88:91], v191 offset:10240\n"
      "s_sub_u32 s28, s11, 9\n"
      "s_cmp_lt_u32 s28, 7\n"
      "s_cbranch_scc0 .Lif58_%=\n"
      "s_lshl_b32 s28, s15, 3\n"
      "s_add_u32 s28, s28, s25\n"
      "s_add_u32 s28, s28, s27\n"
      "s_mov_b64 exec, 0xffff\n"
      "buffer_store_dwordx2 v[12:13], v4, s[0:3], s28 offen sc1\n"
      "s_mov_b64 exec, -1\n"
      ".Lif58_%=:\n"
      "v_exp_f32 v152, v152\n"
      "v_exp_f32 v153, v153\n"
      "v_exp_f32 v154, v154\n"
      "v_exp_f32 v155, v155\n"
      "v_smfmac_f32_32x32x32_bf16 v[96:111], a[216:219], v[144:151], v11\n"
      "v_exp_f32 v156, v156\n"
      "v_exp_f32 v157, v157\n"
      "v_exp_f32 v158, v158\n"
      "v_exp_f32 v159, v159\n"
      "v_exp_f32 v112, v112\n"
      "v_exp_f32 v113, v113\n"
      "v_exp_f32 v114, v114\n"
      "v_exp_f32 v115, v115\n"
      "s_waitcnt lgkmcnt(9)\n"
      "v_pk_add_f32 v[60:61], v[60:61], v[76:77]\n"
      "v_pk_add_f32 v[62:63], v[62:63], v[78:79]\n"
      "v_pk_add_f32 v[60:61], v[60:61], v[92:93]\n"
      "v_pk_add_f32 v[62:63], v[62:63], v[94:95]\n"
      "s_waitcnt lgkmcnt(6)\n"
      "v_pk_add_f32 v[48:49], v[48:49], v[64:65]\n"
      "v_pk_add_f32 v[50:51], v[50:51], v[66:67]\n"
      "v_pk_add_f32 v[48:49], v[48:49], v[80:81]\n"
      "v_pk_add_f32 v[50:51], v[50:51], v[82:83]\n"
      "v_exp_f32 v116, v116\n"
      "v_exp_f32 v117, v117\n"
      "v_exp_f32 v118, v118\n"
      "v_exp_f32 v119, v119\n"
      "v_smfmac_f32_32x32x32_bf16 v[96:111], a[220:223], v[152:159], v11\n"
      "s_waitcnt lgkmcnt(3)\n"
      "v_pk_add_f32 v[52:53], v[52:53], v[68:69]\n"
      "v_pk_add_f32 v[54:55], v[54:55], v[70:71]\n"
      "v_pk_add_f32 v[52:53], v[52:53], v[84:85]\n"
      "v_pk_add_f32 v[54:55], v[54:55], v[86:87]\n"
      "s_waitcnt lgkmcnt(0)\n"
      "v_pk_add_f32 v[56:57], v[56:57], v[72:73]\n"
      "v_pk_add_f32 v[58:59], v[58:59], v[74:75]\n"
      "v_pk_add_f32 v[56:57], v[56:57], v[88:89]\n"
      "v_pk_add_f32 v[58:59], v[58:59], v[90:91]\n"
      "v_exp_f32 v120, v120\n"
      "v_exp_f32 v121, v121\n"
      "v_exp_f32 v122, v122\n"
      "v_exp_f32 v123, v123\n"
      "v_smfmac_f32_32x32x32_bf16 v[96:111], a[232:235], v[112:119], v11\n"
      "v_exp_f32 v124, v124\n"
      "v_exp_f32 v125, v125\n"
      "v_exp_f32 v126, v126\n"
      "v_exp_f32 v127, v127\n"
      "s_nop 1 ; hz\n"
      "v_smfmac_f32_32x32x32_bf16 v[96:111], a[236:239], v[120:127], v11\n"
      "s_nop 7 ; hz\n"
      "s_nop 3 ; hz\n"
      "v_pk_add_f32 v[108:109], v[108:109], v[60:61]\n"
      "v_pk_add_f32 v[110:111], v[110:111], v[62:63]\n"
      "v_pk_add_f32 v[96:97], v[96:97], v[48:49]\n"
      "v_pk_add_f32 v[98:99], v[98:99], v[50:51]\n"
      "v_pk_add_f32 v[100:101], v[100:101], v[52:53]\n"
      "v_pk_add_f32 v[102:103], v[102:103], v[54:55]\n"
      "v_pk_add_f32 v[104:105], v[104:105], v[56:57]\n"
      "v_pk_add_f32 v[106:107], v[106:107], v[58:59]\n"
      ".Lsto7_%=:\n"
      "s_sub_u32 s28, s11, 5\n"
      "s_cmp_lt_u32 s28, 4\n"
      "s_cbranch_scc0 .Lif59_%=\n"
      "s_lshl_b32 s28, s15, 3\n"
      "s_add_u32 s28, s28, s25\n"
      "s_add_u32 s28, s28, s27\n"
      "s_mov_b64 exec, 0xffff\n"
      "buffer_store_dwordx2 v[12:13], v4, s[0:3], s28 offen sc1\n"
      "s_mov_b64 exec, -1\n"
      ".Lif59_%=:\n"
      "v_mov_b32 v118, 0\n"
      "v_mov_b32 v119, 0\n"
      "s_lshl_b32 s28, s15, 11\n"
      "s_add_u32 s28, s28, s22\n"
      "s_nop 3 ; hz\n"
      "v_mov_b32 v120, v108\n"
      "v_mov_b32 v121, v108\n"
      "s_nop 1 ; hz\n"
      "v_permlane32_swap_b32 v120, v121\n"
      "v_add_f32 v121, s68, v121\n"
      "v_rcp_f32 v122, v121\n"
      "s_nop 0 ; hz\n"
      "v_max_f32 v120, v121, v122\n"
      "v_mul_f32 v120, s4, v120\n"
      "v_fma_f32 v122, 0, v120, v122\n"
      "v_pk_mul_f32 v[128:129], v[96:97], v[122:123] op_sel_hi:[1,0]\n"
      "v_pk_mul_f32 v[130:131], v[98:99], v[122:123] op_sel_hi:[1,0]\n"
      "v_pk_mul_f32 v[132:133], v[100:101], v[122:123] op_sel_hi:[1,0]\n"
      "v_pk_mul_f32 v[134:135], v[102:103], v[122:123] op_sel_hi:[1,0]\n"
      "v_pk_mul_f32 v[136:137], v[104:105], v[122:123] op_sel_hi:[1,0]\n"
      "v_pk_mul_f32 v[138:139], v[106:107], v[122:123] op_sel_hi:[1,0]\n"
      "v_pk_mul_f32 v[140:141], v[108:109], v[122:123] op_sel_hi:[1,0]\n"
      "v_pk_mul_f32 v[142:143], v[110:111], v[122:123] op_sel_hi:[1,0]\n"
      "v_cvt_pk_bf16_f32 v112, v128, v129\n"
      "v_cvt_pk_bf16_f32 v113, v130, v131\n"
      "v_cvt_pk_bf16_f32 v114, v132, v133\n"
      "v_cvt_pk_bf16_f32 v115, v134, v135\n"
      "buffer_store_dwordx4 v[112:115], v0, s[0:3], s28 offen offset:0 sc1\n"
      "v_cvt_pk_bf16_f32 v116, v136, v137\n"
      "v_cvt_pk_bf16_f32 v117, v138, v139\n"
      "s_mov_b32 exec_hi, 0\n"
      "v_cvt_pk_bf16_f32 v118, v140, v141\n"
      "v_cvt_pk_bf16_f32 v119, v142, v143\n"
      "s_mov_b32 exec_hi, -1\n"
      "buffer_store_dwordx4 v[116:119], v0, s[0:3], s28 offen offset:1024 sc1\n"
      "s_barrier\n"
      "s_sub_u32 s28, s11, 2\n"
      "s_cmp_lt_u32 s28, 3\n"
      "s_cbranch_scc0 .Lif62_%=\n"
      "s_lshl_b32 s28, s15, 3\n"
      "s_add_u32 s28, s28, s25\n"
      "s_add_u32 s28, s28, s27\n"
      "s_mov_b64 exec, 0xffff\n"
      "buffer_store_dwordx2 v[12:13], v4, s[0:3], s28 offen sc1\n"
      "s_mov_b64 exec, -1\n"
      ".Lif62_%=:\n"
      "s_lshl_b32 s28, s14, 8\n"
      "s_add_u32 s28, s28, s26\n"
      "s_add_u32 s28, s28, s27\n"
      "s_sub_u32 s28, s28, 3968\n"
      "s_or_b32 s28, s28, s6\n"
      "s_mov_b64 exec, 0xffff\n"
      "buffer_store_dwordx2 v[12:13], v6, s[0:3], s28 offen sc1\n"
      "s_mov_b64 exec, -1\n"
      "s_cmp_ge_u32 s69, 2\n"
      "s_cbranch_scc1 .Lopub60_%=\n"
      "v_pk_mul_f32 v[124:125], v[128:129], s[66:67]\n"
      "v_pk_fma_f32 v[124:125], v[130:131], s[66:67], v[124:125]\n"
      "v_pk_fma_f32 v[124:125], v[132:133], s[66:67], v[124:125]\n"
      "v_pk_fma_f32 v[124:125], v[134:135], s[66:67], v[124:125]\n"
      "v_pk_fma_f32 v[124:125], v[136:137], s[66:67], v[124:125]\n"
      "v_pk_fma_f32 v[124:125], v[138:139], s[66:67], v[124:125]\n"
      "v_pk_fma_f32 v[124:125], v[140:141], s[66:67], v[124:125]\n"
      "v_pk_fma_f32 v[124:125], v[142:143], s[66:67], v[124:125]\n"
      "v_cmp_o_f32 vcc, v124, v125\n"
      "s_nop 0\n"
      "s_nand_b32 s10, vcc_lo, vcc_hi\n"
      "s_cselect_b32 s28, 1, 0\n"
      "s_lshl_b32 s29, s16, 2\n"
      "s_add_u32 s29, s29, 163328\n"
      "v_mov_b32 v124, s28\n"
      "v_mov_b32 v125, s29\n"
      "v_mov_b32 v126, 163328\n"
      "ds_write_b32 v125, v124\n"
      "s_waitcnt lgkmcnt(0)\n"
      "s_barrier\n"
      "ds_read_b128 v[144:147], v126\n"
      "s_sub_u32 s28, s11, 0\n"
      "s_cmp_lt_u32 s28, 2\n"
      "s_cbranch_scc0 .Lif64_%=\n"
      "s_lshl_b32 s28, s15, 3\n"
      "s_add_u32 s28, s28, s25\n"
      "s_add_u32 s28, s28, s27\n"
      "s_mov_b64 exec, 0xffff\n"
      "buffer_store_dwordx2 v[12:13], v4, s[0:3], s28 offen sc1\n"
      "s_mov_b64 exec, -1\n"
      ".Lif64_%=:\n"
      "s_waitcnt lgkmcnt(0)\n"
      "v_or3_b32 v144, v144, v145, v146\n"
      "v_or_b32 v144, v144, v147\n"
      "s_nop 0 ; hz\n"
      "v_readfirstlane_b32 s28, v144\n"
      "s_nop 0 ; hz\n"
      "s_cmp_lg_u32 s28, 0\n"
      "s_cbranch_scc1 .Lfb61_%=\n"
      ".Lopub60_%=:\n"
      "s_bitcmp1_b32 s17, 0\n"
      "s_cselect_b32 s28, 262144, 0\n"
      "s_cselect_b32 s29, 0, 32768\n"
      "s_add_u32 s24, s24, s28\n"
      "s_add_u32 s23, s23, s29\n"
      "s_cmp_eq_u32 s17, 0\n"
      "s_cbranch_scc0 .Lif67_%=\n"
      "s_add_u32 s11, s11, s15\n"
      "s_getpc_b64 s[30:31]\n"
      ".Lpc68_%=:\n"
      "s_sub_u32 s30, s30, .Lpc68_%=-.Lmixb6_%=\n"
      "s_subb_u32 s31, s31, 0\n"
      "s_lshr_b32 s28, s54, 3\n"
      "s_and_b32 s28, s28, 3\n"
      "s_lshl_b32 s28, s28, 2\n"
      "s_add_u32 s28, s28, s16\n"
      "s_lshl_b32 s28, s28, 10\n"
      "v_lshl_add_u32 v152, v0, 3, s28\n"
      "v_min_u32 v152, .Lpc68_%=-.Lmixb6_%=, v152\n"
      "s_mov_b64 exec, 0xff\n"
      "global_load_dword v152, v152, s[30:31]\n"
      "s_mov_b64 exec, -1\n"
      "v_mov_b64 v[148:149], -1\n"
      "v_mov_b64 v[150:151], -1\n"
      "s_add_u32 s31, s79, 0\n"
      "buffer_store_dwordx4 v[148:151], v0, s[0:3], s31 offen offset:0 sc1\n"
      "s_add_u32 s31, s79, 0\n"
      "buffer_store_dwordx4 v[148:151], v0, s[0:3], s31 offen offset:1024 sc1\n"
      "s_add_u32 s31, s79, 0\n"
      "buffer_store_dwordx4 v[148:151], v0, s[0:3], s31 offen offset:2048 sc1\n"
      "s_add_u32 s31, s79, 0\n"
      "buffer_store_dwordx4 v[148:151], v0, s[0:3], s31 offen offset:3072 sc1\n"
      "s_mov_b32 exec_lo, 0xf0000000\n"
      "s_mov_b32 exec_hi, 0xf0000000\n"
      "v_accvgpr_write_b32 a0, 0\n"
      "v_accvgpr_write_b32 a1, 0\n"
      "v_accvgpr_write_b32 a2, 0\n"
      "v_accvgpr_write_b32 a3, 0\n"
      "v_accvgpr_write_b32 a4, 0\n"
      "v_accvgpr_write_b32 a5, 0\n"
      "v_accvgpr_write_b32 a6, 0\n"
      "v_accvgpr_write_b32 a7, 0\n"
      "v_accvgpr_write_b32 a8, 0\n"
      "v_accvgpr_write_b32 a9, 0\n"
      "v_accvgpr_write_b32 a10, 0\n"
      "v_accvgpr_write_b32 a11, 0\n"
      "v_accvgpr_write_b32 a12, 0\n"
      "v_accvgpr_write_b32 a13, 0\n"
      "v_accvgpr_write_b32 a14, 0\n"
      "v_accvgpr_write_b32 a15, 0\n"
      "v_accvgpr_write_b32 a16, 0\n"
      "v_accvgpr_write_b32 a17, 0\n"
      "v_accvgpr_write_b32 a18, 0\n"
      "v_accvgpr_write_b32 a19, 0\n"
      "v_accvgpr_write_b32 a20, 0\n"
      "v_accvgpr_write_b32 a21, 0\n"
      "v_accvgpr_write_b32 a22, 0\n"
      "v_accvgpr_write_b32 a23, 0\n"
      "v_accvgpr_write_b32 a24, 0\n"
      "v_accvgpr_write_b32 a25, 0\n"
      "v_accvgpr_write_b32 a26, 0\n"
      "v_accvgpr_write_b32 a27, 0\n"
      "v_accvgpr_write_b32 a28, 0\n"
      "v_accvgpr_write_b32 a29, 0\n"
      "v_accvgpr_write_b32 a30, 0\n"
      "v_accvgpr_write_b32 a31, 0\n"
      "v_accvgpr_write_b32 a32, 0\n"
      "v_accvgpr_write_b32 a33, 0\n"
      "v_accvgpr_write_b32 a34, 0\n"
      "v_accvgpr_write_b32 a35, 0\n"
      "v_accvgpr_write_b32 a36, 0\n"
      "v_accvgpr_write_b32 a37, 0\n"
      "v_accvgpr_write_b32 a38, 0\n"
      "v_accvgpr_write_b32 a39, 0\n"
      "v_accvgpr_write_b32 a40, 0\n"
      "v_accvgpr_write_b32 a41, 0\n"
      "v_accvgpr_write_b32 a42, 0\n"
      "v_accvgpr_write_b32 a43, 0\n"
      "v_accvgpr_write_b32 a44, 0\n"
      "v_accvgpr_write_b32 a45, 0\n"
      "v_accvgpr_write_b32 a46, 0\n"
      "v_accvgpr_write_b32 a47, 0\n"
      "v_accvgpr_write_b32 a48, 0\n"
      "v_accvgpr_write_b32 a49, 0\n"
      "v_accvgpr_write_b32 a50, 0\n"
      "v_accvgpr_write_b32 a51, 0\n"
      "v_accvgpr_write_b32 a52, 0\n"
      "v_accvgpr_write_b32 a53, 0\n"
      "v_accvgpr_write_b32 a54, 0\n"
      "v_accvgpr_write_b32 a55, 0\n"
      "v_accvgpr_write_b32 a56, 0\n"
      "v_accvgpr_write_b32 a57, 0\n"
      "v_accvgpr_write_b32 a58, 0\n"
      "v_accvgpr_write_b32 a59, 0\n"
      "v_accvgpr_write_b32 a60, 0\n"
      "v_accvgpr_write_b32 a61, 0\n"
      "v_accvgpr_write_b32 a62, 0\n"
      "v_accvgpr_write_b32 a63, 0\n"
      "v_accvgpr_write_b32 a64, 0\n"
      "v_accvgpr_write_b32 a65, 0\n"
      "v_accvgpr_write_b32 a66, 0\n"
      "v_accvgpr_write_b32 a67, 0\n"
      "v_accvgpr_write_b32 a68, 0\n"
      "v_accvgpr_write_b32 a69, 0\n"
      "v_accvgpr_write_b32 a70, 0\n"
      "v_accvgpr_write_b32 a71, 0\n"
      "v_accvgpr_write_b32 a72, 0\n"
      "v_accvgpr_write_b32 a73, 0\n"
      "v_accvgpr_write_b32 a74, 0\n"
      "v_accvgpr_write_b32 a75, 0\n"
      "v_accvgpr_write_b32 a76, 0\n"
      "v_accvgpr_write_b32 a77, 0\n"
      "v_accvgpr_write_b32 a78, 0\n"
      "v_accvgpr_write_b32 a79, 0\n"
      "v_accvgpr_write_b32 a80, 0\n"
      "v_accvgpr_write_b32 a81, 0\n"
      "v_accvgpr_write_b32 a82, 0\n"
      "v_accvgpr_write_b32 a83, 0\n"
      "v_accvgpr_write_b32 a168, 0\n"
      "v_accvgpr_write_b32 a169, 0\n"
      "v_accvgpr_write_b32 a172, 0\n"
      "v_accvgpr_write_b32 a173, 0\n"
      "v_accvgpr_write_b32 a176, 0\n"
      "v_accvgpr_write_b32 a177, 0\n"
      "s_mov_b64 exec, -1\n"
      "s_waitcnt vmcnt(8)\n"
      ".Lif67_%=:\n"
      "s_sleep 3\n"
      "s_cmp_eq_u32 s16, 0\n"
      "s_cbranch_scc0 .Lif69_%=\n"
      "s_add_u32 s28, s25, s27\n"
      "s_lshl_b32 s29, s15, 8\n"
      "s_add_u32 s28, s28, s29\n"
      "s_mov_b32 exec_lo, 65535\n"
      "s_mov_b32 exec_hi, 0\n"
      "buffer_load_dwordx2 v[156:157], v6, s[0:3], s28 offen sc1\n"
      "s_mov_b64 exec, -1\n"
      ".Lif69_%=:\n"
      "s_bitcmp1_b32 s17, 0\n"
      "s_cbranch_scc0 .Lob65_%=\n"
      "s_cmp_lg_u32 s17, 1\n"
      "s_cbranch_scc1 .Lod66_%=\n"
      "s_lshl_b32 s28, s82, 12\n"
      "v_add_u32 v86, s28, v7\n"
      "ds_write_b128 v86, a[0:3] offset:0\n"
      "s_lshl_b32 s28, s83, 12\n"
      "v_add_u32 v87, s28, v7\n"
      "ds_write_b128 v87, a[4:7] offset:0\n"
      "s_lshl_b32 s28, s84, 12\n"
      "v_add_u32 v86, s28, v7\n"
      "ds_write_b128 v86, a[8:11] offset:0\n"
      "s_lshl_b32 s28, s85, 12\n"
      "v_add_u32 v87, s28, v7\n"
      "ds_write_b128 v87, a[12:15] offset:0\n"
      "s_lshl_b32 s28, s86, 12\n"
      "v_add_u32 v86, s28, v7\n"
      "ds_write_b128 v86, a[16:19] offset:0\n"
      "s_lshl_b32 s28, s87, 12\n"
      "v_add_u32 v87, s28, v7\n"
      "ds_write_b128 v87, a[20:23] offset:0\n"
      "s_lshl_b32 s28, s88, 12\n"
      "v_add_u32 v86, s28, v7\n"
      "ds_write_b128 v86, a[24:27] offset:0\n"
      "s_lshl_b32 s28, s82, 12\n"
      "v_add_u32 v86, s28, v7\n"
      "ds_write_b128 v86, a[28:31] offset:28672\n"
      "s_lshl_b32 s28, s83, 12\n"
      "v_add_u32 v87, s28, v7\n"
      "ds_write_b128 v87, a[32:35] offset:28672\n"
      "s_lshl_b32 s28, s84, 12\n"
      "v_add_u32 v86, s28, v7\n"
      "ds_write_b128 v86, a[36:39] offset:28672\n"
      "s_branch .Lod66_%=\n"
      ".Lob65_%=:\n"
      "ds_write_b128 v7, a[84:87] offset:0\n"
      "ds_write_b128 v7, a[88:91] offset:4096\n"
      "ds_write_b128 v7, a[92:95] offset:8192\n"
      "ds_write_b128 v7, a[96:99] offset:12288\n"
      "ds_write_b128 v7, a[100:103] offset:16384\n"
      "ds_write_b128 v7, a[104:107] offset:20480\n"
      "ds_write_b128 v7, a[108:111] offset:24576\n"
      "ds_write_b128 v7, a[112:115] offset:28672\n"
      "ds_write_b128 v7, a[116:119] offset:32768\n"
      "ds_write_b128 v7, a[120:123] offset:36864\n"
      ".Lod66_%=:\n"
      "s_cmp_eq_u32 s16, 0\n"
      "s_cbranch_scc0 .Lif73_%=\n"
      "s_add_u32 s28, s25, s27\n"
      "s_lshl_b32 s29, s15, 8\n"
      "s_add_u32 s28, s28, s29\n"
      "s_mov_b32 s71, 0x10000\n"
      "s_mov_b32 exec_lo, 65535\n"
      "s_mov_b32 exec_hi, 0\n"
      "s_branch .Lpc72_%=\n"
      ".Lpl70_%=:\n"
      "buffer_load_dwordx2 v[156:157], v6, s[0:3], s28 offen sc1\n"
      ".Lpc72_%=:\n"
      "s_waitcnt vmcnt(0)\n"
      "v_cmp_eq_u64 vcc, s[12:13], v[156:157]\n"
      "s_nop 0 ; hz\n"
      "s_cmp_eq_u64 vcc, exec\n"
      "s_cbranch_scc1 .Lpo71_%=\n"
      "s_sleep 1\n"
      "s_sub_u32 s71, s71, 1\n"
      "s_cmp_lg_u32 s71, 0\n"
      "s_cbranch_scc1 .Lpl70_%=\n"
      ".Lpo71_%=:\n"
      "s_mov_b64 exec, -1\n"
      ".Lif73_%=:\n"
      "s_add_u32 s27, s27, 4096\n"
      "s_waitcnt vmcnt(0) lgkmcnt(0)\n"
      "s_barrier\n"
      "s_add_u32 s17, s17, 1\n"
      "s_add_u32 s20, s20, 114688\n"
      "s_add_u32 s21, s21, 16384\n"
      "s_cmp_ge_u32 s15, 14\n"
      "s_cbranch_scc1 .Lnowo74_%=\n"
      ".Lwohead75_%=:\n"
      "s_mov_b32 s30, s22\n"
      "buffer_load_dwordx4 v[112:115], v0, s[0:3], s30 offen offset:0 sc1\n"
      "buffer_load_dwordx4 v[116:119], v0, s[0:3], s30 offen offset:1024 sc1\n"
      "buffer_load_dwordx4 v[120:123], v0, s[0:3], s30 offen offset:2048 sc1\n"
      "buffer_load_dwordx4 v[124:127], v0, s[0:3], s30 offen offset:3072 sc1\n"
      "ds_read_b128 v[64:67], v3 offset:0\n"
      "ds_read_b128 v[68:71], v3 offset:1024\n"
      "ds_read_b128 v[72:75], v3 offset:2048\n"
      "ds_read_b128 v[76:79], v3 offset:3072\n"
      "ds_read_b128 v[80:83], v3 offset:4096\n"
      "ds_read_b128 v[84:87], v3 offset:5120\n"
      "s_add_u32 s30, s30, 0x1000\n"
      "buffer_load_dwordx4 v[128:131], v0, s[0:3], s30 offen offset:0 sc1\n"
      "buffer_load_dwordx4 v[132:135], v0, s[0:3], s30 offen offset:1024 sc1\n"
      "buffer_load_dwordx4 v[136:139], v0, s[0:3], s30 offen offset:2048 sc1\n"
      "buffer_load_dwordx4 v[140:143], v0, s[0:3], s30 offen offset:3072 sc1\n"
      "s_add_u32 s30, s30, 0x1000\n"
      "buffer_load_dwordx4 v[144:147], v0, s[0:3], s30 offen offset:0 sc1\n"
      "buffer_load_dwordx4 v[148:151], v0, s[0:3], s30 offen offset:1024 sc1\n"
      "buffer_load_dwordx4 v[152:155], v0, s[0:3], s30 offen offset:2048 sc1\n"
      "buffer_load_dwordx4 v[156:159], v0, s[0:3], s30 offen offset:3072 sc1\n"
      "s_add_u32 s30, s30, 0x1000\n"
      "buffer_load_dwordx4 v[160:163], v0, s[0:3], s30 offen offset:0 sc1\n"
      "s_waitcnt vmcnt(12) lgkmcnt(5)\n"
      "v_mfma_f32_32x32x16_bf16 v[32:47], v[64:67], v[112:115], v[240:255]\n"
      "ds_read_b128 v[88:91], v3 offset:6144\n"
      "buffer_load_dwordx4 v[164:167], v0, s[0:3], s30 offen offset:1024 sc1\n"
      "s_waitcnt vmcnt(12) lgkmcnt(5)\n"
      "v_mfma_f32_32x32x16_bf16 v[32:47], v[68:71], v[116:119], v[32:47]\n"
      "ds_read_b128 v[92:95], v3 offset:7168\n"
      "buffer_load_dwordx4 v[168:171], v0, s[0:3], s30 offen offset:2048 sc1\n"
      "s_waitcnt vmcnt(12) lgkmcnt(5)\n"
      "v_mfma_f32_32x32x16_bf16 v[32:47], v[72:75], v[120:123], v[32:47]\n"
      "ds_read_b128 v[64:67], v3 offset:8192\n"
      "buffer_load_dwordx4 v[172:175], v0, s[0:3], s30 offen offset:3072 sc1\n"
      "s_waitcnt vmcnt(12) lgkmcnt(5)\n"
      "v_mfma_f32_32x32x16_bf16 v[32:47], v[76:79], v[124:127], v[32:47]\n"
      "ds_read_b128 v[68:71], v3 offset:9216\n"
      "s_add_u32 s30, s30, 0x1000\n"
      "buffer_load_dwordx4 v[176:179], v0, s[0:3], s30 offen offset:0 sc1\n"
      "s_waitcnt vmcnt(12) lgkmcnt(5)\n"
      "v_mfma_f32_32x32x16_bf16 v[32:47], v[80:83], v[128:131], v[32:47]\n"
      "ds_read_b128 v[72:75], v3 offset:10240\n"
      "buffer_load_dwordx4 v[180:183], v0, s[0:3], s30 offen offset:1024 sc1\n"
      "s_waitcnt vmcnt(12) lgkmcnt(5)\n"
      "v_mfma_f32_32x32x16_bf16 v[32:47], v[84:87], v[132:135], v[32:47]\n"
      "ds_read_b128 v[76:79], v3 offset:11264\n"
      "buffer_load_dwordx4 v[184:187], v0, s[0:3], s30 offen offset:2048 sc1\n"
      "s_waitcnt vmcnt(12) lgkmcnt(5)\n"
      "v_mfma_f32_32x32x16_bf16 v[32:47], v[88:91], v[136:139], v[32:47]\n"
      "ds_read_b128 v[80:83], v3 offset:12288\n"
      "buffer_load_dwordx4 v[188:191], v0, s[0:3], s30 offen offset:3072 sc1\n"
      "s_waitcnt vmcnt(12) lgkmcnt(5)\n"
      "v_mfma_f32_32x32x16_bf16 v[32:47], v[92:95], v[140:143], v[32:47]\n"
      "ds_read_b128 v[84:87], v3 offset:13312\n"
      "s_add_u32 s30, s30, 0x1000\n"
      "buffer_load_dwordx4 v[192:195], v0, s[0:3], s30 offen offset:0 sc1\n"
      "s_waitcnt vmcnt(12) lgkmcnt(5)\n"
      "v_mfma_f32_32x32x16_bf16 v[32:47], v[64:67], v[144:147], v[32:47]\n"
      "ds_read_b128 v[88:91], v3 offset:14336\n"
      "buffer_load_dwordx4 v[196:199], v0, s[0:3], s30 offen offset:1024 sc1\n"
      "s_waitcnt vmcnt(12) lgkmcnt(5)\n"
      "v_mfma_f32_32x32x16_bf16 v[32:47], v[68:71], v[148:151], v[32:47]\n"
      "ds_read_b128 v[92:95], v3 offset:15360\n"
      "buffer_load_dwordx4 v[200:203], v0, s[0:3], s30 offen offset:2048 sc1\n"
      "s_waitcnt vmcnt(12) lgkmcnt(5)\n"
      "v_mfma_f32_32x32x16_bf16 v[32:47], v[72:75], v[152:155], v[32:47]\n"
      "ds_read_b128 v[64:67], v3 offset:16384\n"
      "buffer_load_dwordx4 v[204:207], v0, s[0:3], s30 offen offset:3072 sc1\n"
      "s_waitcnt vmcnt(12) lgkmcnt(5)\n"
      "v_mfma_f32_32x32x16_bf16 v[32:47], v[76:79], v[156:159], v[32:47]\n"
      "ds_read_b128 v[68:71], v3 offset:17408\n"
      "s_add_u32 s30, s30, 0x1000\n"
      "buffer_load_dwordx4 v[208:211], v0, s[0:3], s30 offen offset:0 sc1\n"
      "s_waitcnt vmcnt(12) lgkmcnt(5)\n"
      "v_mfma_f32_32x32x16_bf16 v[32:47], v[80:83], v[160:163], v[32:47]\n"
      "ds_read_b128 v[72:75], v3 offset:18432\n"
      "buffer_load_dwordx4 v[212:215], v0, s[0:3], s30 offen offset:1024 sc1\n"
      "s_waitcnt vmcnt(12) lgkmcnt(5)\n"
      "v_mfma_f32_32x32x16_bf16 v[32:47], v[84:87], v[164:167], v[32:47]\n"
      "ds_read_b128 v[76:79], v3 offset:19456\n"
      "buffer_load_dwordx4 v[216:219], v0, s[0:3], s30 offen offset:2048 sc1\n"
      "s_waitcnt vmcnt(12) lgkmcnt(5)\n"
      "v_mfma_f32_32x32x16_bf16 v[32:47], v[88:91], v[168:171], v[32:47]\n"
      "ds_read_b128 v[80:83], v3 offset:20480\n"
      "buffer_load_dwordx4 v[220:223], v0, s[0:3], s30 offen offset:3072 sc1\n"
      "s_waitcnt vmcnt(12) lgkmcnt(5)\n"
      "v_mfma_f32_32x32x16_bf16 v[32:47], v[92:95], v[172:175], v[32:47]\n"
      "ds_read_b128 v[84:87], v3 offset:21504\n"
      "s_add_u32 s30, s30, 0x1000\n"
      "buffer_load_dwordx4 v[224:227], v0, s[0:3], s30 offen offset:0 sc1\n"
      "s_waitcnt vmcnt(12) lgkmcnt(5)\n"
      "v_mfma_f32_32x32x16_bf16 v[32:47], v[64:67], v[176:179], v[32:47]\n"
      "ds_read_b128 v[88:91], v3 offset:22528\n"
      "buffer_load_dwordx4 v[228:231], v0, s[0:3], s30 offen offset:1024 sc1\n"
      "s_waitcnt vmcnt(12) lgkmcnt(5)\n"
      "v_mfma_f32_32x32x16_bf16 v[32:47], v[68:71], v[180:183], v[32:47]\n"
      "ds_read_b128 v[92:95], v3 offset:23552\n"
      "buffer_load_dwordx4 v[232:235], v0, s[0:3], s30 offen offset:2048 sc1\n"
      "s_waitcnt vmcnt(12) lgkmcnt(5)\n"
      "v_mfma_f32_32x32x16_bf16 v[32:47], v[72:75], v[184:187], v[32:47]\n"
      "ds_read_b128 v[64:67], v3 offset:24576\n"
      "buffer_load_dwordx4 v[236:239], v0, s[0:3], s30 offen offset:3072 sc1\n"
      "s_waitcnt vmcnt(12) lgkmcnt(5)\n"
      "v_mfma_f32_32x32x16_bf16 v[32:47], v[76:79], v[188:191], v[32:47]\n"
      "ds_read_b128 v[68:71], v3 offset:25600\n"
      "s_lshl_b32 s28, s15, 8\n"
      "s_add_u32 s28, s28, s25\n"
      "s_add_u32 s28, s28, s27\n"
      "s_sub_u32 s28, s28, 3968\n"
      "s_or_b32 s28, s28, s6\n"
      "s_mov_b64 exec, 0xffff\n"
      "buffer_store_dwordx2 v[12:13], v6, s[0:3], s28 offen sc1\n"
      "s_mov_b64 exec, -1\n"
      "s_cmp_lg_u32 s17, 1\n"
      "s_cbranch_scc1 .Lw076_%=\n"
      "s_add_u32 s31, s72, 45056\n"
      "buffer_load_dwordx4 a[128:131], v0, s[0:3], s31 offen sc1\n"
      "s_waitcnt vmcnt(13) lgkmcnt(5)\n"
      "v_mfma_f32_32x32x16_bf16 v[32:47], v[80:83], v[192:195], v[32:47]\n"
      "ds_read_b128 v[72:75], v3 offset:26624\n"
      "s_add_u32 s31, s72, 49152\n"
      "buffer_load_dwordx4 a[132:135], v0, s[0:3], s31 offen sc1\n"
      "s_waitcnt vmcnt(13) lgkmcnt(5)\n"
      "v_mfma_f32_32x32x16_bf16 v[32:47], v[84:87], v[196:199], v[32:47]\n"
      "ds_read_b128 v[76:79], v3 offset:27648\n"
      "s_add_u32 s31, s72, 53248\n"
      "buffer_load_dwordx4 a[136:139], v0, s[0:3], s31 offen sc1\n"
      "s_waitcnt vmcnt(13) lgkmcnt(5)\n"
      "v_mfma_f32_32x32x16_bf16 v[32:47], v[88:91], v[200:203], v[32:47]\n"
      "ds_read_b128 v[80:83], v3 offset:28672\n"
      "s_add_u32 s31, s72, 57344\n"
      "buffer_load_dwordx4 a[140:143], v0, s[0:3], s31 offen sc1\n"
      "s_waitcnt vmcnt(13) lgkmcnt(5)\n"
      "v_mfma_f32_32x32x16_bf16 v[32:47], v[92:95], v[204:207], v[32:47]\n"
      "ds_read_b128 v[84:87], v3 offset:29696\n"
      "s_add_u32 s31, s72, 61440\n"
      "buffer_load_dwordx4 a[144:147], v0, s[0:3], s31 offen sc1\n"
      "s_waitcnt vmcnt(13) lgkmcnt(5)\n"
      "v_mfma_f32_32x32x16_bf16 v[32:47], v[64:67], v[208:211], v[32:47]\n"
      "ds_read_b128 v[88:91], v3 offset:30720\n"
      "s_add_u32 s31, s72, 65536\n"
      "buffer_load_dwordx4 a[148:151], v0, s[0:3], s31 offen sc1\n"
      "s_waitcnt vmcnt(13) lgkmcnt(5)\n"
      "v_mfma_f32_32x32x16_bf16 v[32:47], v[68:71], v[212:215], v[32:47]\n"
      "ds_read_b128 v[92:95], v3 offset:31744\n"
      "s_add_u32 s31, s72, 69632\n"
      "buffer_load_dwordx4 a[152:155], v0, s[0:3], s31 offen sc1\n"
      "s_waitcnt vmcnt(13) lgkmcnt(5)\n"
      "v_mfma_f32_32x32x16_bf16 v[32:47], v[72:75], v[216:219], v[32:47]\n"
      "s_add_u32 s31, s72, 73728\n"
      "buffer_load_dwordx4 a[156:159], v0, s[0:3], s31 offen sc1\n"
      "s_waitcnt vmcnt(13) lgkmcnt(4)\n"
      "v_mfma_f32_32x32x16_bf16 v[32:47], v[76:79], v[220:223], v[32:47]\n"
      "s_add_u32 s31, s72, 77824\n"
      "buffer_load_dwordx4 a[160:163], v0, s[0:3], s31 offen sc1\n"
      "s_waitcnt vmcnt(13) lgkmcnt(3)\n"
      "v_mfma_f32_32x32x16_bf16 v[32:47], v[80:83], v[224:227], v[32:47]\n"
      "s_add_u32 s31, s72, 81920\n"
      "buffer_load_dwordx4 a[164:167], v0, s[0:3], s31 offen sc1\n"
      "s_waitcnt vmcnt(13) lgkmcnt(2)\n"
      "v_mfma_f32_32x32x16_bf16 v[32:47], v[84:87], v[228:231], v[32:47]\n"
      "s_waitcnt vmcnt(12) lgkmcnt(1)\n"
      "v_mfma_f32_32x32x16_bf16 v[32:47], v[88:91], v[232:235], v[32:47]\n"
      "s_waitcnt vmcnt(11) lgkmcnt(0)\n"
      "v_mfma_f32_32x32x16_bf16 v[32:47], v[92:95], v[236:239], v[32:47]\n"
      "s_branch .Lw177_%=\n"
      ".Lw076_%=:\n"
      "s_waitcnt vmcnt(12) lgkmcnt(5)\n"
      "v_mfma_f32_32x32x16_bf16 v[32:47], v[80:83], v[192:195], v[32:47]\n"
      "ds_read_b128 v[72:75], v3 offset:26624\n"
      "s_lshl_b32 s28, s15, 3\n"
      "s_add_u32 s28, s28, s25\n"
      "s_add_u32 s28, s28, s27\n"
      "s_or_b32 s28, s28, s6\n"
      "s_cmp_lg_u32 s17, 4\n"
      "s_cselect_b32 s28, s28, 2147483648\n"
      "s_cmp_ge_u32 s15, 8\n"
      "s_cselect_b32 s28, s28, 2147483648\n"
      "s_mov_b64 exec, 0xffff\n"
      "buffer_store_dwordx2 v[12:13], v4, s[0:3], s28 offen sc1\n"
      "s_mov_b64 exec, -1\n"
      "s_waitcnt vmcnt(12) lgkmcnt(5)\n"
      "v_mfma_f32_32x32x16_bf16 v[32:47], v[84:87], v[196:199], v[32:47]\n"
      "ds_read_b128 v[76:79], v3 offset:27648\n"
      "s_waitcnt vmcnt(11) lgkmcnt(5)\n"
      "v_mfma_f32_32x32x16_bf16 v[32:47], v[88:91], v[200:203], v[32:47]\n"
      "ds_read_b128 v[80:83], v3 offset:28672\n"
      "s_waitcnt vmcnt(10) lgkmcnt(5)\n"
      "v_mfma_f32_32x32x16_bf16 v[32:47], v[92:95], v[204:207], v[32:47]\n"
      "ds_read_b128 v[84:87], v3 offset:29696\n"
      "s_waitcnt vmcnt(9) lgkmcnt(5)\n"
      "v_mfma_f32_32x32x16_bf16 v[32:47], v[64:67], v[208:211], v[32:47]\n"
      "ds_read_b128 v[88:91], v3 offset:30720\n"
      "s_waitcnt vmcnt(8) lgkmcnt(5)\n"
      "v_mfma_f32_32x32x16_bf16 v[32:47], v[68:71], v[212:215], v[32:47]\n"
      "ds_read_b128 v[92:95], v3 offset:31744\n"
      "s_waitcnt vmcnt(7) lgkmcnt(5)\n"
      "v_mfma_f32_32x32x16_bf16 v[32:47], v[72:75], v[216:219], v[32:47]\n"
      "s_waitcnt vmcnt(6) lgkmcnt(4)\n"
      "v_mfma_f32_32x32x16_bf16 v[32:47], v[76:79], v[220:223], v[32:47]\n"
      "s_waitcnt vmcnt(5) lgkmcnt(3)\n"
      "v_mfma_f32_32x32x16_bf16 v[32:47], v[80:83], v[224:227], v[32:47]\n"
      "s_lshl_b32 s28, s15, 3\n"
      "s_add_u32 s28, s28, s25\n"
      "s_add_u32 s28, s28, s27\n"
      "s_or_b32 s28, s28, s6\n"
      "s_cmp_lg_u32 s17, 4\n"
      "s_cselect_b32 s28, s28, 2147483648\n"
      "s_cmp_lt_u32 s15, 8\n"
      "s_cselect_b32 s28, s28, 2147483648\n"
      "s_mov_b64 exec, 0xffff\n"
      "buffer_store_dwordx2 v[12:13], v4, s[0:3], s28 offen sc1\n"
      "s_mov_b64 exec, -1\n"
      "s_waitcnt vmcnt(5) lgkmcnt(2)\n"
      "v_mfma_f32_32x32x16_bf16 v[32:47], v[84:87], v[228:231], v[32:47]\n"
      "s_waitcnt vmcnt(4) lgkmcnt(1)\n"
      "v_mfma_f32_32x32x16_bf16 v[32:47], v[88:91], v[232:235], v[32:47]\n"
      "s_waitcnt vmcnt(3) lgkmcnt(0)\n"
      "v_mfma_f32_32x32x16_bf16 v[32:47], v[92:95], v[236:239], v[32:47]\n"
      ".Lw177_%=:\n"
      "s_lshl_b32 s32, s15, 11\n"
      "s_add_u32 s32, s32, s20\n"
      "s_lshl_b32 s33, s15, 8\n"
      "s_add_u32 s33, s33, s21\n"
      "s_nop 7 ; hz\n"
      "v_cmp_class_f32 vcc, v32, s7\n"
      "s_cbranch_vccz .Lck78_%=\n"
      "s_cmp_eq_u32 s5, 0\n"
      "s_cbranch_scc1 .Lslowo26_%=\n"
      ".Lck78_%=:\n"
      "s_mov_b32 s5, 0\n"
      "v_cvt_pk_bf16_f32 v24, v32, v33\n"
      "v_cvt_pk_bf16_f32 v25, v34, v35\n"
      "v_cvt_pk_bf16_f32 v26, v36, v37\n"
      "v_cvt_pk_bf16_f32 v27, v38, v39\n"
      "v_cvt_pk_bf16_f32 v28, v40, v41\n"
      "v_cvt_pk_bf16_f32 v29, v42, v43\n"
      "v_cvt_pk_bf16_f32 v30, v44, v45\n"
      "v_cvt_pk_bf16_f32 v31, v46, v47\n"
      "s_add_u32 s22, s22, 131072\n"
      "v_xor_b32 v3, 0x8000, v3\n"
      "s_cmp_lg_u32 s17, 4\n"
      "s_nop 0\n"
      "s_cbranch_scc1 .Lxpub4_%=\n"
      "v_permlane32_swap_b32 v24, v26\n"
      "v_permlane32_swap_b32 v25, v27\n"
      "v_permlane32_swap_b32 v28, v30\n"
      "v_permlane32_swap_b32 v29, v31\n"
      "s_mov_b32 s56, s50\n"
      "s_and_b32 s57, s51, 0xffff\n"
      "s_mov_b32 s58, 1835008\n"
      "s_mov_b32 s59, 0x20000\n"
      "v_lshrrev_b32 v84, 3, v6\n"
      "v_mul_u32_u24 v84, 896, v84\n"
      "v_lshrrev_b32 v85, 9, v0\n"
      "v_lshl_add_u32 v84, v85, 4, v84\n"
      "buffer_store_dwordx4 v[24:27], v84, s[56:59], s19 offen\n"
      "buffer_store_dwordx4 v[28:31], v84, s[56:59], s19 offen offset:32\n"
      "s_branch .Llast8_%=\n"
      ".Lnowo74_%=:\n"
      "s_lshl_b32 s28, s15, 8\n"
      "s_add_u32 s28, s28, s25\n"
      "s_add_u32 s28, s28, s27\n"
      "s_sub_u32 s28, s28, 3968\n"
      "s_or_b32 s28, s28, s6\n"
      "s_mov_b64 exec, 0xffff\n"
      "buffer_store_dwordx2 v[12:13], v6, s[0:3], s28 offen sc1\n"
      "s_mov_b64 exec, -1\n"
      "s_xor_b32 s28, s15, 15\n"
      "s_or_b32 s28, s28, s14\n"
      "s_or_b32 s28, s28, s16\n"
      "s_xor_b32 s29, s17, 2\n"
      "s_or_b32 s28, s28, s29\n"
      "s_cmp_eq_u32 s28, 0\n"
      "s_cbranch_scc0 .Lif79_%=\n"
      "s_mov_b64 exec, 1\n"
      "buffer_store_dwordx2 v[12:13], off, s[0:3], s74 sc1\n"
      "s_mov_b64 exec, -1\n"
      ".Lif79_%=:\n"
      "s_lshl_b32 s28, s15, 3\n"
      "s_add_u32 s28, s28, s25\n"
      "s_add_u32 s28, s28, s27\n"
      "s_or_b32 s28, s28, s6\n"
      "s_cmp_lg_u32 s17, 4\n"
      "s_cselect_b32 s28, s28, 2147483648\n"
      "s_mov_b64 exec, 0xffff\n"
      "buffer_store_dwordx2 v[12:13], v4, s[0:3], s28 offen sc1\n"
      "s_mov_b64 exec, -1\n"
      "s_add_u32 s22, s22, 131072\n"
      "s_cmp_eq_u32 s17, 1\n"
      "s_cbranch_scc0 .Lif80_%=\n"
      "s_add_u32 s31, s72, 45056\n"
      "buffer_load_dwordx4 a[128:131], v0, s[0:3], s31 offen sc1\n"
      "s_add_u32 s31, s72, 49152\n"
      "buffer_load_dwordx4 a[132:135], v0, s[0:3], s31 offen sc1\n"
      "s_add_u32 s31, s72, 53248\n"
      "buffer_load_dwordx4 a[136:139], v0, s[0:3], s31 offen sc1\n"
      "s_add_u32 s31, s72, 57344\n"
      "buffer_load_dwordx4 a[140:143], v0, s[0:3], s31 offen sc1\n"
      "s_add_u32 s31, s72, 61440\n"
      "buffer_load_dwordx4 a[144:147], v0, s[0:3], s31 offen sc1\n"
      "s_add_u32 s31, s72, 65536\n"
      "buffer_load_dwordx4 a[148:151], v0, s[0:3], s31 offen sc1\n"
      "s_add_u32 s31, s72, 69632\n"
      "buffer_load_dwordx4 a[152:155], v0, s[0:3], s31 offen sc1\n"
      "s_add_u32 s31, s72, 73728\n"
      "buffer_load_dwordx4 a[156:159], v0, s[0:3], s31 offen sc1\n"
      "s_add_u32 s31, s72, 77824\n"
      "buffer_load_dwordx4 a[160:163], v0, s[0:3], s31 offen sc1\n"
      "s_add_u32 s31, s72, 81920\n"
      "buffer_load_dwordx4 a[164:167], v0, s[0:3], s31 offen sc1\n"
      ".Lif80_%=:\n"
      "s_cmp_lg_u32 s17, 4\n"
      "s_cbranch_scc1 .Lxhop5_%=\n"
      ".Llast8_%=:\n"
      "s_branch .Lend1_%=\n"
      ".Lslowa24_%=:\n"
      "s_mov_b32 s5, 1\n"
      "s_barrier\n"
      "s_lshl_b32 s28, s82, 12\n"
      "v_add_u32 v86, s28, v7\n"
      "ds_write_b128 v86, a[0:3] offset:0\n"
      "s_lshl_b32 s28, s83, 12\n"
      "v_add_u32 v87, s28, v7\n"
      "ds_write_b128 v87, a[4:7] offset:0\n"
      "s_lshl_b32 s28, s84, 12\n"
      "v_add_u32 v86, s28, v7\n"
      "ds_write_b128 v86, a[8:11] offset:0\n"
      "s_lshl_b32 s28, s85, 12\n"
      "v_add_u32 v87, s28, v7\n"
      "ds_write_b128 v87, a[12:15] offset:0\n"
      "s_lshl_b32 s28, s86, 12\n"
      "v_add_u32 v86, s28, v7\n"
      "ds_write_b128 v86, a[16:19] offset:0\n"
      "s_lshl_b32 s28, s87, 12\n"
      "v_add_u32 v87, s28, v7\n"
      "ds_write_b128 v87, a[20:23] offset:0\n"
      "s_lshl_b32 s28, s88, 12\n"
      "v_add_u32 v86, s28, v7\n"
      "ds_write_b128 v86, a[24:27] offset:0\n"
      "s_add_u32 s28, s25, s27\n"
      "s_sub_u32 s28, s28, 3968\n"
      "s_mov_b32 s71, 0x10000\n"
      "s_mov_b64 exec, 0xffff\n"
      ".Lwc81_%=:\n"
      "buffer_load_dwordx2 v[84:85], v4, s[0:3], s28 offen sc1\n"
      "s_waitcnt vmcnt(0)\n"
      "v_cmp_eq_u64 vcc, s[12:13], v[84:85]\n"
      "s_nop 0 ; hz\n"
      "s_cmp_eq_u64 vcc, exec\n"
      "s_cbranch_scc1 .Lwo82_%=\n"
      "s_sleep 4\n"
      "s_sub_u32 s71, s71, 1\n"
      "s_cmp_lg_u32 s71, 0\n"
      "s_cbranch_scc1 .Lwc81_%=\n"
      ".Lwo82_%=:\n"
      "s_mov_b64 exec, -1\n"
      "s_waitcnt lgkmcnt(0)\n"
      "s_barrier\n"
      "s_branch .Lxhead23_%=\n"
      ".Lslowb25_%=:\n"
      "s_mov_b32 s5, 1\n"
      "s_add_u32 s28, s25, s27\n"
      "s_sub_u32 s28, s28, 3968\n"
      "s_mov_b32 s71, 0x10000\n"
      "s_mov_b64 exec, 0xffff\n"
      ".Lwc83_%=:\n"
      "buffer_load_dwordx2 v[84:85], v4, s[0:3], s28 offen sc1\n"
      "s_waitcnt vmcnt(0)\n"
      "v_cmp_eq_u64 vcc, s[12:13], v[84:85]\n"
      "s_nop 0 ; hz\n"
      "s_cmp_eq_u64 vcc, exec\n"
      "s_cbranch_scc1 .Lwo84_%=\n"
      "s_sleep 4\n"
      "s_sub_u32 s71, s71, 1\n"
      "s_cmp_lg_u32 s71, 0\n"
      "s_cbranch_scc1 .Lwc83_%=\n"
      ".Lwo84_%=:\n"
      "s_mov_b64 exec, -1\n"
      "s_nop 0\n"
      "s_branch .Lxhead23_%=\n"
      ".Lslowo26_%=:\n"
      "s_mov_b32 s5, 1\n"
      "s_add_u32 s28, s25, s27\n"
      "s_sub_u32 s28, s28, 3968\n"
      "s_mov_b32 s71, 0x10000\n"
      "s_mov_b64 exec, 0xffff\n"
      ".Lwc85_%=:\n"
      "buffer_load_dwordx2 v[16:17], v4, s[0:3], s28 offen sc1\n"
      "s_waitcnt vmcnt(0)\n"
      "v_cmp_eq_u64 vcc, s[12:13], v[16:17]\n"
      "s_nop 0 ; hz\n"
      "s_cmp_eq_u64 vcc, exec\n"
      "s_cbranch_scc1 .Lwo86_%=\n"
      "s_sleep 4\n"
      "s_sub_u32 s71, s71, 1\n"
      "s_cmp_lg_u32 s71, 0\n"
      "s_cbranch_scc1 .Lwc85_%=\n"
      ".Lwo86_%=:\n"
      "s_mov_b64 exec, -1\n"
      "s_nop 0\n"
      "s_branch .Lwohead75_%=\n"
      ".Lfb61_%=:\n"
      "s_add_u32 s28, s26, s27\n"
      "s_sub_u32 s28, s28, 3968\n"
      "s_mov_b32 s71, 0x10000\n"
      "s_mov_b64 exec, 0xffff\n"
      ".Lwc88_%=:\n"
      "buffer_load_dwordx2 v[152:153], v4, s[0:3], s28 offen sc1\n"
      "s_waitcnt vmcnt(0)\n"
      "v_cmp_eq_u64 vcc, s[12:13], v[152:153]\n"
      "s_nop 0 ; hz\n"
      "s_cmp_eq_u64 vcc, exec\n"
      "s_cbranch_scc1 .Lwo89_%=\n"
      "s_sleep 4\n"
      "s_sub_u32 s71, s71, 1\n"
      "s_cmp_lg_u32 s71, 0\n"
      "s_cbranch_scc1 .Lwc88_%=\n"
      ".Lwo89_%=:\n"
      "s_mov_b64 exec, -1\n"
      "s_barrier\n"
      "s_add_u32 s69, s69, 1\n"
      "s_bitcmp1_b32 s17, 0\n"
      "s_cselect_b32 s69, s69, 2\n"
      "s_cbranch_scc0 .Lkvhead39_%=\n"
      "s_cmp_eq_u32 s69, 1\n"
      "s_cbranch_scc1 .Lagain87_%=\n"
      "s_mov_b32 s4, 0\n"
      "v_mov_b64 v[236:237], v[182:183]\n"
      "s_mov_b32 exec_lo, 0\n"
      "v_mov_b32 v182, 0\n"
      "v_mov_b32 v183, 0\n"
      "s_mov_b32 exec_lo, -1\n"
      "v_mov_b32 v186, 0xff800000\n"
      "s_lshl_b32 s28, s16, 16\n"
      "s_sub_u32 s28, s24, s28\n"
      "s_add_u32 s29, s28, 262144\n"
      ".Lfbl90_%=:\n"
      "buffer_load_dwordx4 v[16:19], v0, s[0:3], s28 offen sc1\n"
      "buffer_load_dwordx4 v[20:23], v0, s[0:3], s28 offen offset:1024 sc1\n"
      "s_waitcnt vmcnt(0)\n"
      "v_mfma_f32_32x32x16_bf16 v[112:127], v[16:19], v[176:179], 0\n"
      "v_mfma_f32_32x32x16_bf16 v[112:127], v[20:23], v[180:183], v[112:127]\n"
      "s_nop 7 ; hz\n"
      "s_nop 3 ; hz\n"
      "v_max3_f32 v186, v186, v112, v113\n"
      "v_max3_f32 v186, v186, v114, v115\n"
      "v_max3_f32 v186, v186, v116, v117\n"
      "v_max3_f32 v186, v186, v118, v119\n"
      "v_max3_f32 v186, v186, v120, v121\n"
      "v_max3_f32 v186, v186, v122, v123\n"
      "v_max3_f32 v186, v186, v124, v125\n"
      "v_max3_f32 v186, v186, v126, v127\n"
      "s_add_u32 s28, s28, 0x1000\n"
      "s_cmp_lt_u32 s28, s29\n"
      "s_cbranch_scc1 .Lfbl90_%=\n"
      "v_mov_b64 v[182:183], v[236:237]\n"
      "v_mov_b32 v187, v186\n"
      "s_nop 1 ; hz\n"
      "v_permlane32_swap_b32 v186, v187\n"
      "v_max_f32 v186, v186, v187\n"
      "v_sub_f32 v186, 0, v186\n"
      "v_cvt_pk_bf16_f32 v187, v186, v186\n"
      "v_and_b32 v187, 0xffff0000, v187\n"
      "v_sub_f32 v187, v186, v187\n"
      "s_mov_b32 exec_lo, 0\n"
      "s_mov_b32 exec_hi, s10\n"
      "v_cvt_pk_bf16_f32 v182, v186, v187\n"
      "v_and_b32 v186, 0xffff0000, v182\n"
      "v_sub_f32 v186, v187, v186\n"
      "v_cvt_pk_bf16_f32 v183, v186, 0\n"
      "s_mov_b64 exec, -1\n"
      "ds_write_b128 v184, v[180:183] offset:1024\n"
      ".Lagain87_%=:\n"
      "v_mov_b64 v[48:49], 0\n"
      "v_mov_b64 v[50:51], 0\n"
      "v_mov_b64 v[52:53], 0\n"
      "v_mov_b64 v[54:55], 0\n"
      "v_mov_b64 v[56:57], 0\n"
      "v_mov_b64 v[58:59], 0\n"
      "v_mov_b64 v[60:61], 0\n"
      "v_mov_b64 v[62:63], 0\n"
      "v_mov_b64 v[64:65], 0\n"
      "v_mov_b64 v[66:67], 0\n"
      "v_mov_b64 v[68:69], 0\n"
      "v_mov_b64 v[70:71], 0\n"
      "v_mov_b64 v[72:73], 0\n"
      "v_mov_b64 v[74:75], 0\n"
      "v_mov_b64 v[76:77], 0\n"
      "v_mov_b64 v[78:79], 0\n"
      "v_mov_b64 v[80:81], 0\n"
      "v_mov_b64 v[82:83], 0\n"
      "v_mov_b64 v[84:85], 0\n"
      "v_mov_b64 v[86:87], 0\n"
      "v_mov_b64 v[88:89], 0\n"
      "v_mov_b64 v[90:91], 0\n"
      "v_mov_b64 v[92:93], 0\n"
      "v_mov_b64 v[94:95], 0\n"
      "v_mov_b64 v[96:97], 0\n"
      "v_mov_b64 v[98:99], 0\n"
      "v_mov_b64 v[100:101], 0\n"
      "v_mov_b64 v[102:103], 0\n"
      "v_mov_b64 v[104:105], 0\n"
      "v_mov_b64 v[106:107], 0\n"
      "v_mov_b64 v[108:109], 0\n"
      "v_mov_b64 v[110:111], 0\n"
      "v_mfma_f32_32x32x16_bf16 v[112:127], v[160:163], v[176:179], 0\n"
      "v_mfma_f32_32x32x16_bf16 v[112:127], v[164:167], v[180:183], v[112:127]\n"
      "s_waitcnt lgkmcnt(0)\n"
      "s_barrier\n"
      "s_nop 7\n"
      "s_nop 1\n"
      "s_branch .Latt51_%=\n"
      ".Lend1_%=:\n"
      : "+{v0}"(tid)
      : "{s[94:95]}"(ka), "{s54}"(bid), "{s55}"(lds0)
      : "memory", "scc", "vcc", "s0", "s1", "s2", "s3", "s4", "s5", "s6", "s7", "s8", "s9", "s10", "s11", "s12", "s13", "s14", "s15", "s16", "s17", "s18", "s19", "s20", "s21", "s22", "s23", "s24", "s25", "s26", "s27", "s28", "s29", "s30", "s31", "s33", "s34", "s35", "s36", "s37", "s38", "s39", "s40", "s41", "s42", "s43", "s44", "s45", "s46", "s47", "s48", "s49", "s50", "s51", "s52", "s53", "s56", "s57", "s58", "s59", "s60", "s61", "s62", "s63", "s64", "s65", "s66", "s67", "s68", "s69", "s70", "s71", "s72", "s73", "s74", "s75", "s76", "s77", "s78", "s79", "s80", "s81", "s82", "s83", "s84", "s85", "s86", "s87", "s88", "s89", "s90", "s91", "s92", "s93", "s96", "s97", "s98", "s99", "v1", "v2", "v3", "v4", "v5", "v6", "v7", "v8", "v9", "v10", "v11", "v12", "v13", "v14", "v15", "v16", "v17", "v18", "v19", "v20", "v21", "v22", "v23", "v24", "v25", "v26", "v27", "v28", "v29", "v30", "v31", "v32", "v33", "v34", "v35", "v36", "v37", "v38", "v39", "v40", "v41", "v42", "v43", "v44", "v45", "v46", "v47", "v48", "v49", "v50", "v51", "v52", "v53", "v54", "v55", "v56", "v57", "v58", "v59", "v60", "v61", "v62", "v63", "v64", "v65", "v66", "v67", "v68", "v69", "v70", "v71", "v72", "v73", "v74", "v75", "v76", "v77", "v78", "v79", "v80", "v81", "v82", "v83", "v84", "v85", "v86", "v87", "v88", "v89", "v90", "v91", "v92", "v93", "v94", "v95", "v96", "v97", "v98", "v99", "v100", "v101", "v102", "v103", "v104", "v105", "v106", "v107", "v108", "v109", "v110", "v111", "v112", "v113", "v114", "v115", "v116", "v117", "v118", "v119", "v120", "v121", "v122", "v123", "v124", "v125", "v126", "v127", "v128", "v129", "v130", "v131", "v132", "v133", "v134", "v135", "v136", "v137", "v138", "v139", "v140", "v141", "v142", "v143", "v144", "v145", "v146", "v147", "v148", "v149", "v150", "v151", "v152", "v153", "v154", "v155", "v156", "v157", "v158", "v159", "v160", "v161", "v162", "v163", "v164", "v165", "v166", "v167", "v168", "v169", "v170", "v171", "v172", "v173", "v174", "v175", "v176", "v177", "v178", "v179", "v180", "v181", "v182", "v183", "v184", "v185", "v186", "v187", "v188", "v189", "v190", "v191", "v192", "v193", "v194", "v195", "v196", "v197", "v198", "v199", "v200", "v201", "v202", "v203", "v204", "v205", "v206", "v207", "v208", "v209", "v210", "v211", "v212", "v213", "v214", "v215", "v216", "v217", "v218", "v219", "v220", "v221", "v222", "v223", "v224", "v225", "v226", "v227", "v228", "v229", "v230", "v231", "v232", "v233", "v234", "v235", "v236", "v237", "v238", "v239", "v240", "v241", "v242", "v243", "v244", "v245", "v246", "v247", "v248", "v249", "v250", "v251", "v252", "v253", "v254", "v255", "a0", "a1", "a2", "a3", "a4", "a5", "a6", "a7", "a8", "a9", "a10", "a11", "a12", "a13", "a14", "a15", "a16", "a17", "a18", "a19", "a20", "a21", "a22", "a23", "a24", "a25", "a26", "a27", "a28", "a29", "a30", "a31", "a32", "a33", "a34", "a35", "a36", "a37", "a38", "a39", "a40", "a41", "a42", "a43", "a44", "a45", "a46", "a47", "a48", "a49", "a50", "a51", "a52", "a53", "a54", "a55", "a56", "a57", "a58", "a59", "a60", "a61", "a62", "a63", "a64", "a65", "a66", "a67", "a68", "a69", "a70", "a71", "a72", "a73", "a74", "a75", "a76", "a77", "a78", "a79", "a80", "a81", "a82", "a83", "a84", "a85", "a86", "a87", "a88", "a89", "a90", "a91", "a92", "a93", "a94", "a95", "a96", "a97", "a98", "a99", "a100", "a101", "a102", "a103", "a104", "a105", "a106", "a107", "a108", "a109", "a110", "a111", "a112", "a113", "a114", "a115", "a116", "a117", "a118", "a119", "a120", "a121", "a122", "a123", "a124", "a125", "a126", "a127", "a128", "a129", "a130", "a131", "a132", "a133", "a134", "a135", "a136", "a137", "a138", "a139", "a140", "a141", "a142", "a143", "a144", "a145", "a146", "a147", "a148", "a149", "a150", "a151", "a152", "a153", "a154", "a155", "a156", "a157", "a158", "a159", "a160", "a161", "a162", "a163", "a164", "a165", "a166", "a167", "a168", "a169", "a170", "a171", "a172", "a173", "a174", "a175", "a176", "a177", "a178", "a179", "a180", "a181", "a182", "a183", "a184", "a185", "a186", "a187", "a188", "a189", "a190", "a191", "a192", "a193", "a194", "a195", "a196", "a197", "a198", "a199", "a200", "a201", "a202", "a203", "a204", "a205", "a206", "a207", "a208", "a209", "a210", "a211", "a212", "a213", "a214", "a215", "a216", "a217", "a218", "a219", "a220", "a221", "a222", "a223", "a224", "a225", "a226", "a227", "a228", "a229", "a230", "a231", "a232", "a233", "a234", "a235", "a236", "a237", "a238", "a239", "a240", "a241", "a242", "a243", "a244", "a245", "a246", "a247", "a248", "a249", "a250", "a251", "a252", "a253", "a254", "a255");
}

extern "C" void kernel_launch(void* const* d_in, const int* in_sizes, int n_in, void* d_out, int out_size, void* d_ws, size_t ws_size, hipStream_t stream) {
  fused_kernel<<<256, 256, 0, stream>>>(d_in[0], d_in[1], d_in[2], d_in[4], d_in[5], d_out, d_ws);
}
```
